```python
import torch

def _softmax(x, axis=-1):
    return torch.softmax(x, dim=axis)
def _gelu(x):
    return torch.nn.functional.gelu(x, approximate="tanh")
def _silu(x):
    return torch.nn.functional.silu(x)
def _sigmoid(x):
    return torch.sigmoid(x)
def _layernorm(x, axis=-1, eps=1e-5):
    m = x.mean(dim=axis, keepdim=True)
    v = x.var(dim=axis, keepdim=True, unbiased=False)
    return (x - m) / torch.sqrt(v + eps)
def _scatter_add(tgt, idx, src, axis=0):
    return tgt.clone().index_add_(axis, idx, src)
def _segment_sum(src, seg, n):
    out = torch.zeros((n,) + src.shape[1:], dtype=src.dtype)
    return out.index_add_(0, seg, src)
def _pad(x, pw):
    flat = tuple(v for pair in reversed(pw) for v in pair)
    return torch.nn.functional.pad(x, flat)

kernel_name = 'parallel_sum__sliding_local_dilated__mixer_token_mlp__prompt'

def setup_inputs():

    _w = (lambda a: (lambda t: t.to(torch.bfloat16) if t.is_floating_point() else t)(torch.as_tensor(a)))
    def _A_setup():
        B, S, D = 1, 2048, 512
        return {
            "x": _w((lambda *s: torch.randn(*s, dtype=torch.bfloat16) * 0.05)(B, S, D)),
            "wqkv": _w((lambda *s: torch.randn(*s, dtype=torch.bfloat16) * 0.05)(D, 3 * D)),
            "wo": _w((lambda *s: torch.randn(*s, dtype=torch.bfloat16) * 0.05)(D, D)),
        }
    _A_in = _A_setup()

    def _B_setup():
        B, S, D, F = 1, 2048, 512, 1376
        return {
            "x": _w((lambda *s: torch.randn(*s, dtype=torch.bfloat16) * 0.05)(B, S, D)),
            "w1": _w((lambda *s: torch.randn(*s, dtype=torch.bfloat16) * 1.6)(S, F) * (S ** -0.5)),
            "b1": _w((lambda *s: torch.randn(*s, dtype=torch.bfloat16) * 12.8)(F) * 0.02),
            "w2": _w((lambda *s: torch.randn(*s, dtype=torch.bfloat16) * 0.8)(F, S) * (F ** -0.5)),
            "b2": _w((lambda *s: torch.randn(*s, dtype=torch.bfloat16) * 12.8)(S) * 0.02),
        }
    _B_in = _B_setup()

    return {
        "A_x": _A_in["x"],
        "A_wqkv": _A_in["wqkv"],
        "A_wo": _A_in["wo"],
        "B_x": _B_in["x"],
        "B_w1": _B_in["w1"],
        "B_b1": _B_in["b1"],
        "B_w2": _B_in["w2"],
        "B_b2": _B_in["b2"],
    }

def reference(A_x, A_wqkv, A_wo, B_x, B_w1, B_b1, B_w2, B_b2):
    def _A_ref(x, wqkv, wo):
        B, S, D = x.shape
        H = 32; Dh = D // H
        W, dil = 8, 2
        qkv = (lambda x, *d: x.permute(*d))((x @ wqkv).reshape(B, S, 3, H, Dh), 2, 0, 3, 1, 4)
        q, k, v = qkv[0], qkv[1], qkv[2]
        scale = Dh ** -0.5
        pos = torch.arange(S)
        off = torch.arange(W) * dil
        idx = pos[:, None] - off[None, :]
        mask = idx >= 0
        idx_c = torch.where(mask, idx, 0).reshape(-1)
        kg = k[:, :, idx_c, :].reshape(B, H, S, W, Dh)
        vg = v[:, :, idx_c, :].reshape(B, H, S, W, Dh)
        logits = torch.einsum("bhsd,bhswd->bhsw", q, kg) * scale
        logits = torch.where(mask[None, None, :, :], logits, -1e9)
        attn = _softmax(logits, axis=-1)
        o = torch.einsum("bhsw,bhswd->bhsd", attn, vg)
        return (lambda x, *d: x.permute(*d))(o, 0, 2, 1, 3).reshape(B, S, D) @ wo

    def _B_ref(x, w1, b1, w2, b2):
        B, S, D = x.shape
        h = _layernorm(x, axis=-1)
        u = torch.einsum("bsd,sf->bdf", h, w1) + b1
        u = _gelu(u)
        y = torch.einsum("bdf,fs->bds", u, w2) + b2
        return (lambda x, *d: x.permute(*d))(y, 0, 2, 1)

    x = A_x
    xn = _layernorm(x, axis=-1)
    return x + _A_ref(xn, A_wqkv, A_wo) + _B_ref(xn, B_w1, B_b1, B_w2, B_b2)
```

```cpp
#include <hip/hip_runtime.h>
#include <hip/hip_bf16.h>
typedef unsigned short u16;
static __device__ __forceinline__ float bf2f(u16 v){ return __uint_as_float(((unsigned)v)<<16); }
static __device__ __forceinline__ u16 f2bf(float f){ unsigned u=__float_as_uint(f); u += 0x7fffu + ((u>>16)&1u); return (u16)(u>>16); }
constexpr int S=2048, D=512, F=1376, H=32, DH=16;

__global__ void k_ln(const u16* x, u16* xn, u16* h){
  int row = blockIdx.x; int lane = threadIdx.x;
  float v[8]; float s=0;
  for(int j=0;j<8;j++){ v[j]=bf2f(x[row*D+lane*8+j]); s+=v[j]; }
  for(int m=32;m>=1;m>>=1) s+=__shfl_xor(s,m);
  float mean=s/D; float q=0;
  for(int j=0;j<8;j++){ float d=v[j]-mean; q+=d*d; }
  for(int m=32;m>=1;m>>=1) q+=__shfl_xor(q,m);
  float r=rsqrtf(q/D+1e-5f);
  float s2=0;
  for(int j=0;j<8;j++){ u16 b=f2bf((v[j]-mean)*r); xn[row*D+lane*8+j]=b; v[j]=bf2f(b); s2+=v[j]; }
  for(int m=32;m>=1;m>>=1) s2+=__shfl_xor(s2,m);
  float mean2=s2/D; float q2=0;
  for(int j=0;j<8;j++){ float d=v[j]-mean2; q2+=d*d; }
  for(int m=32;m>=1;m>>=1) q2+=__shfl_xor(q2,m);
  float r2=rsqrtf(q2/D+1e-5f);
  for(int j=0;j<8;j++){ h[row*D+lane*8+j]=f2bf((v[j]-mean2)*r2); }
}
__global__ void k_qkv(const u16* xn, const u16* w, u16* qkv){
  int c = blockIdx.x*256+threadIdx.x; int s=blockIdx.y;
  float acc=0; for(int k=0;k<D;k++) acc+=bf2f(xn[s*D+k])*bf2f(w[k*3*D+c]);
  qkv[s*3*D+c]=f2bf(acc);
}
__global__ void k_attn(const u16* qkv, u16* o){
  int idx=blockIdx.x*256+threadIdx.x; int hh=idx%H; int s=idx/H;
  float lg[8]; float mx=-1e30f;
  for(int w=0;w<8;w++){ int p=s-2*w; float a=0; if(p>=0){ for(int d=0;d<DH;d++) a+=bf2f(qkv[s*3*D+hh*DH+d])*bf2f(qkv[p*3*D+D+hh*DH+d]); a*=0.25f;} else a=-1e9f; lg[w]=a; mx=fmaxf(mx,a);}
  float sum=0; for(int w=0;w<8;w++){ lg[w]=__expf(lg[w]-mx); sum+=lg[w]; }
  for(int d=0;d<DH;d++){ float a=0; for(int w=0;w<8;w++){ int p=s-2*w; if(p<0)p=0; a+=lg[w]*bf2f(qkv[p*3*D+2*D+hh*DH+d]); } o[s*D+hh*DH+d]=f2bf(a/sum); }
}
__global__ void k_g1(const u16* h, const u16* w1, const u16* b1, u16* g){
  int f=blockIdx.x*32+threadIdx.x; int d=blockIdx.y*8+threadIdx.y;
  float acc=0; for(int s=0;s<S;s++) acc+=bf2f(h[s*D+d])*bf2f(w1[s*F+f]);
  acc+=bf2f(b1[f]);
  float t=tanhf(0.7978845608028654f*(acc+0.044715f*acc*acc*acc));
  g[d*F+f]=f2bf(0.5f*acc*(1.f+t));
}
__global__ void k_out(const u16* x, const u16* o, const u16* wo, const u16* g, const u16* w2, const u16* b2, u16* out){
  int d=blockIdx.x*256+threadIdx.x; int s=blockIdx.y;
  float a=0; for(int k=0;k<D;k++) a+=bf2f(o[s*D+k])*bf2f(wo[k*D+d]);
  float b=0; for(int f=0;f<F;f++) b+=bf2f(g[d*F+f])*bf2f(w2[f*S+s]);
  out[s*D+d]=f2bf(bf2f(x[s*D+d])+a+b+bf2f(b2[s]));
}
extern "C" void kernel_launch(void* const* d_in, const int* in_sizes, int n_in, void* d_out, int out_size, void* d_ws, size_t ws_size, hipStream_t stream){
  const u16* x=(const u16*)d_in[0]; const u16* wqkv=(const u16*)d_in[1]; const u16* wo=(const u16*)d_in[2];
  const u16* w1=(const u16*)d_in[4]; const u16* b1=(const u16*)d_in[5]; const u16* w2=(const u16*)d_in[6]; const u16* b2=(const u16*)d_in[7];
  u16* ws=(u16*)d_ws; u16* xn=ws; u16* h=xn+S*D; u16* qkv=h+S*D; u16* o=qkv+S*3*D; u16* g=o+S*D;
  k_ln<<<S,64,0,stream>>>(x,xn,h);
  k_qkv<<<dim3(6,S),256,0,stream>>>(xn,wqkv,qkv);
  k_attn<<<S*H/256,256,0,stream>>>(qkv,o);
  k_g1<<<dim3(F/32,D/8),dim3(32,8),0,stream>>>(h,w1,b1,g);
  k_out<<<dim3(2,S),256,0,stream>>>(x,o,wo,g,w2,b2,(u16*)d_out);
}
```

```cpp
#include <hip/hip_runtime.h>
#include <stdint.h>

typedef unsigned short u16;
typedef short s16x4 __attribute__((ext_vector_type(4)));
typedef __bf16 bf16x2 __attribute__((ext_vector_type(2)));
typedef __bf16 bf16x4 __attribute__((ext_vector_type(4)));
typedef __bf16 bf16x8 __attribute__((ext_vector_type(8)));
typedef float f32x2 __attribute__((ext_vector_type(2)));
typedef float f32x4 __attribute__((ext_vector_type(4)));
typedef unsigned u32x2 __attribute__((ext_vector_type(2)));
typedef unsigned u32x4 __attribute__((ext_vector_type(4)));
#define LAS __attribute__((address_space(3)))
#define DI __device__ __forceinline__

constexpr int S = 2048, D = 512, F = 1376;
constexpr float EPS = 1e-5f;
constexpr unsigned HT_BYTES = 32u * 64u * 1024u;
constexpr unsigned W1T_BYTES = 86u * 64u * 1024u;
constexpr unsigned G_BYTES = 32u * 43u * 1024u;
constexpr unsigned O_BYTES = 128u * 16u * 1024u;
constexpr size_t WS_HT = 0;
constexpr size_t WS_W1T = WS_HT + HT_BYTES;
constexpr size_t WS_G = WS_W1T + W1T_BYTES;
constexpr size_t WS_O = WS_G + G_BYTES;

typedef __amdgpu_buffer_rsrc_t rsrc_t;
DI rsrc_t mk_rsrc(const void* p, unsigned bytes) { return __builtin_amdgcn_make_buffer_rsrc((void*)p, 0, bytes, 0x00020000); }
DI u32x4 mk_srd(const void* p, unsigned bytes) {
  const uint64_t a = (uint64_t)p;
  u32x4 r = {(unsigned)a, (unsigned)(a >> 32) & 0xffffu, bytes, 0x00020000u};
  return r;
}
DI void glds16(u32x4 srd, unsigned voff, unsigned soff, unsigned lds) {
  asm volatile("s_mov_b32 m0, %3\n\ts_nop 0\n\tbuffer_load_dwordx4 %0, %1, %2 offen lds" ::"v"(voff), "s"(srd), "s"(soff), "s"(lds) : "memory");
}
#define WAITV(n) asm volatile("s_waitcnt vmcnt(" #n ")" ::: "memory")
#define BAR() do { asm volatile("s_waitcnt lgkmcnt(0)" ::: "memory"); __builtin_amdgcn_s_barrier(); asm volatile("" ::: "memory"); } while (0)

DI bf16x8 lds_b128(unsigned a) { return *(const LAS bf16x8*)(uintptr_t)a; }
DI f32x4 lds_f4(unsigned a) { return *(const LAS f32x4*)(uintptr_t)a; }
DI bf16x4 lds_tr(unsigned a) { return __builtin_amdgcn_ds_read_tr16_b64_v4bf16((LAS bf16x4*)(uintptr_t)a); }
DI bf16x8 cat8(bf16x4 a, bf16x4 b) { return __builtin_shufflevector(a, b, 0, 1, 2, 3, 4, 5, 6, 7); }
DI f32x4 mfma32(bf16x8 a, bf16x8 b, f32x4 c) { return __builtin_amdgcn_mfma_f32_16x16x32_bf16(a, b, c, 0, 0, 0); }
DI f32x4 mfma16(bf16x4 a, bf16x4 b, f32x4 c) { return __builtin_amdgcn_mfma_f32_16x16x16bf16_1k(__builtin_bit_cast(s16x4, a), __builtin_bit_cast(s16x4, b), c, 0, 0, 0); }
DI unsigned pk2(float a, float b) { f32x2 f = {a, b}; return __builtin_bit_cast(unsigned, __builtin_convertvector(f, bf16x2)); }
DI bf16x4 pk4(float a, float b, float c, float d) { u32x2 u = {pk2(a, b), pk2(c, d)}; return __builtin_bit_cast(bf16x4, u); }
DI float bflo(unsigned u) { return __uint_as_float(u << 16); }
DI float bfhi(unsigned u) { return __uint_as_float(u & 0xffff0000u); }
DI float dot2(unsigned a, unsigned b, float c) { return __builtin_amdgcn_fdot2_f32_bf16(__builtin_bit_cast(bf16x2, a), __builtin_bit_cast(bf16x2, b), c, false); }
template <int CTRL> DI float dpp(float v) { return __uint_as_float(__builtin_amdgcn_update_dpp(0u, __float_as_uint(v), CTRL, 0xf, 0xf, true)); }
DI float sum8(float v) { v += dpp<0xB1>(v); v += dpp<0x4E>(v); v += dpp<0x141>(v); return v; }
DI float sum16(float v) { v = sum8(v); v += dpp<0x140>(v); return v; }
DI float xsum16(float v) { auto r = __builtin_amdgcn_permlane16_swap(__float_as_uint(v), __float_as_uint(v), false, false); return __uint_as_float(r[0]) + __uint_as_float(r[1]); }
DI float xsum32(float v) { auto r = __builtin_amdgcn_permlane32_swap(__float_as_uint(v), __float_as_uint(v), false, false); return __uint_as_float(r[0]) + __uint_as_float(r[1]); }
DI float xmax16(float v) { auto r = __builtin_amdgcn_permlane16_swap(__float_as_uint(v), __float_as_uint(v), false, false); return fmaxf(__uint_as_float(r[0]), __uint_as_float(r[1])); }
DI float xmax32(float v) { auto r = __builtin_amdgcn_permlane32_swap(__float_as_uint(v), __float_as_uint(v), false, false); return fmaxf(__uint_as_float(r[0]), __uint_as_float(r[1])); }
DI float sum64(float v) { return xsum32(xsum16(sum16(v))); }

constexpr unsigned QST = 34816;
constexpr unsigned QPRE = 3 * QST;
constexpr unsigned QPRE_X = QPRE + 24576;
constexpr unsigned QSTAT = 4 * QST;
constexpr unsigned K1_LDS = QSTAT + 640;

__global__ __launch_bounds__(256, 1) void k1(const u16* __restrict__ x, const u16* __restrict__ wqkv, const u16* __restrict__ w1, char* __restrict__ ws) {
  extern __shared__ __attribute__((aligned(16))) char smem[];
  const unsigned L0 = (unsigned)(uintptr_t)(LAS char*)smem;
  const int tid = threadIdx.x, lane = tid & 63;
  const int w = __builtin_amdgcn_readfirstlane(tid >> 6);
  const int xcd = blockIdx.x & 7, idx = blockIdx.x >> 3;
  const int sq = xcd >> 1, hh = xcd & 1;
  const int st = sq * 8 + (idx >> 2), hg = hh * 4 + (idx & 3);
  const int rg = sq * 64 + hh * 32 + idx;
  const int c = lane & 15, g = lane >> 4, r8 = lane >> 3, pc = lane & 7;
  const u32x4 sx = mk_srd(x, S * D * 2), sw = mk_srd(wqkv, D * 3 * D * 2), s1 = mk_srd(w1, S * F * 2);

  glds16(sx, lane * 16, (rg * 8 + 2 * w) * 1024, L0 + QPRE_X + (2 * w) * 1088);
  glds16(sx, lane * 16, (rg * 8 + 2 * w + 1) * 1024, L0 + QPRE_X + (2 * w + 1) * 1088);
#pragma unroll
  for (int n = 0; n < 6; n++) {
    const unsigned o = (w + 4 * n) * 1024 + lane * 16;
    glds16(s1, o < 22016u ? o : 0x80000000u, rg * 22016, L0 + QPRE + (w + 4 * n) * 1024);
  }
  const unsigned vx0 = (unsigned)((64 * st - 16 + 8 * w + r8) * 1024 + ((pc ^ (((w & 1) << 2) | (r8 >> 1))) << 4));
  const unsigned vx1 = vx0 + 32768u, vx2 = vx0 + 65536u;
  const unsigned vw = (8 * w + r8) * 3072 + hg * 128 + ((((pc >> 1) ^ (((r8 >> 1) & 1) | ((w & 1) << 1))) << 5) | ((pc & 1) << 4));
  auto stage = [&](int s) {
    const unsigned b = L0 + (s & 3) * QST + w * 1024;
    glds16(sx, vx0, s * 128, b);
    glds16(sx, vx1, s * 128, b + 4096);
    if (w < 2) glds16(sx, vx2, s * 128, b + 8192);
#pragma unroll
    for (int sec = 0; sec < 3; sec++) {
      glds16(sw, vw, s * 64 * 3072 + sec * 1024, b + 10240 + sec * 8192);
      glds16(sw, vw, s * 64 * 3072 + 32 * 3072 + sec * 1024, b + 10240 + sec * 8192 + 4096);
    }
  };
  stage(0); stage(1); stage(2);

  WAITV(24);
#pragma unroll
  for (int rr = 0; rr < 2; rr++) {
    const unsigned a = L0 + QPRE_X + (2 * w + rr) * 1088 + lane * 16;
    const u32x4 u = *(const LAS u32x4*)(uintptr_t)a;
    float v[8];
#pragma unroll
    for (int j = 0; j < 4; j++) { v[2 * j] = bflo(u[j]); v[2 * j + 1] = bfhi(u[j]); }
    float s = 0;
#pragma unroll
    for (int j = 0; j < 8; j++) s += v[j];
    const float m = sum64(s) * (1.f / D);
    float q = 0;
#pragma unroll
    for (int j = 0; j < 8; j++) { v[j] -= m; q += v[j] * v[j]; }
    const float r = __builtin_amdgcn_rsqf(sum64(q) * (1.f / D) + EPS);
    float s2 = 0;
#pragma unroll
    for (int j = 0; j < 8; j++) { v[j] *= r; s2 += v[j]; }
    const float m2 = sum64(s2) * (1.f / D);
    float q2 = 0;
#pragma unroll
    for (int j = 0; j < 8; j++) { v[j] -= m2; q2 += v[j] * v[j]; }
    const float r2 = __builtin_amdgcn_rsqf(sum64(q2) * (1.f / D) + EPS);
    u32x4 o;
#pragma unroll
    for (int j = 0; j < 4; j++) o[j] = pk2(v[2 * j] * r2, v[2 * j + 1] * r2);
    *(LAS u32x4*)(uintptr_t)a = o;
  }
  BAR();
  {
    const rsrc_t rht = mk_rsrc(ws + WS_HT, HT_BYTES), rw1t = mk_rsrc(ws + WS_W1T, W1T_BYTES);
    const int q4 = c >> 2, p4 = c & 3;
    const unsigned vo = g * 65536 + (rg >> 2) * 1024 + ((rg & 3) * 16 + c) * 16;
#pragma unroll
    for (int n = 0; n < 2; n++) {
      const unsigned a = L0 + QPRE_X + q4 * 1088 + ((w + 4 * n) * 4 + g) * 32 + p4 * 8;
      const bf16x8 v = cat8(lds_tr(a), lds_tr(a + 4 * 1088));
      __builtin_amdgcn_raw_buffer_store_b128(__builtin_bit_cast(u32x4, v), rht, vo, (w + 4 * n) * 4 * 65536, 0);
    }
#pragma unroll
    for (int n = 0; n < 6; n++) {
      const unsigned a = L0 + QPRE + q4 * 2752 + ((w + 4 * n) * 4 + g) * 32 + p4 * 8;
      const bf16x8 v = cat8(lds_tr(a), lds_tr(a + 4 * 2752));
      __builtin_amdgcn_raw_buffer_store_b128(__builtin_bit_cast(u32x4, v), rw1t, vo + (w + 4 * n) * 4 * 65536, 0, 0);
    }
  }

  f32x4 aq[4], ak[5], av[5], cq, ck, cv;
  const f32x4 z4 = {0.f, 0.f, 0.f, 0.f};
#pragma unroll
  for (int b = 0; b < 5; b++) { ak[b] = z4; av[b] = z4; if (b < 4) aq[b] = z4; }
  cq = z4; ck = z4; cv = z4;
  float sa[3] = {0.f, 0.f, 0.f}, sb[3] = {0.f, 0.f, 0.f};
  const u32x4 ones_u = {0x3f803f80u, 0x3f803f80u, 0x3f803f80u, 0x3f803f80u};
  const bf16x8 ones = __builtin_bit_cast(bf16x8, ones_u);
  const unsigned ax = c * 128 + ((g ^ (c >> 1)) << 4);
  const unsigned aw = 10240 + (8 * g + (c >> 2)) * 128 + ((w ^ ((c >> 3) | ((g & 1) << 1))) << 5) + (c & 3) * 8;
#pragma unroll 1
  for (int s = 0; s < 8; s++) {
    if (s < 3) WAITV(24); else if (s < 6) WAITV(16); else if (s == 6) WAITV(8); else WAITV(0);
    BAR();
    if (s < 5) stage(s + 3);
    const unsigned b = L0 + (s & 3) * QST;
#pragma unroll
    for (int kk = 0; kk < 2; kk++) {
      bf16x8 xf[5], wf[3];
#pragma unroll
      for (int sec = 0; sec < 3; sec++) wf[sec] = cat8(lds_tr(b + aw + sec * 8192 + kk * 4096), lds_tr(b + aw + sec * 8192 + kk * 4096 + 512));
#pragma unroll
      for (int t = 0; t < 5; t++) xf[t] = lds_b128(b + (ax ^ (kk * 64)) + t * 2048);
      cq = mfma32(wf[0], ones, cq);
      ck = mfma32(wf[1], ones, ck);
      cv = mfma32(ones, wf[2], cv);
#pragma unroll
      for (int t = 0; t < 5; t++) {
        ak[t] = mfma32(wf[1], xf[t], ak[t]);
        av[t] = mfma32(xf[t], wf[2], av[t]);
        if (t > 0) aq[t - 1] = mfma32(wf[0], xf[t], aq[t - 1]);
      }
    }
#pragma unroll
    for (int n = 0; n < 3; n++) {
      if (n < 2 || w < 2) {
        const u32x4 u = *(const LAS u32x4*)(uintptr_t)(b + n * 4096 + tid * 16);
#pragma unroll
        for (int j = 0; j < 4; j++) { sa[n] = dot2(u[j], 0x3f803f80u, sa[n]); sb[n] = dot2(u[j], u[j], sb[n]); }
      }
    }
  }
#pragma unroll
  for (int n = 0; n < 3; n++) {
    if (n < 2 || w < 2) {
      const float m = sum8(sa[n]) * (1.f / D);
      const float var = fmaxf(sum8(sb[n]) * (1.f / D) - m * m, 0.f);
      const float r = __builtin_amdgcn_rsqf(var + EPS);
      if (pc == 0) { f32x2 o = {r, m * r}; *(LAS f32x2*)(uintptr_t)(L0 + QSTAT + (n * 32 + (tid >> 3)) * 8) = o; }
    }
  }
  BAR();
  bf16x4 qf[4], kf[5], vf[5];
#pragma unroll
  for (int t = 0; t < 5; t++) {
    const f32x2 sc = *(const LAS f32x2*)(uintptr_t)(L0 + QSTAT + (16 * t + c) * 8);
    kf[t] = pk4(sc[0] * ak[t][0] - sc[1] * ck[0], sc[0] * ak[t][1] - sc[1] * ck[1], sc[0] * ak[t][2] - sc[1] * ck[2], sc[0] * ak[t][3] - sc[1] * ck[3]);
    if (t > 0) qf[t - 1] = pk4(sc[0] * aq[t - 1][0] - sc[1] * cq[0], sc[0] * aq[t - 1][1] - sc[1] * cq[1], sc[0] * aq[t - 1][2] - sc[1] * cq[2], sc[0] * aq[t - 1][3] - sc[1] * cq[3]);
    const f32x4 s0 = lds_f4(L0 + QSTAT + (16 * t + 4 * g) * 8), s1v = lds_f4(L0 + QSTAT + (16 * t + 4 * g) * 8 + 16);
    vf[t] = pk4(s0[0] * av[t][0] - s0[1] * cv[0], s0[2] * av[t][1] - s0[3] * cv[0], s1v[0] * av[t][2] - s1v[1] * cv[0], s1v[2] * av[t][3] - s1v[3] * cv[0]);
  }
  u32x2 op[4];
  const bool odd = c & 1;
  const int k0 = 4 * g + (c & 1), k1 = k0 + 2;
  const bool cur0 = k0 <= c, cur1 = k1 <= c;
  constexpr float SC = 0.25f * 1.4426950408889634f;
#pragma unroll
  for (int t = 1; t < 5; t++) {
    const f32x4 sp = mfma16(kf[t - 1], qf[t - 1], z4), scu = mfma16(kf[t], qf[t - 1], z4);
    const float p0 = odd ? sp[1] : sp[0], p1 = odd ? sp[3] : sp[2];
    const float c0 = odd ? scu[1] : scu[0], c1 = odd ? scu[3] : scu[2];
    float l0 = (cur0 ? c0 : p0) * SC, l1 = (cur1 ? c1 : p1) * SC;
    if (t == 1 && st == 0) { l0 = cur0 ? l0 : -1e9f; l1 = cur1 ? l1 : -1e9f; }
    const float mx = xmax32(xmax16(fmaxf(l0, l1)));
    const float e0 = __builtin_amdgcn_exp2f(l0 - mx), e1 = __builtin_amdgcn_exp2f(l1 - mx);
    const float inv = __builtin_amdgcn_rcpf(xsum32(xsum16(e0 + e1)));
    const unsigned sh = odd ? 16 : 0;
    const unsigned b0 = (pk2(e0 * inv, 0.f) & 0xffffu) << sh, b1 = (pk2(e1 * inv, 0.f) & 0xffffu) << sh;
    const u32x4 pu = {cur0 ? 0u : b0, cur1 ? 0u : b1, cur0 ? b0 : 0u, cur1 ? b1 : 0u};
    const f32x4 o = mfma32(cat8(vf[t - 1], vf[t]), __builtin_bit_cast(bf16x8, pu), z4);
    op[t - 1][0] = pk2(o[0], o[1]); op[t - 1][1] = pk2(o[2], o[3]);
  }
  {
    const rsrc_t ro = mk_rsrc(ws + WS_O, O_BYTES);
#pragma unroll
    for (int t = 0; t < 4; t += 2) {
      const auto r0 = __builtin_amdgcn_permlane16_swap(op[t][0], op[t + 1][0], false, false);
      const auto r1 = __builtin_amdgcn_permlane16_swap(op[t][1], op[t + 1][1], false, false);
      const u32x4 v = {r0[0], r1[0], r0[1], r1[1]};
      const unsigned vo = (g & 1) * 16384 + ((g >> 1) * 16 + c) * 16;
      __builtin_amdgcn_raw_buffer_store_b128(v, ro, vo, ((4 * st + t) * 16 + 2 * hg + (w >> 1)) * 1024 + (w & 1) * 512, 0);
    }
  }
}

constexpr unsigned K2_LDS = 4 * 12 * 1024;
__global__ __launch_bounds__(256, 1) void k2(const u16* __restrict__ b1, char* __restrict__ ws) {
  extern __shared__ __attribute__((aligned(16))) char smem[];
  const unsigned L0 = (unsigned)(uintptr_t)(LAS char*)smem;
  const int tid = threadIdx.x, lane = tid & 63;
  const int w = __builtin_amdgcn_readfirstlane(tid >> 6);
  const int xcd = blockIdx.x & 7, idx = blockIdx.x >> 3;
  const int fq = xcd >> 1, dt = (xcd & 1) * 4 + (idx & 3), fl = idx >> 2;
  if (fq > 0 && fl == 7) return;
  const int ft = (fq == 0 ? 0 : 7 * fq + 1) + fl;
  const int c = lane & 15, g = lane >> 4;
  const rsrc_t rht = mk_rsrc(ws + WS_HT, HT_BYTES), rw1t = mk_rsrc(ws + WS_W1T, W1T_BYTES);
  constexpr int DEP = 8;
  bf16x8 fa[DEP][3], fb[DEP][4];
  const unsigned va = lane * 16 + (3 * ft * 64 + 16 * w) * 1024, vb = lane * 16 + (4 * dt * 64 + 16 * w) * 1024;
  auto load = [&](int k) {
#pragma unroll
    for (int j = 0; j < 4; j++) fb[k % DEP][j] = __builtin_bit_cast(bf16x8, __builtin_amdgcn_raw_buffer_load_b128(rht, vb, j * 65536 + k * 1024, 0));
#pragma unroll
    for (int i = 0; i < 3; i++) fa[k % DEP][i] = __builtin_bit_cast(bf16x8, __builtin_amdgcn_raw_buffer_load_b128(rw1t, va + i * 65536, k * 1024, 0));
  };
  f32x4 acc[3][4];
#pragma unroll
  for (int i = 0; i < 3; i++)
#pragma unroll
    for (int j = 0; j < 4; j++) acc[i][j] = (f32x4){0.f, 0.f, 0.f, 0.f};
#pragma unroll
  for (int k = 0; k < DEP; k++) load(k);
  __builtin_amdgcn_sched_barrier(0);
#pragma unroll
  for (int k = 0; k < 16; k++) {
#pragma unroll
    for (int i = 0; i < 3; i++)
#pragma unroll
      for (int j = 0; j < 4; j++) acc[i][j] = mfma32(fa[k % DEP][i], fb[k % DEP][j], acc[i][j]);
    if (k + DEP < 16) load(k + DEP);
    __builtin_amdgcn_sched_barrier(0);
  }
#pragma unroll
  for (int j = 0; j < 4; j++)
#pragma unroll
    for (int i = 0; i < 3; i++) *(LAS f32x4*)(uintptr_t)(L0 + ((w * 4 + j) * 3 + i) * 1024 + lane * 16) = acc[i][j];
  BAR();
  u32x2 gp[3];
#pragma unroll
  for (int i = 0; i < 3; i++) {
    f32x4 u = {0.f, 0.f, 0.f, 0.f};
#pragma unroll
    for (int ww = 0; ww < 4; ww++) u += lds_f4(L0 + ((ww * 4 + w) * 3 + i) * 1024 + lane * 16);
    const int f0 = min(48 * ft + 16 * i + 4 * g, F - 4);
    const u32x2 bb = *(const u32x2*)(b1 + f0);
    float o[4];
    const float bv[4] = {bflo(bb[0]), bfhi(bb[0]), bflo(bb[1]), bfhi(bb[1])};
#pragma unroll
    for (int r = 0; r < 4; r++) {
      const float t = u[r] + bv[r];
      const float z = t * (1.f + 0.044715f * t * t) * (-2.f * 0.7978845608028654f * 1.4426950408889634f);
      o[r] = t * __builtin_amdgcn_rcpf(1.f + __builtin_amdgcn_exp2f(z));
    }
    gp[i][0] = pk2(o[0], o[1]); gp[i][1] = pk2(o[2], o[3]);
  }
  const rsrc_t rgg = mk_rsrc(ws + WS_G, G_BYTES);
  {
    const auto r0 = __builtin_amdgcn_permlane16_swap(gp[0][0], gp[1][0], false, false);
    const auto r1 = __builtin_amdgcn_permlane16_swap(gp[0][1], gp[1][1], false, false);
    const u32x4 v = {r0[0], r1[0], r0[1], r1[1]};
    const int f8 = 6 * ft + 2 * (g & 1) + (g >> 1);
    __builtin_amdgcn_raw_buffer_store_b128(v, rgg, f8 * 256 + c * 16, (4 * dt + w) * 43 * 1024, 0);
    const auto r2 = __builtin_amdgcn_permlane16_swap(gp[2][0], gp[2][0], false, false);
    const auto r3 = __builtin_amdgcn_permlane16_swap(gp[2][1], gp[2][1], false, false);
    const u32x4 v2 = {r2[0], r3[0], r2[1], r3[1]};
    const int f8b = 6 * ft + 4 + (g >> 1);
    if ((g & 1) == 0 && f8b < 172) __builtin_amdgcn_raw_buffer_store_b128(v2, rgg, f8b * 256 + c * 16, (4 * dt + w) * 43 * 1024, 0);
  }
}

constexpr unsigned K3_LDS = 4 * 16 * 1024;
__global__ __launch_bounds__(256, 1) void k3(const u16* __restrict__ x, const u16* __restrict__ wo, const u16* __restrict__ w2, const u16* __restrict__ b2,
                                             const char* __restrict__ ws, u16* __restrict__ out) {
  extern __shared__ __attribute__((aligned(16))) char smem[];
  const unsigned L0 = (unsigned)(uintptr_t)(LAS char*)smem;
  const int tid = threadIdx.x, lane = tid & 63;
  const int w = __builtin_amdgcn_readfirstlane(tid >> 6);
  const int xcd = blockIdx.x & 7, idx = blockIdx.x >> 3;
  const int st = (xcd >> 1) * 8 + (idx >> 2), dt = (xcd & 1) * 4 + (idx & 3);
  const int c = lane & 15, g = lane >> 4, r8 = lane >> 3, pc = lane & 7;
  const rsrc_t ro = mk_rsrc(ws + WS_O, O_BYTES), rgg = mk_rsrc(ws + WS_G, G_BYTES), rwo = mk_rsrc(wo, D * D * 2), rw2 = mk_rsrc(w2, F * S * 2);
  constexpr int NS = 15, DEP = 7;
  u32x4 fd[DEP][4], fw[DEP][4];
  const unsigned vo = lane * 16 + (4 * st * 16 + 4 * w) * 1024;
  const unsigned vg = lane * 16 + (4 * dt * 43 + 11 * w) * 1024;
  const unsigned vwo = (128 * w + r8) * 1024 + dt * 128 + pc * 16;
  const unsigned vw2 = (352 * w + r8) * 4096 + st * 128 + pc * 16;
  auto load = [&](int k) {
    if (k < 4) {
#pragma unroll
      for (int j = 0; j < 4; j++) fw[k % DEP][j] = __builtin_amdgcn_raw_buffer_load_b128(rwo, vwo, (32 * k + 8 * j) * 1024, 0);
#pragma unroll
      for (int j = 0; j < 4; j++) fd[k % DEP][j] = __builtin_amdgcn_raw_buffer_load_b128(ro, vo, j * 16384 + k * 1024, 0);
    } else {
#pragma unroll
      for (int j = 0; j < 4; j++) fw[k % DEP][j] = __builtin_amdgcn_raw_buffer_load_b128(rw2, vw2 + (32 * (k - 4) + 8 * j) * 4096, 0, 0);
      const unsigned vgk = (k == NS - 1 && w == 3) ? 0x80000000u : vg;
#pragma unroll
      for (int j = 0; j < 4; j++) fd[k % DEP][j] = __builtin_amdgcn_raw_buffer_load_b128(rgg, vgk, j * 43 * 1024 + (k - 4) * 1024, 0);
    }
  };
  f32x4 acc[4][4];
#pragma unroll
  for (int i = 0; i < 4; i++)
#pragma unroll
    for (int j = 0; j < 4; j++) acc[i][j] = (f32x4){0.f, 0.f, 0.f, 0.f};
#pragma unroll
  for (int k = 0; k < DEP; k++) load(k);
  __builtin_amdgcn_sched_barrier(0);
  const unsigned lw = L0 + w * 8192;
  const unsigned aw0 = lw + r8 * 128 + ((((pc >> 1) ^ ((r8 >> 1) & 1)) << 5) | ((pc & 1) << 4));
  const unsigned ar = lw + (8 * g + (c >> 2)) * 128 + ((((c >> 3) | ((g & 1) << 1))) << 5) + (c & 3) * 8;
#pragma unroll
  for (int k = 0; k < NS; k++) {
    const unsigned bo = (k & 1) * 4096;
#pragma unroll
    for (int j = 0; j < 4; j++) *(LAS u32x4*)(uintptr_t)((aw0 ^ ((j & 1) << 6)) + j * 1024 + bo) = fw[k % DEP][j];
    bf16x8 tw[4];
#pragma unroll
    for (int e = 0; e < 4; e++) tw[e] = cat8(lds_tr((ar ^ (e << 5)) + bo), lds_tr((ar ^ (e << 5)) + 512 + bo));
#pragma unroll
    for (int i = 0; i < 4; i++)
#pragma unroll
      for (int j = 0; j < 4; j++)
        acc[i][j] = (k < 4) ? mfma32(tw[i], __builtin_bit_cast(bf16x8, fd[k % DEP][j]), acc[i][j]) : mfma32(__builtin_bit_cast(bf16x8, fd[k % DEP][i]), tw[j], acc[i][j]);
    if (k + DEP < NS) load(k + DEP);
    __builtin_amdgcn_sched_barrier(0);
  }
  BAR();
#pragma unroll
  for (int j = 0; j < 4; j++)
#pragma unroll
    for (int i = 0; i < 4; i++) *(LAS f32x4*)(uintptr_t)(L0 + ((w * 4 + j) * 4 + i) * 1024 + lane * 16) = acc[i][j];
  BAR();
  const int srow = 64 * st + 16 * w + c;
  const float bias = bflo(b2[srow]);
  f32x4 u[4];
#pragma unroll
  for (int i = 0; i < 4; i++) {
    u[i] = (f32x4){bias, bias, bias, bias};
#pragma unroll
    for (int ww = 0; ww < 4; ww++) u[i] += lds_f4(L0 + ((ww * 4 + w) * 4 + i) * 1024 + lane * 16);
  }
#pragma unroll
  for (int i = 0; i < 4; i += 2) {
    float v[8];
#pragma unroll
    for (int r = 0; r < 4; r++) {
      const auto sw = __builtin_amdgcn_permlane16_swap(__float_as_uint(u[i][r]), __float_as_uint(u[i + 1][r]), false, false);
      v[r] = __uint_as_float(sw[0]); v[4 + r] = __uint_as_float(sw[1]);
    }
    const int off = srow * D + 64 * dt + 16 * i + 16 * (g & 1) + 8 * (g >> 1);
    const u32x4 xv = *(const u32x4*)(x + off);
    u32x4 o;
#pragma unroll
    for (int j = 0; j < 4; j++) o[j] = pk2(v[2 * j] + bflo(xv[j]), v[2 * j + 1] + bfhi(xv[j]));
    *(u32x4*)(out + off) = o;
  }
}

extern "C" void kernel_launch(void* const* d_in, const int* in_sizes, int n_in, void* d_out, int out_size, void* d_ws, size_t ws_size, hipStream_t stream) {
  const u16* x = (const u16*)d_in[0];
  const u16* wqkv = (const u16*)d_in[1];
  const u16* wo = (const u16*)d_in[2];
  const u16* w1 = (const u16*)d_in[4];
  const u16* b1 = (const u16*)d_in[5];
  const u16* w2 = (const u16*)d_in[6];
  const u16* b2 = (const u16*)d_in[7];
  (void)hipFuncSetAttribute((const void*)k1, hipFuncAttributeMaxDynamicSharedMemorySize, K1_LDS);
  k1<<<256, 256, K1_LDS, stream>>>(x, wqkv, w1, (char*)d_ws);
  k2<<<256, 256, K2_LDS, stream>>>(b1, (char*)d_ws);
  k3<<<256, 256, K3_LDS, stream>>>(x, wo, w2, b2, (const char*)d_ws, (u16*)d_out);
}
```

```cpp
#include <hip/hip_runtime.h>
#include <stdint.h>

typedef unsigned short u16;
typedef unsigned long long u64;
typedef short s16x4 __attribute__((ext_vector_type(4)));
typedef __bf16 bf16x2 __attribute__((ext_vector_type(2)));
typedef __bf16 bf16x4 __attribute__((ext_vector_type(4)));
typedef __bf16 bf16x8 __attribute__((ext_vector_type(8)));
typedef float f32x2 __attribute__((ext_vector_type(2)));
typedef float f32x4 __attribute__((ext_vector_type(4)));
typedef unsigned u32x2 __attribute__((ext_vector_type(2)));
typedef unsigned u32x4 __attribute__((ext_vector_type(4)));
#define LAS __attribute__((address_space(3)))
#define GAS __attribute__((address_space(1)))
#define DI __device__ __forceinline__

constexpr int S = 2048, D = 512, F = 1376;
constexpr float EPS = 1e-5f;
constexpr unsigned HT_BYTES = 32u * 64u * 1024u;
constexpr unsigned W1T_BYTES = 86u * 64u * 1024u;
constexpr unsigned G_BYTES = 32u * 43u * 1024u;
constexpr unsigned O_BYTES = 128u * 16u * 1024u;
constexpr size_t WS_EPOCH = 0;
constexpr size_t WS_F1 = 4096;
constexpr size_t WS_F2 = WS_F1 + 256 * 32;
constexpr size_t WS_F3 = WS_F2 + 256 * 128;
constexpr size_t WS_HT = 131072;
constexpr size_t WS_W1T = WS_HT + HT_BYTES;
constexpr size_t WS_G = WS_W1T + W1T_BYTES;
constexpr size_t WS_O = WS_G + G_BYTES;
constexpr int SC1 = 16;

typedef __amdgpu_buffer_rsrc_t rsrc_t;
DI rsrc_t mk_rsrc(const void* p, unsigned bytes) { return __builtin_amdgcn_make_buffer_rsrc((void*)p, 0, bytes, 0x00020000); }
DI u32x4 mk_srd(const void* p, unsigned bytes) {
  const uint64_t a = (uint64_t)p;
  u32x4 r = {(unsigned)a, (unsigned)(a >> 32) & 0xffffu, bytes, 0x00020000u};
  return r;
}
DI void glds16(u32x4 srd, unsigned voff, unsigned soff, unsigned lds) {
  asm volatile("s_mov_b32 m0, %3\n\ts_nop 0\n\tbuffer_load_dwordx4 %0, %1, %2 offen lds" ::"v"(voff), "s"(srd), "s"(soff), "s"(lds) : "memory");
}
#define WAITV(n) asm volatile("s_waitcnt vmcnt(" #n ")" ::: "memory")
#define FENCE() asm volatile("" ::: "memory")
#define SCHED() __builtin_amdgcn_sched_barrier(0)
#define BAR() do { asm volatile("s_waitcnt lgkmcnt(0)" ::: "memory"); __builtin_amdgcn_s_barrier(); FENCE(); } while (0)

DI bf16x8 lds_b128(unsigned a) { return *(const LAS bf16x8*)(uintptr_t)a; }
DI f32x4 lds_f4(unsigned a) { return *(const LAS f32x4*)(uintptr_t)a; }
DI bf16x4 lds_tr(unsigned a) { return __builtin_amdgcn_ds_read_tr16_b64_v4bf16((LAS bf16x4*)(uintptr_t)a); }
DI bf16x8 cat8(bf16x4 a, bf16x4 b) { return __builtin_shufflevector(a, b, 0, 1, 2, 3, 4, 5, 6, 7); }
DI f32x4 mfma32(bf16x8 a, bf16x8 b, f32x4 c) { return __builtin_amdgcn_mfma_f32_16x16x32_bf16(a, b, c, 0, 0, 0); }
DI f32x4 mfma16(bf16x4 a, bf16x4 b, f32x4 c) { return __builtin_amdgcn_mfma_f32_16x16x16bf16_1k(__builtin_bit_cast(s16x4, a), __builtin_bit_cast(s16x4, b), c, 0, 0, 0); }
DI unsigned pk2(float a, float b) { f32x2 f = {a, b}; return __builtin_bit_cast(unsigned, __builtin_convertvector(f, bf16x2)); }
DI bf16x4 pk4(float a, float b, float c, float d) { u32x2 u = {pk2(a, b), pk2(c, d)}; return __builtin_bit_cast(bf16x4, u); }
DI float bflo(unsigned u) { return __uint_as_float(u << 16); }
DI float bfhi(unsigned u) { return __uint_as_float(u & 0xffff0000u); }
DI float dot2(unsigned a, unsigned b, float c) { return __builtin_amdgcn_fdot2_f32_bf16(__builtin_bit_cast(bf16x2, a), __builtin_bit_cast(bf16x2, b), c, false); }
template <int CTRL> DI float dpp(float v) { return __uint_as_float(__builtin_amdgcn_update_dpp(0u, __float_as_uint(v), CTRL, 0xf, 0xf, true)); }
DI float sum8(float v) { v += dpp<0xB1>(v); v += dpp<0x4E>(v); v += dpp<0x141>(v); return v; }
DI float sum16(float v) { v = sum8(v); v += dpp<0x140>(v); return v; }
DI float xsum16(float v) { auto r = __builtin_amdgcn_permlane16_swap(__float_as_uint(v), __float_as_uint(v), false, false); return __uint_as_float(r[0]) + __uint_as_float(r[1]); }
DI float xsum32(float v) { auto r = __builtin_amdgcn_permlane32_swap(__float_as_uint(v), __float_as_uint(v), false, false); return __uint_as_float(r[0]) + __uint_as_float(r[1]); }
DI float xmax16(float v) { auto r = __builtin_amdgcn_permlane16_swap(__float_as_uint(v), __float_as_uint(v), false, false); return fmaxf(__uint_as_float(r[0]), __uint_as_float(r[1])); }
DI float xmax32(float v) { auto r = __builtin_amdgcn_permlane32_swap(__float_as_uint(v), __float_as_uint(v), false, false); return fmaxf(__uint_as_float(r[0]), __uint_as_float(r[1])); }
DI float sum64(float v) { return xsum32(xsum16(sum16(v))); }
DI u32x4 bload(rsrc_t r, unsigned v, unsigned s) { return __builtin_amdgcn_raw_buffer_load_b128(r, v, s, 0); }
DI u32x4 bload1(rsrc_t r, unsigned v, unsigned s) { return __builtin_amdgcn_raw_buffer_load_b128(r, v, s, SC1); }
DI bf16x8 as_bf(u32x4 v) { return __builtin_bit_cast(bf16x8, v); }

constexpr unsigned LDS_BYTES = 163840;
constexpr unsigned QST = 34816;
constexpr unsigned QPRE = 3 * QST;
constexpr unsigned QPRE_X = QPRE + 22528;
constexpr unsigned P2STG = 0;
constexpr unsigned P2TMP = 114688;
constexpr unsigned E2SCR = 114688;
constexpr unsigned QSTAT = LDS_BYTES - 1024;
constexpr unsigned SY = QSTAT + 640;
enum { Y_FULL = 0, Y_FREE = 16, Y_H = 32, Y_W1 = 36, Y_PRE = 40, Y_F1 = 44, Y_QS = 48, Y_F2 = 52, Y_ISS = 56, Y_E2 = 60, Y_F3 = 64, Y_TAGOK = 68, Y_TAG = 72 };
constexpr unsigned SPIN_LDS = 1u << 22, SPIN_GLB = 1u << 18;

DI unsigned lds_ld(unsigned a) { return __hip_atomic_load((LAS unsigned*)(uintptr_t)a, __ATOMIC_RELAXED, __HIP_MEMORY_SCOPE_WORKGROUP); }
DI void lds_st(unsigned a, unsigned v) { __hip_atomic_store((LAS unsigned*)(uintptr_t)a, v, __ATOMIC_RELAXED, __HIP_MEMORY_SCOPE_WORKGROUP); }
DI void lds_arrive(unsigned a, int lane) { FENCE(); if (lane == 0) __hip_atomic_fetch_add((LAS unsigned*)(uintptr_t)a, 1u, __ATOMIC_RELAXED, __HIP_MEMORY_SCOPE_WORKGROUP); FENCE(); }
DI void lds_wait(unsigned a, unsigned target) {
  FENCE();
#pragma unroll 1
  for (unsigned n = 0; n < SPIN_LDS; n++) { if ((unsigned)__builtin_amdgcn_readfirstlane(lds_ld(a)) >= target) break; __builtin_amdgcn_s_sleep(1); }
  FENCE();
}
DI void publish(unsigned cnt, unsigned nwaves, char* flag, u64 tag, int lane) {
  WAITV(0);
  if (lane == 0) {
    const unsigned old = __hip_atomic_fetch_add((LAS unsigned*)(uintptr_t)cnt, 1u, __ATOMIC_RELAXED, __HIP_MEMORY_SCOPE_WORKGROUP);
    if (old == nwaves - 1) __hip_atomic_store((GAS u64*)flag, tag, __ATOMIC_RELAXED, __HIP_MEMORY_SCOPE_AGENT);
  }
  FENCE();
}
DI void poll(const char* flag, u64 tag) {
  FENCE();
#pragma unroll 1
  for (unsigned n = 0; n < SPIN_GLB; n++) {
    const u64 v = __hip_atomic_load((const GAS u64*)flag, __ATOMIC_RELAXED, __HIP_MEMORY_SCOPE_AGENT);
    if (__all(v == tag)) break;
    __builtin_amdgcn_s_sleep(1);
  }
  FENCE();
}
DI u64 get_tag(unsigned L0) {
  lds_wait(L0 + SY + Y_TAGOK, 1);
  const unsigned lo = __builtin_amdgcn_readfirstlane(lds_ld(L0 + SY + Y_TAG)), hi = __builtin_amdgcn_readfirstlane(lds_ld(L0 + SY + Y_TAG + 4));
  return ((u64)hi << 32) | lo;
}

#define ST(i)
#define STDECL
#define STFLUSH()

__global__ __launch_bounds__(512, 2) void fused(const u16* __restrict__ x, const u16* __restrict__ wqkv, const u16* __restrict__ wo, const u16* __restrict__ w1,
                                                const u16* __restrict__ b1, const u16* __restrict__ w2, const u16* __restrict__ b2, char* __restrict__ ws,
                                                u16* __restrict__ out) {
  extern __shared__ __attribute__((aligned(16))) char smem[];
  const unsigned L0 = (unsigned)(uintptr_t)(LAS char*)smem;
  const int tid = threadIdx.x, lane = tid & 63;
  const int W = __builtin_amdgcn_readfirstlane(tid >> 6), w = W & 3;
  const int xcd = blockIdx.x & 7, idx = blockIdx.x >> 3;
  const int sq = xcd >> 1, hh = xcd & 1;
  const int st = sq * 8 + (idx >> 2), ct = hh * 4 + (idx & 3);
  const int rg = sq * 64 + hh * 32 + idx;
  const int c = lane & 15, g = lane >> 4, r8 = lane >> 3, pc = lane & 7;
  const f32x4 z4 = {0.f, 0.f, 0.f, 0.f};
  STDECL ST(0);
  if (tid < 24) lds_st(L0 + SY + tid * 4, 0u);

  if (W < 4) {
    const rsrc_t rx = mk_rsrc(x, S * D * 2);
    u32x4 xr[2];
#pragma unroll
    for (int rr = 0; rr < 2; rr++) xr[rr] = bload(rx, lane * 16, (rg * 8 + 2 * w + rr) * 1024);
    u64 ep = 0;
    if (w == 0) ep = __hip_atomic_load((const GAS u64*)(ws + WS_EPOCH), __ATOMIC_RELAXED, __HIP_MEMORY_SCOPE_AGENT);
    BAR();
#pragma unroll
    for (int rr = 0; rr < 2; rr++) {
      float v[8];
#pragma unroll
      for (int j = 0; j < 4; j++) { v[2 * j] = bflo(xr[rr][j]); v[2 * j + 1] = bfhi(xr[rr][j]); }
      float s = 0;
#pragma unroll
      for (int j = 0; j < 8; j++) s += v[j];
      const float m = sum64(s) * (1.f / D);
      float q = 0;
#pragma unroll
      for (int j = 0; j < 8; j++) { v[j] -= m; q += v[j] * v[j]; }
      const float r = __builtin_amdgcn_rsqf(sum64(q) * (1.f / D) + EPS);
      float s2 = 0;
#pragma unroll
      for (int j = 0; j < 8; j++) { v[j] *= r; s2 += v[j]; }
      const float m2 = sum64(s2) * (1.f / D);
      float q2 = 0;
#pragma unroll
      for (int j = 0; j < 8; j++) { v[j] -= m2; q2 += v[j] * v[j]; }
      const float r2 = __builtin_amdgcn_rsqf(sum64(q2) * (1.f / D) + EPS);
      u32x4 o;
#pragma unroll
      for (int j = 0; j < 4; j++) o[j] = pk2(v[2 * j] * r2, v[2 * j + 1] * r2);
      *(LAS u32x4*)(uintptr_t)(L0 + QPRE_X + (2 * w + rr) * 1088 + lane * 16) = o;
    }
    lds_arrive(L0 + SY + Y_H, lane);
    if (w == 0 && lane == 0) { const u64 t = ep + 1; lds_st(L0 + SY + Y_TAG, (unsigned)t); lds_st(L0 + SY + Y_TAG + 4, (unsigned)(t >> 32)); FENCE(); lds_st(L0 + SY + Y_TAGOK, 1u); }
    ST(1);

    f32x4 aq[5], ak[5], av[5];
#pragma unroll
    for (int b = 0; b < 5; b++) { ak[b] = z4; av[b] = z4; aq[b] = z4; }
    float sa[3] = {0.f, 0.f, 0.f}, sb[3] = {0.f, 0.f, 0.f};
    const unsigned ax = c * 128 + ((g ^ (c >> 1)) << 4);
    const unsigned aw = 10240 + (8 * g + (c >> 2)) * 128 + ((w ^ ((c >> 3) | ((g & 1) << 1))) << 5) + (c & 3) * 8;
    const unsigned as2 = (w < 2 ? 8192 : 0) + tid * 16;
    bf16x8 xf[2][5], wf[2][3];
    auto rd = [&](int buf, unsigned b, int kk) {
#pragma unroll
      for (int sec = 0; sec < 3; sec++) wf[buf][sec] = cat8(lds_tr(b + aw + sec * 8192 + kk * 4096), lds_tr(b + aw + sec * 8192 + kk * 4096 + 512));
#pragma unroll
      for (int t = 0; t < 5; t++) xf[buf][t] = lds_b128(b + (ax ^ (kk * 64)) + t * 2048);
    };
    auto mm = [&](int buf) {
      u32x4 x0 = __builtin_bit_cast(u32x4, xf[buf][0]);
#pragma unroll
      for (int j = 0; j < 4; j++) x0[j] = c == 0 ? 0x3f803f80u : x0[j];
#pragma unroll
      for (int t = 4; t >= 0; t--) {
        const bf16x8 xx = t == 0 ? as_bf(x0) : xf[buf][t];
        ak[t] = mfma32(wf[buf][1], xx, ak[t]);
        av[t] = mfma32(xx, wf[buf][2], av[t]);
        aq[t] = mfma32(wf[buf][0], xx, aq[t]);
      }
    };
    auto wait_full = [&](unsigned n) {
      FENCE();
#pragma unroll 1
      for (unsigned i = 0; i < SPIN_LDS; i++) {
        const unsigned f0 = lds_ld(L0 + SY + Y_FULL), f1 = lds_ld(L0 + SY + Y_FULL + 4);
        if ((unsigned)__builtin_amdgcn_readfirstlane(min(f0, f1)) >= n) break;
        __builtin_amdgcn_s_sleep(1);
      }
      FENCE();
    };
    wait_full(1);
    ST(2);
    rd(0, L0, 0);
#pragma unroll 1
    for (int s = 0; s < 8; s++) {
      const unsigned b = L0 + (s & 3) * QST;
      rd(1, b, 1);
      u32x4 su[3];
      su[0] = *(const LAS u32x4*)(uintptr_t)(b + tid * 16);
      su[1] = *(const LAS u32x4*)(uintptr_t)(b + 4096 + tid * 16);
      su[2] = *(const LAS u32x4*)(uintptr_t)(b + as2);
      FENCE();
      if (lane == 0) lds_st(L0 + SY + Y_FREE + w * 4, s + 1);
      SCHED();
      mm(0);
      SCHED();
      if (s < 7) { wait_full(s + 2); rd(0, L0 + ((s + 1) & 3) * QST, 0); }
      SCHED();
      mm(1);
#pragma unroll
      for (int n = 0; n < 3; n++)
#pragma unroll
        for (int j = 0; j < 4; j++) { sa[n] = dot2(su[n][j], 0x3f803f80u, sa[n]); sb[n] = dot2(su[n][j], su[n][j], sb[n]); }
      SCHED();
    }
    ST(3);
#pragma unroll
    for (int n = 0; n < 3; n++) {
      const float m = sum8(sa[n]) * (1.f / D);
      const float var = fmaxf(sum8(sb[n]) * (1.f / D) - m * m, 0.f);
      const float r = __builtin_amdgcn_rsqf(var + EPS);
      if (pc == 0 && (n < 2 || w < 2)) { f32x2 o = {r, m * r}; *(LAS f32x2*)(uintptr_t)(L0 + QSTAT + (n * 32 + (tid >> 3)) * 8) = o; }
    }
    lds_arrive(L0 + SY + Y_QS, lane);
    float cq[4], ck[4];
#pragma unroll
    for (int j = 0; j < 4; j++) { cq[j] = dpp<0x150>(aq[0][j]); ck[j] = dpp<0x150>(ak[0][j]); }
    const float cv = xsum32(xsum16(g == 0 ? av[0][0] : 0.f));
    lds_wait(L0 + SY + Y_QS, 4);
    bf16x4 qf[4], kf[5], vf[5];
#pragma unroll
    for (int t = 0; t < 5; t++) {
      const f32x2 sc = *(const LAS f32x2*)(uintptr_t)(L0 + QSTAT + (16 * t + c) * 8);
      kf[t] = pk4(sc[0] * ak[t][0] - sc[1] * ck[0], sc[0] * ak[t][1] - sc[1] * ck[1], sc[0] * ak[t][2] - sc[1] * ck[2], sc[0] * ak[t][3] - sc[1] * ck[3]);
      if (t > 0) qf[t - 1] = pk4(sc[0] * aq[t][0] - sc[1] * cq[0], sc[0] * aq[t][1] - sc[1] * cq[1], sc[0] * aq[t][2] - sc[1] * cq[2], sc[0] * aq[t][3] - sc[1] * cq[3]);
      const f32x4 s0 = lds_f4(L0 + QSTAT + (16 * t + 4 * g) * 8), s1v = lds_f4(L0 + QSTAT + (16 * t + 4 * g) * 8 + 16);
      vf[t] = pk4(s0[0] * av[t][0] - s0[1] * cv, s0[2] * av[t][1] - s0[3] * cv, s1v[0] * av[t][2] - s1v[1] * cv, s1v[2] * av[t][3] - s1v[3] * cv);
    }
    u32x2 op[4];
    const bool odd = c & 1;
    const int k0 = 4 * g + (c & 1), k1 = k0 + 2;
    const bool cur0 = k0 <= c, cur1 = k1 <= c;
    constexpr float SC = 0.25f * 1.4426950408889634f;
#pragma unroll
    for (int t = 1; t < 5; t++) {
      const f32x4 sp = mfma16(kf[t - 1], qf[t - 1], z4), scu = mfma16(kf[t], qf[t - 1], z4);
      const float p0 = odd ? sp[1] : sp[0], p1 = odd ? sp[3] : sp[2];
      const float c0 = odd ? scu[1] : scu[0], c1 = odd ? scu[3] : scu[2];
      float l0 = (cur0 ? c0 : p0) * SC, l1 = (cur1 ? c1 : p1) * SC;
      if (t == 1 && st == 0) { l0 = cur0 ? l0 : -1e9f; l1 = cur1 ? l1 : -1e9f; }
      const float mx = xmax32(xmax16(fmaxf(l0, l1)));
      const float e0 = __builtin_amdgcn_exp2f(l0 - mx), e1 = __builtin_amdgcn_exp2f(l1 - mx);
      const float inv = __builtin_amdgcn_rcpf(xsum32(xsum16(e0 + e1)));
      const unsigned sh = odd ? 16 : 0;
      const unsigned b0 = (pk2(e0 * inv, 0.f) & 0xffffu) << sh, b1v = (pk2(e1 * inv, 0.f) & 0xffffu) << sh;
      const u32x4 pu = {cur0 ? 0u : b0, cur1 ? 0u : b1v, cur0 ? b0 : 0u, cur1 ? b1v : 0u};
      const f32x4 o = mfma32(cat8(vf[t - 1], vf[t]), as_bf(pu), z4);
      op[t - 1][0] = pk2(o[0], o[1]); op[t - 1][1] = pk2(o[2], o[3]);
    }
    const rsrc_t ro = mk_rsrc(ws + WS_O, O_BYTES);
#pragma unroll
    for (int t = 0; t < 4; t += 2) {
      const auto r0 = __builtin_amdgcn_permlane16_swap(op[t][0], op[t + 1][0], false, false);
      const auto r1 = __builtin_amdgcn_permlane16_swap(op[t][1], op[t + 1][1], false, false);
      const u32x4 v = {r0[0], r1[0], r0[1], r1[1]};
      const unsigned vo = (g & 1) * 16384 + ((g >> 1) * 16 + c) * 16;
      __builtin_amdgcn_raw_buffer_store_b128(v, ro, vo, ((4 * st + t) * 16 + 2 * ct + (w >> 1)) * 1024 + (w & 1) * 512, SC1);
    }
    const u64 tag = get_tag(L0);
    publish(L0 + SY + Y_F2, 4, ws + WS_F2 + (st * 8 + ct) * 128, tag, lane);
    ST(4);

    const rsrc_t rgg = mk_rsrc(ws + WS_G, G_BYTES), rwo = mk_rsrc(wo, D * D * 2), rw2 = mk_rsrc(w2, F * S * 2);
    lds_wait(L0 + SY + Y_ISS, 4);
    poll(ws + WS_F2 + (st * 8 + (lane & 7)) * 128, tag);
    ST(5);
    f32x4 acc[4][4];
#pragma unroll
    for (int i = 0; i < 4; i++)
#pragma unroll
      for (int j = 0; j < 4; j++) acc[i][j] = z4;
    const unsigned vo = lane * 16 + (4 * st * 16 + 4 * w) * 1024;
    const unsigned vg = lane * 16 + (4 * ct * 43 + 11 * w) * 1024;
    const unsigned vwo = (128 * w + r8) * 1024 + ct * 128 + pc * 16;
    const unsigned vw2 = (352 * w + r8) * 4096 + st * 128 + pc * 16;
    const unsigned lw = L0 + P2STG + w * 28672;
    const unsigned aw0 = lw + r8 * 128 + ((((pc >> 1) ^ ((r8 >> 1) & 1)) << 5) | ((pc & 1) << 4));
    const unsigned ar = lw + (8 * g + (c >> 2)) * 128 + ((((c >> 3) | ((g & 1) << 1))) << 5) + (c & 3) * 8;
    u32x4 ow[4][4], of[4][4], wr[11][4], gf[11][4];
    auto LO = [&](int k) {
#pragma unroll
      for (int j = 0; j < 4; j++) ow[k][j] = bload(rwo, vwo, (32 * k + 8 * j) * 1024);
#pragma unroll
      for (int j = 0; j < 4; j++) of[k][j] = bload1(ro, vo, ((w + j) & 3) * 16384 + k * 1024);
    };
    auto LW = [&](int k) {
#pragma unroll
      for (int j = 0; j < 4; j++) wr[k][j] = bload(rw2, vw2 + (32 * k + 8 * j) * 4096, 0);
    };
    auto LG = [&](int k) {
      const unsigned vgk = (k == 10 && w == 3) ? 0x80000000u : vg;
#pragma unroll
      for (int j = 0; j < 4; j++) gf[k][j] = bload1(rgg, vgk, j * 43 * 1024 + k * 1024);
    };
    auto SW = [&](const u32x4 (&v)[4], int slot) {
#pragma unroll
      for (int j = 0; j < 4; j++) *(LAS u32x4*)(uintptr_t)((aw0 ^ ((j & 1) << 6)) + j * 1024 + slot * 4096) = v[j];
    };
    auto CO = [&](int k) {
      SW(ow[k], 5 + (k & 1));
      bf16x8 tw[4];
#pragma unroll
      for (int e = 0; e < 4; e++) tw[e] = cat8(lds_tr((ar ^ (e << 5)) + (5 + (k & 1)) * 4096), lds_tr((ar ^ (e << 5)) + 512 + (5 + (k & 1)) * 4096));
#pragma unroll
      for (int i = 0; i < 4; i++)
#pragma unroll
        for (int j = 0; j < 4; j++) acc[i][j] = mfma32(tw[i], as_bf(of[k][j]), acc[i][j]);
    };
    unsigned arr[4];
#pragma unroll
    for (int j = 0; j < 4; j++) arr[j] = ar ^ (((w + j) & 3) << 5);
    auto TR = [&](bf16x8 (&t)[4], int slot) {
#pragma unroll
      for (int j = 0; j < 4; j++) t[j] = cat8(lds_tr(arr[j] + slot * 4096), lds_tr(arr[j] + 512 + slot * 4096));
    };
    LO(0); LO(1); LO(2); SCHED();
    CO(0); SCHED(); LO(3); SCHED();
    CO(1); SCHED(); LW(7); LW(8); SCHED();
    CO(2); SCHED(); LW(9); LW(10); SCHED();
    CO(3); SCHED(); LW(0); LW(1); SCHED();
    bf16x8 tk[4][4];
    SW(wr[7], 5); TR(tk[0], 5); SW(wr[8], 6); TR(tk[1], 6); SCHED(); LW(2); LW(3); SCHED();
    SW(wr[9], 5); TR(tk[2], 5); SW(wr[10], 6); TR(tk[3], 6); SCHED(); LW(4); LW(5); SCHED();
    SW(wr[0], 0); SW(wr[1], 1); SCHED(); LW(6);
    const int srow = 64 * st + 16 * w + c;
    u32x4 xv[2];
#pragma unroll
    for (int i = 0; i < 2; i++) xv[i] = bload(rx, (srow * D + 64 * ct + 32 * i + 16 * (g & 1) + 8 * (g >> 1)) * 2, 0);
    const float bias = bflo(b2[srow]);
    SCHED();
    SW(wr[2], 2); SW(wr[3], 3); SW(wr[4], 4); SW(wr[5], 5); SW(wr[6], 6);
    ST(6);
    {
      const int f0 = (352 * w) / 48 + (lane & 15);
      poll(ws + WS_F3 + (ct * 32 + min(f0, min(28, (352 * w + 351) / 48))) * 128, tag);
    }
    ST(7);
    LG(0); LG(1); LG(2); LG(3);
    bf16x8 tw[2][4];
    TR(tw[0], 0);
    SCHED();
#pragma unroll
    for (int k = 0; k < 11; k++) {
      if (k + 1 < 7) TR(tw[(k + 1) & 1], k + 1);
#pragma unroll
      for (int i = 0; i < 4; i++)
#pragma unroll
        for (int j = 0; j < 4; j++) acc[i][j] = mfma32(as_bf(gf[k][i]), k < 7 ? tw[k & 1][j] : tk[k < 7 ? 0 : k - 7][j], acc[i][j]);
      if (k + 4 < 11) LG(k + 4);
      SCHED();
    }
    ST(8);
    BAR();
#pragma unroll
    for (int j = 1; j < 4; j++)
#pragma unroll
      for (int i = 0; i < 4; i++) *(LAS f32x4*)(uintptr_t)(L0 + ((((w + j) & 3) * 3 + (j - 1)) * 4 + i) * 1024 + lane * 16) = acc[i][j];
    BAR();
    ST(9);
    f32x4 u[4];
#pragma unroll
    for (int i = 0; i < 4; i++) {
      u[i] = acc[i][0] + bias;
#pragma unroll
      for (int j = 0; j < 3; j++) u[i] += lds_f4(L0 + ((w * 3 + j) * 4 + i) * 1024 + lane * 16);
    }
#pragma unroll
    for (int i = 0; i < 4; i += 2) {
      float v[8];
#pragma unroll
      for (int r = 0; r < 4; r++) {
        const auto sw = __builtin_amdgcn_permlane16_swap(__float_as_uint(u[i][r]), __float_as_uint(u[i + 1][r]), false, false);
        v[r] = __uint_as_float(sw[0]); v[4 + r] = __uint_as_float(sw[1]);
      }
      u32x4 o;
#pragma unroll
      for (int j = 0; j < 4; j++) o[j] = pk2(v[2 * j] + bflo(xv[i >> 1][j]), v[2 * j + 1] + bfhi(xv[i >> 1][j]));
      *(u32x4*)(out + srow * D + 64 * ct + 16 * i + 16 * (g & 1) + 8 * (g >> 1)) = o;
    }
    ST(10);
  } else {
    if (w < 2) {
      const u32x4 sx = mk_srd(x, S * D * 2), sw = mk_srd(wqkv, D * 3 * D * 2);
      const unsigned vx0 = (unsigned)((64 * st - 16 + 8 * w + r8) * 1024 + ((pc ^ ((w << 2) | (r8 >> 1))) << 4));
      const unsigned vx1 = vx0 + 16384u;
      const unsigned vw = (8 * w + r8) * 3072 + ct * 128 + ((((pc >> 1) ^ (((r8 >> 1) & 1) | (w << 1))) << 5) | ((pc & 1) << 4));
      auto half0 = [&](int s) {
        const unsigned b = L0 + (s & 3) * QST + w * 1024;
        glds16(sx, vx0, s * 128, b);
#pragma unroll
        for (int j = 0; j < 4; j++) glds16(sx, vx1, s * 128 + j * 16384, b + 2048 + j * 2048);
#pragma unroll
        for (int sec = 0; sec < 3; sec++) glds16(sw, vw, s * 64 * 3072 + sec * 1024, b + 10240 + sec * 8192);
      };
      auto half1 = [&](int s) {
        const unsigned b = L0 + (s & 3) * QST + w * 1024;
#pragma unroll
        for (int j = 1; j < 4; j++)
#pragma unroll
          for (int sec = 0; sec < 3; sec++) glds16(sw, vw, s * 64 * 3072 + j * 16 * 3072 + sec * 1024, b + 10240 + sec * 8192 + j * 2048);
      };
      half0(0);
      BAR();
      half1(0);
#pragma unroll 1
      for (int s = 1; s < 8; s++) {
        if (s == 3) lds_wait(L0 + SY + Y_PRE, 2);
        if (s >= 4) {
          FENCE();
#pragma unroll 1
          for (unsigned i = 0; i < SPIN_LDS; i++) {
            const unsigned f = min(min(lds_ld(L0 + SY + Y_FREE), lds_ld(L0 + SY + Y_FREE + 4)), min(lds_ld(L0 + SY + Y_FREE + 8), lds_ld(L0 + SY + Y_FREE + 12)));
            if ((unsigned)__builtin_amdgcn_readfirstlane(f) >= (unsigned)(s - 3)) break;
            __builtin_amdgcn_s_sleep(1);
          }
          FENCE();
        }
        half0(s);
        if (s >= 2) { WAITV(25); if (lane == 0) lds_st(L0 + SY + Y_FULL + w * 4, s - 1); FENCE(); }
        half1(s);
        if (s == 2) { WAITV(17); if (lane == 0) lds_st(L0 + SY + Y_FULL + w * 4, 2); FENCE(); }
      }
      ST(1);
      WAITV(17); if (lane == 0) lds_st(L0 + SY + Y_FULL + w * 4, 7);
      WAITV(0); if (lane == 0) lds_st(L0 + SY + Y_FULL + w * 4, 8);
      FENCE();
    } else {
      const u32x4 s1 = mk_srd(w1, S * F * 2);
      const int v = w - 2;
#pragma unroll
      for (int n = 0; n < 11; n++) {
        const unsigned o = (v + 2 * n) * 1024 + lane * 16;
        glds16(s1, (n < 10 || o < 22016u) ? o : 0x80000000u, rg * 22016, L0 + QPRE + (v + 2 * n) * 1024);
      }
      BAR();
      WAITV(0);
      lds_arrive(L0 + SY + Y_W1, lane);
      const rsrc_t rht = mk_rsrc(ws + WS_HT, HT_BYTES), rw1t = mk_rsrc(ws + WS_W1T, W1T_BYTES);
      const int q4 = c >> 2, p4 = c & 3;
      const unsigned vo = (4 * v + g) * 65536 + (rg >> 2) * 1024 + ((rg & 3) * 16 + c) * 16;
      lds_wait(L0 + SY + Y_W1, 2);
      bf16x8 tv[15];
#pragma unroll
      for (int n = 0; n < 11; n++) {
        const unsigned a = L0 + QPRE + q4 * 2752 + ((v + 2 * n) * 4 + g) * 32 + p4 * 8;
        tv[n] = cat8(lds_tr(a), lds_tr(a + 4 * 2752));
      }
      lds_wait(L0 + SY + Y_H, 4);
#pragma unroll
      for (int n = 0; n < 4; n++) {
        const unsigned a = L0 + QPRE_X + q4 * 1088 + ((v + 2 * n) * 4 + g) * 32 + p4 * 8;
        tv[11 + n] = cat8(lds_tr(a), lds_tr(a + 4 * 1088));
      }
      lds_arrive(L0 + SY + Y_PRE, lane);
#pragma unroll
      for (int n = 0; n < 11; n++) __builtin_amdgcn_raw_buffer_store_b128(__builtin_bit_cast(u32x4, tv[n]), rw1t, vo + n * 8 * 65536, 0, SC1);
#pragma unroll
      for (int n = 0; n < 4; n++) __builtin_amdgcn_raw_buffer_store_b128(__builtin_bit_cast(u32x4, tv[11 + n]), rht, vo, n * 8 * 65536, SC1);
      ST(1);
      publish(L0 + SY + Y_F1, 2, ws + WS_F1 + rg * 32, get_tag(L0), lane);
    }
    ST(2);
    const int fl = idx >> 2;
    if (sq == 0 || fl < 7) {
      const u64 tag = get_tag(L0);
      const int ft = (sq == 0 ? 0 : 7 * sq + 1) + fl;
      const rsrc_t rht = mk_rsrc(ws + WS_HT, HT_BYTES), rw1t = mk_rsrc(ws + WS_W1T, W1T_BYTES);
      u32x2 bb[3];
#pragma unroll
      for (int i = 0; i < 3; i++) bb[i] = *(const u32x2*)(b1 + min(48 * ft + 16 * i + 4 * g, F - 4));
      poll(ws + WS_F1 + (64 * w + lane) * 32, tag);
      ST(3);
      constexpr int DEP = 4;
      u32x4 fa[DEP][3], fb[DEP][4];
      const unsigned va = lane * 16 + (3 * ft * 64 + 16 * w) * 1024, vb = lane * 16 + (4 * ct * 64 + 16 * w) * 1024;
      auto load = [&](int k) {
#pragma unroll
        for (int j = 0; j < 4; j++) fb[k % DEP][j] = bload1(rht, vb, ((w + j) & 3) * 65536 + k * 1024);
#pragma unroll
        for (int i = 0; i < 3; i++) fa[k % DEP][i] = bload1(rw1t, va + i * 65536, k * 1024);
      };
      f32x4 acc[3][4];
#pragma unroll
      for (int i = 0; i < 3; i++)
#pragma unroll
        for (int j = 0; j < 4; j++) acc[i][j] = z4;
#pragma unroll
      for (int k = 0; k < DEP; k++) load(k);
      SCHED();
#pragma unroll
      for (int k = 0; k < 16; k++) {
#pragma unroll
        for (int i = 0; i < 3; i++)
#pragma unroll
          for (int j = 0; j < 4; j++) acc[i][j] = mfma32(as_bf(fa[k % DEP][i]), as_bf(fb[k % DEP][j]), acc[i][j]);
        if (k + DEP < 16) load(k + DEP);
        if (k + DEP == 15) lds_arrive(L0 + SY + Y_ISS, lane);
        SCHED();
      }
      ST(4);
      lds_wait(L0 + SY + Y_QS, 4);
#pragma unroll
      for (int j = 1; j < 4; j++)
#pragma unroll
        for (int i = 0; i < 3; i++) *(LAS f32x4*)(uintptr_t)(L0 + E2SCR + ((((w + j) & 3) * 3 + (j - 1)) * 3 + i) * 1024 + lane * 16) = acc[i][j];
      lds_arrive(L0 + SY + Y_E2, lane);
      lds_wait(L0 + SY + Y_E2, 4);
      ST(5);
      u32x2 gp[3];
#pragma unroll
      for (int i = 0; i < 3; i++) {
        f32x4 u = acc[i][0];
#pragma unroll
        for (int j = 0; j < 3; j++) u += lds_f4(L0 + E2SCR + ((w * 3 + j) * 3 + i) * 1024 + lane * 16);
        float o[4];
        const float bv[4] = {bflo(bb[i][0]), bfhi(bb[i][0]), bflo(bb[i][1]), bfhi(bb[i][1])};
#pragma unroll
        for (int r = 0; r < 4; r++) {
          const float t = u[r] + bv[r];
          const float z = t * (1.f + 0.044715f * t * t) * (-2.f * 0.7978845608028654f * 1.4426950408889634f);
          o[r] = t * __builtin_amdgcn_rcpf(1.f + __builtin_amdgcn_exp2f(z));
        }
        gp[i][0] = pk2(o[0], o[1]); gp[i][1] = pk2(o[2], o[3]);
      }
      const rsrc_t rgg = mk_rsrc(ws + WS_G, G_BYTES);
      const auto r0 = __builtin_amdgcn_permlane16_swap(gp[0][0], gp[1][0], false, false);
      const auto r1 = __builtin_amdgcn_permlane16_swap(gp[0][1], gp[1][1], false, false);
      const u32x4 v = {r0[0], r1[0], r0[1], r1[1]};
      const int f8 = 6 * ft + 2 * (g & 1) + (g >> 1);
      __builtin_amdgcn_raw_buffer_store_b128(v, rgg, f8 * 256 + c * 16, (4 * ct + w) * 43 * 1024, SC1);
      const auto r2 = __builtin_amdgcn_permlane16_swap(gp[2][0], gp[2][0], false, false);
      const auto r3 = __builtin_amdgcn_permlane16_swap(gp[2][1], gp[2][1], false, false);
      const u32x4 v2 = {r2[0], r3[0], r2[1], r3[1]};
      const int f8b = 6 * ft + 4 + (g >> 1);
      __builtin_amdgcn_raw_buffer_store_b128(v2, rgg, ((g & 1) == 0 && f8b < 172) ? f8b * 256 + c * 16 : 0x80000000u, (4 * ct + w) * 43 * 1024, SC1);
      ST(6);
      publish(L0 + SY + Y_F3, 4, ws + WS_F3 + (ct * 32 + ft) * 128, tag, lane);
      if (blockIdx.x == 0 && w == 0 && lane == 0) __hip_atomic_store((GAS u64*)(ws + WS_EPOCH), tag, __ATOMIC_RELAXED, __HIP_MEMORY_SCOPE_AGENT);
      ST(7);
    } else {
      lds_arrive(L0 + SY + Y_ISS, lane);
    }
    BAR();
    BAR();
  }
  STFLUSH();
}

extern "C" void kernel_launch(void* const* d_in, const int* in_sizes, int n_in, void* d_out, int out_size, void* d_ws, size_t ws_size, hipStream_t stream) {
  (void)hipFuncSetAttribute((const void*)fused, hipFuncAttributeMaxDynamicSharedMemorySize, LDS_BYTES);
  fused<<<256, 512, LDS_BYTES, stream>>>((const u16*)d_in[0], (const u16*)d_in[1], (const u16*)d_in[2], (const u16*)d_in[4], (const u16*)d_in[5],
                                         (const u16*)d_in[6], (const u16*)d_in[7], (char*)d_ws, (u16*)d_out);
}
```

```cpp
#define PROBE_W 0
#define PROBE_I 15
#include <hip/hip_runtime.h>
#include <stdint.h>

typedef unsigned short u16;
typedef unsigned long long u64;
typedef short s16x4 __attribute__((ext_vector_type(4)));
typedef __bf16 bf16x2 __attribute__((ext_vector_type(2)));
typedef __bf16 bf16x4 __attribute__((ext_vector_type(4)));
typedef __bf16 bf16x8 __attribute__((ext_vector_type(8)));
typedef float f32x2 __attribute__((ext_vector_type(2)));
typedef float f32x4 __attribute__((ext_vector_type(4)));
typedef unsigned u32x2 __attribute__((ext_vector_type(2)));
typedef unsigned u32x4 __attribute__((ext_vector_type(4)));
#define LAS __attribute__((address_space(3)))
#define GAS __attribute__((address_space(1)))
#define DI __device__ __forceinline__

constexpr int S = 2048, D = 512, F = 1376;
constexpr float EPS = 1e-5f;
constexpr unsigned HT_BYTES = 32u * 64u * 1024u;
constexpr unsigned W1T_BYTES = 86u * 64u * 1024u;
constexpr unsigned G_BYTES = 32u * 43u * 1024u;
constexpr unsigned O_BYTES = 128u * 16u * 1024u;
constexpr size_t WS_EPOCH = 0;
constexpr size_t WS_F1 = 4096;
constexpr size_t WS_F2 = WS_F1 + 256 * 32;
constexpr size_t WS_F3 = WS_F2 + 256 * 128;
constexpr size_t WS_HT = 131072;
constexpr size_t WS_W1T = WS_HT + HT_BYTES;
constexpr size_t WS_G = WS_W1T + W1T_BYTES;
constexpr size_t WS_O = WS_G + G_BYTES;
constexpr int SC1 = 16;

typedef __amdgpu_buffer_rsrc_t rsrc_t;
DI rsrc_t mk_rsrc(const void* p, unsigned bytes) { return __builtin_amdgcn_make_buffer_rsrc((void*)p, 0, bytes, 0x00020000); }
DI u32x4 mk_srd(const void* p, unsigned bytes) {
  const uint64_t a = (uint64_t)p;
  u32x4 r = {(unsigned)a, (unsigned)(a >> 32) & 0xffffu, bytes, 0x00020000u};
  return r;
}
DI void glds16(u32x4 srd, unsigned voff, unsigned soff, unsigned lds) {
  asm volatile("s_mov_b32 m0, %3\n\ts_nop 0\n\tbuffer_load_dwordx4 %0, %1, %2 offen lds" ::"v"(voff), "s"(srd), "s"(soff), "s"(lds) : "memory");
}
#define WAITV(n) asm volatile("s_waitcnt vmcnt(" #n ")" ::: "memory")
#define FENCE() asm volatile("" ::: "memory")
#define SCHED() __builtin_amdgcn_sched_barrier(0)
#define BAR() do { asm volatile("s_waitcnt lgkmcnt(0)" ::: "memory"); __builtin_amdgcn_s_barrier(); FENCE(); } while (0)

DI bf16x8 lds_b128(unsigned a) { return *(const LAS bf16x8*)(uintptr_t)a; }
DI f32x4 lds_f4(unsigned a) { return *(const LAS f32x4*)(uintptr_t)a; }
DI bf16x4 lds_tr(unsigned a) { return __builtin_amdgcn_ds_read_tr16_b64_v4bf16((LAS bf16x4*)(uintptr_t)a); }
DI bf16x8 cat8(bf16x4 a, bf16x4 b) { return __builtin_shufflevector(a, b, 0, 1, 2, 3, 4, 5, 6, 7); }
DI f32x4 mfma32(bf16x8 a, bf16x8 b, f32x4 c) { return __builtin_amdgcn_mfma_f32_16x16x32_bf16(a, b, c, 0, 0, 0); }
DI f32x4 mfma16(bf16x4 a, bf16x4 b, f32x4 c) { return __builtin_amdgcn_mfma_f32_16x16x16bf16_1k(__builtin_bit_cast(s16x4, a), __builtin_bit_cast(s16x4, b), c, 0, 0, 0); }
DI unsigned pk2(float a, float b) { f32x2 f = {a, b}; return __builtin_bit_cast(unsigned, __builtin_convertvector(f, bf16x2)); }
DI bf16x4 pk4(float a, float b, float c, float d) { u32x2 u = {pk2(a, b), pk2(c, d)}; return __builtin_bit_cast(bf16x4, u); }
DI float bflo(unsigned u) { return __uint_as_float(u << 16); }
DI float bfhi(unsigned u) { return __uint_as_float(u & 0xffff0000u); }
DI float dot2(unsigned a, unsigned b, float c) { return __builtin_amdgcn_fdot2_f32_bf16(__builtin_bit_cast(bf16x2, a), __builtin_bit_cast(bf16x2, b), c, false); }
template <int CTRL> DI float dpp(float v) { return __uint_as_float(__builtin_amdgcn_update_dpp(0u, __float_as_uint(v), CTRL, 0xf, 0xf, true)); }
DI float sum8(float v) { v += dpp<0xB1>(v); v += dpp<0x4E>(v); v += dpp<0x141>(v); return v; }
DI float sum16(float v) { v = sum8(v); v += dpp<0x140>(v); return v; }
DI float xsum16(float v) { auto r = __builtin_amdgcn_permlane16_swap(__float_as_uint(v), __float_as_uint(v), false, false); return __uint_as_float(r[0]) + __uint_as_float(r[1]); }
DI float xsum32(float v) { auto r = __builtin_amdgcn_permlane32_swap(__float_as_uint(v), __float_as_uint(v), false, false); return __uint_as_float(r[0]) + __uint_as_float(r[1]); }
DI float xmax16(float v) { auto r = __builtin_amdgcn_permlane16_swap(__float_as_uint(v), __float_as_uint(v), false, false); return fmaxf(__uint_as_float(r[0]), __uint_as_float(r[1])); }
DI float xmax32(float v) { auto r = __builtin_amdgcn_permlane32_swap(__float_as_uint(v), __float_as_uint(v), false, false); return fmaxf(__uint_as_float(r[0]), __uint_as_float(r[1])); }
DI float sum64(float v) { return xsum32(xsum16(sum16(v))); }
DI u32x4 bload(rsrc_t r, unsigned v, unsigned s) { return __builtin_amdgcn_raw_buffer_load_b128(r, v, s, 0); }
DI u32x4 bload1(rsrc_t r, unsigned v, unsigned s) { return __builtin_amdgcn_raw_buffer_load_b128(r, v, s, SC1); }
DI bf16x8 as_bf(u32x4 v) { return __builtin_bit_cast(bf16x8, v); }

constexpr unsigned LDS_BYTES = 163840;
constexpr unsigned QST = 34816;
constexpr unsigned QPRE = 3 * QST;
constexpr unsigned QPRE_X = QPRE + 22528;
constexpr unsigned P2STG = 0;
constexpr unsigned P2TMP = 114688;
constexpr unsigned E2SCR = 114688;
constexpr unsigned QSTAT = LDS_BYTES - 1024;
constexpr unsigned SY = QSTAT + 640;
enum { Y_FULL = 0, Y_FREE = 16, Y_H = 32, Y_W1 = 36, Y_PRE = 40, Y_F1 = 44, Y_QS = 48, Y_F2 = 52, Y_ISS = 56, Y_E2 = 60, Y_F3 = 64, Y_TAGOK = 68, Y_TAG = 72 };
constexpr unsigned SPIN_LDS = 1u << 22, SPIN_GLB = 1u << 18;

DI unsigned lds_ld(unsigned a) { return __hip_atomic_load((LAS unsigned*)(uintptr_t)a, __ATOMIC_RELAXED, __HIP_MEMORY_SCOPE_WORKGROUP); }
DI void lds_st(unsigned a, unsigned v) { __hip_atomic_store((LAS unsigned*)(uintptr_t)a, v, __ATOMIC_RELAXED, __HIP_MEMORY_SCOPE_WORKGROUP); }
DI void lds_arrive(unsigned a, int lane) { FENCE(); if (lane == 0) __hip_atomic_fetch_add((LAS unsigned*)(uintptr_t)a, 1u, __ATOMIC_RELAXED, __HIP_MEMORY_SCOPE_WORKGROUP); FENCE(); }
DI void lds_wait(unsigned a, unsigned target) {
  FENCE();
#pragma unroll 1
  for (unsigned n = 0; n < SPIN_LDS; n++) { if ((unsigned)__builtin_amdgcn_readfirstlane(lds_ld(a)) >= target) break; __builtin_amdgcn_s_sleep(1); }
  FENCE();
}
DI void publish(unsigned cnt, unsigned nwaves, char* flag, u64 tag, int lane) {
  WAITV(0);
  if (lane == 0) {
    const unsigned old = __hip_atomic_fetch_add((LAS unsigned*)(uintptr_t)cnt, 1u, __ATOMIC_RELAXED, __HIP_MEMORY_SCOPE_WORKGROUP);
    if (old == nwaves - 1) __hip_atomic_store((GAS u64*)flag, tag, __ATOMIC_RELAXED, __HIP_MEMORY_SCOPE_AGENT);
  }
  FENCE();
}
DI void poll(const char* flag, u64 tag) {
  FENCE();
#pragma unroll 1
  for (unsigned n = 0; n < SPIN_GLB; n++) {
    const u64 v = __hip_atomic_load((const GAS u64*)flag, __ATOMIC_RELAXED, __HIP_MEMORY_SCOPE_AGENT);
    if (__all(v == tag)) break;
    __builtin_amdgcn_s_sleep(1);
  }
  FENCE();
}
DI u64 get_tag(unsigned L0) {
  lds_wait(L0 + SY + Y_TAGOK, 1);
  const unsigned lo = __builtin_amdgcn_readfirstlane(lds_ld(L0 + SY + Y_TAG)), hi = __builtin_amdgcn_readfirstlane(lds_ld(L0 + SY + Y_TAG + 4));
  return ((u64)hi << 32) | lo;
}

#define ST(i) do { if ((i) == 0 || (i) == PROBE_I) { SCHED(); u64 t_; asm volatile("s_memrealtime %0\n\ts_waitcnt lgkmcnt(0)" : "=s"(t_)::"memory"); stv[(i) != 0] = t_; SCHED(); } } while (0)
#define STDECL u64 stv[2] = {0, 0};
#define STFLUSH() do { if (W == PROBE_W) { const u64 dt_ = stv[1] > stv[0] ? stv[1] - stv[0] : 0; const u64 until_ = stv[0] + 10000 + 20 * dt_; while (__builtin_amdgcn_s_memrealtime() < until_) __builtin_amdgcn_s_sleep(2); } } while (0)

__global__ __launch_bounds__(512, 2) void fused(const u16* __restrict__ x, const u16* __restrict__ wqkv, const u16* __restrict__ wo, const u16* __restrict__ w1,
                                                const u16* __restrict__ b1, const u16* __restrict__ w2, const u16* __restrict__ b2, char* __restrict__ ws,
                                                u16* __restrict__ out) {
  extern __shared__ __attribute__((aligned(16))) char smem[];
  const unsigned L0 = (unsigned)(uintptr_t)(LAS char*)smem;
  const int tid = threadIdx.x, lane = tid & 63;
  const int W = __builtin_amdgcn_readfirstlane(tid >> 6), w = W & 3;
  const int xcd = blockIdx.x & 7, idx = blockIdx.x >> 3;
  const int sq = xcd >> 1, hh = xcd & 1;
  const int st = sq * 8 + (idx >> 2), ct = hh * 4 + (idx & 3);
  const int rg = sq * 64 + hh * 32 + idx;
  const int c = lane & 15, g = lane >> 4, r8 = lane >> 3, pc = lane & 7;
  const f32x4 z4 = {0.f, 0.f, 0.f, 0.f};
  STDECL ST(0);
  if (tid < 24) lds_st(L0 + SY + tid * 4, 0u);

  if (W < 4) {
    const rsrc_t rx = mk_rsrc(x, S * D * 2);
    u32x4 xr[2];
#pragma unroll
    for (int rr = 0; rr < 2; rr++) xr[rr] = bload(rx, lane * 16, (rg * 8 + 2 * w + rr) * 1024);
    u64 ep = 0;
    if (w == 0) ep = __hip_atomic_load((const GAS u64*)(ws + WS_EPOCH), __ATOMIC_RELAXED, __HIP_MEMORY_SCOPE_AGENT);
    BAR();
#pragma unroll
    for (int rr = 0; rr < 2; rr++) {
      float v[8];
#pragma unroll
      for (int j = 0; j < 4; j++) { v[2 * j] = bflo(xr[rr][j]); v[2 * j + 1] = bfhi(xr[rr][j]); }
      float s = 0;
#pragma unroll
      for (int j = 0; j < 8; j++) s += v[j];
      const float m = sum64(s) * (1.f / D);
      float q = 0;
#pragma unroll
      for (int j = 0; j < 8; j++) { v[j] -= m; q += v[j] * v[j]; }
      const float r = __builtin_amdgcn_rsqf(sum64(q) * (1.f / D) + EPS);
      float s2 = 0;
#pragma unroll
      for (int j = 0; j < 8; j++) { v[j] *= r; s2 += v[j]; }
      const float m2 = sum64(s2) * (1.f / D);
      float q2 = 0;
#pragma unroll
      for (int j = 0; j < 8; j++) { v[j] -= m2; q2 += v[j] * v[j]; }
      const float r2 = __builtin_amdgcn_rsqf(sum64(q2) * (1.f / D) + EPS);
      u32x4 o;
#pragma unroll
      for (int j = 0; j < 4; j++) o[j] = pk2(v[2 * j] * r2, v[2 * j + 1] * r2);
      *(LAS u32x4*)(uintptr_t)(L0 + QPRE_X + (2 * w + rr) * 1088 + lane * 16) = o;
    }
    lds_arrive(L0 + SY + Y_H, lane);
    if (w == 0 && lane == 0) { const u64 t = ep + 1; lds_st(L0 + SY + Y_TAG, (unsigned)t); lds_st(L0 + SY + Y_TAG + 4, (unsigned)(t >> 32)); FENCE(); lds_st(L0 + SY + Y_TAGOK, 1u); }
    ST(1);

    f32x4 aq[5], ak[5], av[5];
#pragma unroll
    for (int b = 0; b < 5; b++) { ak[b] = z4; av[b] = z4; aq[b] = z4; }
    float sa[3] = {0.f, 0.f, 0.f}, sb[3] = {0.f, 0.f, 0.f};
    const unsigned ax = c * 128 + ((g ^ (c >> 1)) << 4);
    const unsigned aw = 10240 + (8 * g + (c >> 2)) * 128 + ((w ^ ((c >> 3) | ((g & 1) << 1))) << 5) + (c & 3) * 8;
    const unsigned as2 = (w < 2 ? 8192 : 0) + tid * 16;
    bf16x8 xf[2][5], wf[2][3];
    auto rd = [&](int buf, unsigned b, int kk) {
#pragma unroll
      for (int sec = 0; sec < 3; sec++) wf[buf][sec] = cat8(lds_tr(b + aw + sec * 8192 + kk * 4096), lds_tr(b + aw + sec * 8192 + kk * 4096 + 512));
#pragma unroll
      for (int t = 0; t < 5; t++) xf[buf][t] = lds_b128(b + (ax ^ (kk * 64)) + t * 2048);
    };
    auto mm = [&](int buf) {
      u32x4 x0 = __builtin_bit_cast(u32x4, xf[buf][0]);
#pragma unroll
      for (int j = 0; j < 4; j++) x0[j] = c == 0 ? 0x3f803f80u : x0[j];
#pragma unroll
      for (int t = 4; t >= 0; t--) {
        const bf16x8 xx = t == 0 ? as_bf(x0) : xf[buf][t];
        ak[t] = mfma32(wf[buf][1], xx, ak[t]);
        av[t] = mfma32(xx, wf[buf][2], av[t]);
        aq[t] = mfma32(wf[buf][0], xx, aq[t]);
      }
    };
    auto wait_full = [&](unsigned n) {
      FENCE();
#pragma unroll 1
      for (unsigned i = 0; i < SPIN_LDS; i++) {
        const unsigned f0 = lds_ld(L0 + SY + Y_FULL), f1 = lds_ld(L0 + SY + Y_FULL + 4);
        if ((unsigned)__builtin_amdgcn_readfirstlane(min(f0, f1)) >= n) break;
        __builtin_amdgcn_s_sleep(1);
      }
      FENCE();
    };
    wait_full(1);
    ST(2);
    rd(0, L0, 0);
#pragma unroll 1
    for (int s = 0; s < 8; s++) {
      const unsigned b = L0 + (s & 3) * QST;
      rd(1, b, 1);
      u32x4 su[3];
      su[0] = *(const LAS u32x4*)(uintptr_t)(b + tid * 16);
      su[1] = *(const LAS u32x4*)(uintptr_t)(b + 4096 + tid * 16);
      su[2] = *(const LAS u32x4*)(uintptr_t)(b + as2);
      FENCE();
      if (lane == 0) lds_st(L0 + SY + Y_FREE + w * 4, s + 1);
      SCHED();
      mm(0);
      SCHED();
      if (s < 7) { wait_full(s + 2); rd(0, L0 + ((s + 1) & 3) * QST, 0); }
      SCHED();
      mm(1);
#pragma unroll
      for (int n = 0; n < 3; n++)
#pragma unroll
        for (int j = 0; j < 4; j++) { sa[n] = dot2(su[n][j], 0x3f803f80u, sa[n]); sb[n] = dot2(su[n][j], su[n][j], sb[n]); }
      SCHED();
    }
    ST(3);
#pragma unroll
    for (int n = 0; n < 3; n++) {
      const float m = sum8(sa[n]) * (1.f / D);
      const float var = fmaxf(sum8(sb[n]) * (1.f / D) - m * m, 0.f);
      const float r = __builtin_amdgcn_rsqf(var + EPS);
      if (pc == 0 && (n < 2 || w < 2)) { f32x2 o = {r, m * r}; *(LAS f32x2*)(uintptr_t)(L0 + QSTAT + (n * 32 + (tid >> 3)) * 8) = o; }
    }
    lds_arrive(L0 + SY + Y_QS, lane);
    float cq[4], ck[4];
#pragma unroll
    for (int j = 0; j < 4; j++) { cq[j] = dpp<0x150>(aq[0][j]); ck[j] = dpp<0x150>(ak[0][j]); }
    const float cv = xsum32(xsum16(g == 0 ? av[0][0] : 0.f));
    lds_wait(L0 + SY + Y_QS, 4);
    bf16x4 qf[4], kf[5], vf[5];
#pragma unroll
    for (int t = 0; t < 5; t++) {
      const f32x2 sc = *(const LAS f32x2*)(uintptr_t)(L0 + QSTAT + (16 * t + c) * 8);
      kf[t] = pk4(sc[0] * ak[t][0] - sc[1] * ck[0], sc[0] * ak[t][1] - sc[1] * ck[1], sc[0] * ak[t][2] - sc[1] * ck[2], sc[0] * ak[t][3] - sc[1] * ck[3]);
      if (t > 0) qf[t - 1] = pk4(sc[0] * aq[t][0] - sc[1] * cq[0], sc[0] * aq[t][1] - sc[1] * cq[1], sc[0] * aq[t][2] - sc[1] * cq[2], sc[0] * aq[t][3] - sc[1] * cq[3]);
      const f32x4 s0 = lds_f4(L0 + QSTAT + (16 * t + 4 * g) * 8), s1v = lds_f4(L0 + QSTAT + (16 * t + 4 * g) * 8 + 16);
      vf[t] = pk4(s0[0] * av[t][0] - s0[1] * cv, s0[2] * av[t][1] - s0[3] * cv, s1v[0] * av[t][2] - s1v[1] * cv, s1v[2] * av[t][3] - s1v[3] * cv);
    }
    u32x2 op[4];
    const bool odd = c & 1;
    const int k0 = 4 * g + (c & 1), k1 = k0 + 2;
    const bool cur0 = k0 <= c, cur1 = k1 <= c;
    constexpr float SC = 0.25f * 1.4426950408889634f;
#pragma unroll
    for (int t = 1; t < 5; t++) {
      const f32x4 sp = mfma16(kf[t - 1], qf[t - 1], z4), scu = mfma16(kf[t], qf[t - 1], z4);
      const float p0 = odd ? sp[1] : sp[0], p1 = odd ? sp[3] : sp[2];
      const float c0 = odd ? scu[1] : scu[0], c1 = odd ? scu[3] : scu[2];
      float l0 = (cur0 ? c0 : p0) * SC, l1 = (cur1 ? c1 : p1) * SC;
      if (t == 1 && st == 0) { l0 = cur0 ? l0 : -1e9f; l1 = cur1 ? l1 : -1e9f; }
      const float mx = xmax32(xmax16(fmaxf(l0, l1)));
      const float e0 = __builtin_amdgcn_exp2f(l0 - mx), e1 = __builtin_amdgcn_exp2f(l1 - mx);
      const float inv = __builtin_amdgcn_rcpf(xsum32(xsum16(e0 + e1)));
      const unsigned sh = odd ? 16 : 0;
      const unsigned b0 = (pk2(e0 * inv, 0.f) & 0xffffu) << sh, b1v = (pk2(e1 * inv, 0.f) & 0xffffu) << sh;
      const u32x4 pu = {cur0 ? 0u : b0, cur1 ? 0u : b1v, cur0 ? b0 : 0u, cur1 ? b1v : 0u};
      const f32x4 o = mfma32(cat8(vf[t - 1], vf[t]), as_bf(pu), z4);
      op[t - 1][0] = pk2(o[0], o[1]); op[t - 1][1] = pk2(o[2], o[3]);
    }
    const rsrc_t ro = mk_rsrc(ws + WS_O, O_BYTES);
#pragma unroll
    for (int t = 0; t < 4; t += 2) {
      const auto r0 = __builtin_amdgcn_permlane16_swap(op[t][0], op[t + 1][0], false, false);
      const auto r1 = __builtin_amdgcn_permlane16_swap(op[t][1], op[t + 1][1], false, false);
      const u32x4 v = {r0[0], r1[0], r0[1], r1[1]};
      const unsigned vo = (g & 1) * 16384 + ((g >> 1) * 16 + c) * 16;
      __builtin_amdgcn_raw_buffer_store_b128(v, ro, vo, ((4 * st + t) * 16 + 2 * ct + (w >> 1)) * 1024 + (w & 1) * 512, SC1);
    }
    const u64 tag = get_tag(L0);
    publish(L0 + SY + Y_F2, 4, ws + WS_F2 + (st * 8 + ct) * 128, tag, lane);
    ST(4);

    const rsrc_t rgg = mk_rsrc(ws + WS_G, G_BYTES), rwo = mk_rsrc(wo, D * D * 2), rw2 = mk_rsrc(w2, F * S * 2);
    lds_wait(L0 + SY + Y_ISS, 4);
    poll(ws + WS_F2 + (st * 8 + (lane & 7)) * 128, tag);
    ST(5);
    f32x4 acc[4][4];
#pragma unroll
    for (int i = 0; i < 4; i++)
#pragma unroll
      for (int j = 0; j < 4; j++) acc[i][j] = z4;
    const unsigned vo = lane * 16 + (4 * st * 16 + 4 * w) * 1024;
    const unsigned vg = lane * 16 + (4 * ct * 43 + 11 * w) * 1024;
    const unsigned vwo = (128 * w + r8) * 1024 + ct * 128 + pc * 16;
    const unsigned vw2 = (352 * w + r8) * 4096 + st * 128 + pc * 16;
    const unsigned lw = L0 + P2STG + w * 28672;
    const unsigned aw0 = lw + r8 * 128 + ((((pc >> 1) ^ ((r8 >> 1) & 1)) << 5) | ((pc & 1) << 4));
    const unsigned ar = lw + (8 * g + (c >> 2)) * 128 + ((((c >> 3) | ((g & 1) << 1))) << 5) + (c & 3) * 8;
    u32x4 ow[4][4], of[4][4], wr[11][4], gf[11][4];
    auto LO = [&](int k) {
#pragma unroll
      for (int j = 0; j < 4; j++) ow[k][j] = bload(rwo, vwo, (32 * k + 8 * j) * 1024);
#pragma unroll
      for (int j = 0; j < 4; j++) of[k][j] = bload1(ro, vo, ((w + j) & 3) * 16384 + k * 1024);
    };
    auto LW = [&](int k) {
#pragma unroll
      for (int j = 0; j < 4; j++) wr[k][j] = bload(rw2, vw2 + (32 * k + 8 * j) * 4096, 0);
    };
    auto LG = [&](int k) {
      const unsigned vgk = (k == 10 && w == 3) ? 0x80000000u : vg;
#pragma unroll
      for (int j = 0; j < 4; j++) gf[k][j] = bload1(rgg, vgk, j * 43 * 1024 + k * 1024);
    };
    auto SW = [&](const u32x4 (&v)[4], int slot) {
#pragma unroll
      for (int j = 0; j < 4; j++) *(LAS u32x4*)(uintptr_t)((aw0 ^ ((j & 1) << 6)) + j * 1024 + slot * 4096) = v[j];
    };
    auto CO = [&](int k) {
      SW(ow[k], 5 + (k & 1));
      bf16x8 tw[4];
#pragma unroll
      for (int e = 0; e < 4; e++) tw[e] = cat8(lds_tr((ar ^ (e << 5)) + (5 + (k & 1)) * 4096), lds_tr((ar ^ (e << 5)) + 512 + (5 + (k & 1)) * 4096));
#pragma unroll
      for (int i = 0; i < 4; i++)
#pragma unroll
        for (int j = 0; j < 4; j++) acc[i][j] = mfma32(tw[i], as_bf(of[k][j]), acc[i][j]);
    };
    unsigned arr[4];
#pragma unroll
    for (int j = 0; j < 4; j++) arr[j] = ar ^ (((w + j) & 3) << 5);
    auto TR = [&](bf16x8 (&t)[4], int slot) {
#pragma unroll
      for (int j = 0; j < 4; j++) t[j] = cat8(lds_tr(arr[j] + slot * 4096), lds_tr(arr[j] + 512 + slot * 4096));
    };
    LO(0); LO(1); LO(2); SCHED();
    CO(0); SCHED(); LO(3); SCHED();
    CO(1); SCHED(); LW(7); LW(8); SCHED();
    CO(2); SCHED(); LW(9); LW(10); SCHED();
    CO(3); SCHED(); LW(0); LW(1); SCHED();
    bf16x8 tk[4][4];
    SW(wr[7], 5); TR(tk[0], 5); SW(wr[8], 6); TR(tk[1], 6); SCHED(); LW(2); LW(3); SCHED();
    SW(wr[9], 5); TR(tk[2], 5); SW(wr[10], 6); TR(tk[3], 6); SCHED(); LW(4); LW(5); SCHED();
    SW(wr[0], 0); SW(wr[1], 1); SCHED(); LW(6);
    const int srow = 64 * st + 16 * w + c;
    u32x4 xv[2];
#pragma unroll
    for (int i = 0; i < 2; i++) xv[i] = bload(rx, (srow * D + 64 * ct + 32 * i + 16 * (g & 1) + 8 * (g >> 1)) * 2, 0);
    const float bias = bflo(b2[srow]);
    SCHED();
    SW(wr[2], 2); SW(wr[3], 3); SW(wr[4], 4); SW(wr[5], 5); SW(wr[6], 6);
    ST(6);
    {
      const int f0 = (352 * w) / 48 + (lane & 15);
      poll(ws + WS_F3 + (ct * 32 + min(f0, min(28, (352 * w + 351) / 48))) * 128, tag);
    }
    ST(7);
    LG(0); LG(1); LG(2); LG(3);
    bf16x8 tw[2][4];
    TR(tw[0], 0);
    SCHED();
#pragma unroll
    for (int k = 0; k < 11; k++) {
      if (k + 1 < 7) TR(tw[(k + 1) & 1], k + 1);
#pragma unroll
      for (int i = 0; i < 4; i++)
#pragma unroll
        for (int j = 0; j < 4; j++) acc[i][j] = mfma32(as_bf(gf[k][i]), k < 7 ? tw[k & 1][j] : tk[k < 7 ? 0 : k - 7][j], acc[i][j]);
      if (k + 4 < 11) LG(k + 4);
      SCHED();
    }
    ST(8);
    BAR();
#pragma unroll
    for (int j = 1; j < 4; j++)
#pragma unroll
      for (int i = 0; i < 4; i++) *(LAS f32x4*)(uintptr_t)(L0 + ((((w + j) & 3) * 3 + (j - 1)) * 4 + i) * 1024 + lane * 16) = acc[i][j];
    BAR();
    ST(9);
    f32x4 u[4];
#pragma unroll
    for (int i = 0; i < 4; i++) {
      u[i] = acc[i][0] + bias;
#pragma unroll
      for (int j = 0; j < 3; j++) u[i] += lds_f4(L0 + ((w * 3 + j) * 4 + i) * 1024 + lane * 16);
    }
#pragma unroll
    for (int i = 0; i < 4; i += 2) {
      float v[8];
#pragma unroll
      for (int r = 0; r < 4; r++) {
        const auto sw = __builtin_amdgcn_permlane16_swap(__float_as_uint(u[i][r]), __float_as_uint(u[i + 1][r]), false, false);
        v[r] = __uint_as_float(sw[0]); v[4 + r] = __uint_as_float(sw[1]);
      }
      u32x4 o;
#pragma unroll
      for (int j = 0; j < 4; j++) o[j] = pk2(v[2 * j] + bflo(xv[i >> 1][j]), v[2 * j + 1] + bfhi(xv[i >> 1][j]));
      *(u32x4*)(out + srow * D + 64 * ct + 16 * i + 16 * (g & 1) + 8 * (g >> 1)) = o;
    }
    ST(10);
  } else {
    if (w < 2) {
      const u32x4 sx = mk_srd(x, S * D * 2), sw = mk_srd(wqkv, D * 3 * D * 2);
      const unsigned vx0 = (unsigned)((64 * st - 16 + 8 * w + r8) * 1024 + ((pc ^ ((w << 2) | (r8 >> 1))) << 4));
      const unsigned vx1 = vx0 + 16384u;
      const unsigned vw = (8 * w + r8) * 3072 + ct * 128 + ((((pc >> 1) ^ (((r8 >> 1) & 1) | (w << 1))) << 5) | ((pc & 1) << 4));
      auto half0 = [&](int s) {
        const unsigned b = L0 + (s & 3) * QST + w * 1024;
        glds16(sx, vx0, s * 128, b);
#pragma unroll
        for (int j = 0; j < 4; j++) glds16(sx, vx1, s * 128 + j * 16384, b + 2048 + j * 2048);
#pragma unroll
        for (int sec = 0; sec < 3; sec++) glds16(sw, vw, s * 64 * 3072 + sec * 1024, b + 10240 + sec * 8192);
      };
      auto half1 = [&](int s) {
        const unsigned b = L0 + (s & 3) * QST + w * 1024;
#pragma unroll
        for (int j = 1; j < 4; j++)
#pragma unroll
          for (int sec = 0; sec < 3; sec++) glds16(sw, vw, s * 64 * 3072 + j * 16 * 3072 + sec * 1024, b + 10240 + sec * 8192 + j * 2048);
      };
      half0(0);
      BAR();
      half1(0);
#pragma unroll 1
      for (int s = 1; s < 8; s++) {
        if (s == 3) lds_wait(L0 + SY + Y_PRE, 2);
        if (s >= 4) {
          FENCE();
#pragma unroll 1
          for (unsigned i = 0; i < SPIN_LDS; i++) {
            const unsigned f = min(min(lds_ld(L0 + SY + Y_FREE), lds_ld(L0 + SY + Y_FREE + 4)), min(lds_ld(L0 + SY + Y_FREE + 8), lds_ld(L0 + SY + Y_FREE + 12)));
            if ((unsigned)__builtin_amdgcn_readfirstlane(f) >= (unsigned)(s - 3)) break;
            __builtin_amdgcn_s_sleep(1);
          }
          FENCE();
        }
        half0(s);
        if (s >= 2) { WAITV(25); if (lane == 0) lds_st(L0 + SY + Y_FULL + w * 4, s - 1); FENCE(); }
        half1(s);
        if (s == 2) { WAITV(17); if (lane == 0) lds_st(L0 + SY + Y_FULL + w * 4, 2); FENCE(); }
      }
      ST(1);
      WAITV(17); if (lane == 0) lds_st(L0 + SY + Y_FULL + w * 4, 7);
      WAITV(0); if (lane == 0) lds_st(L0 + SY + Y_FULL + w * 4, 8);
      FENCE();
    } else {
      const u32x4 s1 = mk_srd(w1, S * F * 2);
      const int v = w - 2;
#pragma unroll
      for (int n = 0; n < 11; n++) {
        const unsigned o = (v + 2 * n) * 1024 + lane * 16;
        glds16(s1, (n < 10 || o < 22016u) ? o : 0x80000000u, rg * 22016, L0 + QPRE + (v + 2 * n) * 1024);
      }
      BAR();
      WAITV(0);
      lds_arrive(L0 + SY + Y_W1, lane);
      const rsrc_t rht = mk_rsrc(ws + WS_HT, HT_BYTES), rw1t = mk_rsrc(ws + WS_W1T, W1T_BYTES);
      const int q4 = c >> 2, p4 = c & 3;
      const unsigned vo = (4 * v + g) * 65536 + (rg >> 2) * 1024 + ((rg & 3) * 16 + c) * 16;
      lds_wait(L0 + SY + Y_W1, 2);
      bf16x8 tv[15];
#pragma unroll
      for (int n = 0; n < 11; n++) {
        const unsigned a = L0 + QPRE + q4 * 2752 + ((v + 2 * n) * 4 + g) * 32 + p4 * 8;
        tv[n] = cat8(lds_tr(a), lds_tr(a + 4 * 2752));
      }
      lds_wait(L0 + SY + Y_H, 4);
#pragma unroll
      for (int n = 0; n < 4; n++) {
        const unsigned a = L0 + QPRE_X + q4 * 1088 + ((v + 2 * n) * 4 + g) * 32 + p4 * 8;
        tv[11 + n] = cat8(lds_tr(a), lds_tr(a + 4 * 1088));
      }
      lds_arrive(L0 + SY + Y_PRE, lane);
#pragma unroll
      for (int n = 0; n < 11; n++) __builtin_amdgcn_raw_buffer_store_b128(__builtin_bit_cast(u32x4, tv[n]), rw1t, vo + n * 8 * 65536, 0, SC1);
#pragma unroll
      for (int n = 0; n < 4; n++) __builtin_amdgcn_raw_buffer_store_b128(__builtin_bit_cast(u32x4, tv[11 + n]), rht, vo, n * 8 * 65536, SC1);
      ST(1);
      publish(L0 + SY + Y_F1, 2, ws + WS_F1 + rg * 32, get_tag(L0), lane);
    }
    ST(2);
    const int fl = idx >> 2;
    if (sq == 0 || fl < 7) {
      const u64 tag = get_tag(L0);
      const int ft = (sq == 0 ? 0 : 7 * sq + 1) + fl;
      const rsrc_t rht = mk_rsrc(ws + WS_HT, HT_BYTES), rw1t = mk_rsrc(ws + WS_W1T, W1T_BYTES);
      u32x2 bb[3];
#pragma unroll
      for (int i = 0; i < 3; i++) bb[i] = *(const u32x2*)(b1 + min(48 * ft + 16 * i + 4 * g, F - 4));
      poll(ws + WS_F1 + (64 * w + lane) * 32, tag);
      ST(3);
      constexpr int DEP = 4;
      u32x4 fa[DEP][3], fb[DEP][4];
      const unsigned va = lane * 16 + (3 * ft * 64 + 16 * w) * 1024, vb = lane * 16 + (4 * ct * 64 + 16 * w) * 1024;
      auto load = [&](int k) {
#pragma unroll
        for (int j = 0; j < 4; j++) fb[k % DEP][j] = bload1(rht, vb, ((w + j) & 3) * 65536 + k * 1024);
#pragma unroll
        for (int i = 0; i < 3; i++) fa[k % DEP][i] = bload1(rw1t, va + i * 65536, k * 1024);
      };
      f32x4 acc[3][4];
#pragma unroll
      for (int i = 0; i < 3; i++)
#pragma unroll
        for (int j = 0; j < 4; j++) acc[i][j] = z4;
#pragma unroll
      for (int k = 0; k < DEP; k++) load(k);
      SCHED();
#pragma unroll
      for (int k = 0; k < 16; k++) {
#pragma unroll
        for (int i = 0; i < 3; i++)
#pragma unroll
          for (int j = 0; j < 4; j++) acc[i][j] = mfma32(as_bf(fa[k % DEP][i]), as_bf(fb[k % DEP][j]), acc[i][j]);
        if (k + DEP < 16) load(k + DEP);
        if (k + DEP == 15) lds_arrive(L0 + SY + Y_ISS, lane);
        SCHED();
      }
      ST(4);
      lds_wait(L0 + SY + Y_QS, 4);
#pragma unroll
      for (int j = 1; j < 4; j++)
#pragma unroll
        for (int i = 0; i < 3; i++) *(LAS f32x4*)(uintptr_t)(L0 + E2SCR + ((((w + j) & 3) * 3 + (j - 1)) * 3 + i) * 1024 + lane * 16) = acc[i][j];
      lds_arrive(L0 + SY + Y_E2, lane);
      lds_wait(L0 + SY + Y_E2, 4);
      ST(5);
      u32x2 gp[3];
#pragma unroll
      for (int i = 0; i < 3; i++) {
        f32x4 u = acc[i][0];
#pragma unroll
        for (int j = 0; j < 3; j++) u += lds_f4(L0 + E2SCR + ((w * 3 + j) * 3 + i) * 1024 + lane * 16);
        float o[4];
        const float bv[4] = {bflo(bb[i][0]), bfhi(bb[i][0]), bflo(bb[i][1]), bfhi(bb[i][1])};
#pragma unroll
        for (int r = 0; r < 4; r++) {
          const float t = u[r] + bv[r];
          const float z = t * (1.f + 0.044715f * t * t) * (-2.f * 0.7978845608028654f * 1.4426950408889634f);
          o[r] = t * __builtin_amdgcn_rcpf(1.f + __builtin_amdgcn_exp2f(z));
        }
        gp[i][0] = pk2(o[0], o[1]); gp[i][1] = pk2(o[2], o[3]);
      }
      const rsrc_t rgg = mk_rsrc(ws + WS_G, G_BYTES);
      const auto r0 = __builtin_amdgcn_permlane16_swap(gp[0][0], gp[1][0], false, false);
      const auto r1 = __builtin_amdgcn_permlane16_swap(gp[0][1], gp[1][1], false, false);
      const u32x4 v = {r0[0], r1[0], r0[1], r1[1]};
      const int f8 = 6 * ft + 2 * (g & 1) + (g >> 1);
      __builtin_amdgcn_raw_buffer_store_b128(v, rgg, f8 * 256 + c * 16, (4 * ct + w) * 43 * 1024, SC1);
      const auto r2 = __builtin_amdgcn_permlane16_swap(gp[2][0], gp[2][0], false, false);
      const auto r3 = __builtin_amdgcn_permlane16_swap(gp[2][1], gp[2][1], false, false);
      const u32x4 v2 = {r2[0], r3[0], r2[1], r3[1]};
      const int f8b = 6 * ft + 4 + (g >> 1);
      __builtin_amdgcn_raw_buffer_store_b128(v2, rgg, ((g & 1) == 0 && f8b < 172) ? f8b * 256 + c * 16 : 0x80000000u, (4 * ct + w) * 43 * 1024, SC1);
      ST(6);
      publish(L0 + SY + Y_F3, 4, ws + WS_F3 + (ct * 32 + ft) * 128, tag, lane);
      if (blockIdx.x == 0 && w == 0 && lane == 0) __hip_atomic_store((GAS u64*)(ws + WS_EPOCH), tag, __ATOMIC_RELAXED, __HIP_MEMORY_SCOPE_AGENT);
      ST(7);
    } else {
      lds_arrive(L0 + SY + Y_ISS, lane);
    }
    BAR();
    BAR();
  }
  STFLUSH();
}

extern "C" void kernel_launch(void* const* d_in, const int* in_sizes, int n_in, void* d_out, int out_size, void* d_ws, size_t ws_size, hipStream_t stream) {
  (void)hipFuncSetAttribute((const void*)fused, hipFuncAttributeMaxDynamicSharedMemorySize, LDS_BYTES);
  fused<<<256, 512, LDS_BYTES, stream>>>((const u16*)d_in[0], (const u16*)d_in[1], (const u16*)d_in[2], (const u16*)d_in[4], (const u16*)d_in[5],
                                         (const u16*)d_in[6], (const u16*)d_in[7], (char*)d_ws, (u16*)d_out);
}
```

```cpp
#define PBLOCKS 256
#define PTHREADS 512
#define PLDS 0
#define PNV 32
#include <hip/hip_runtime.h>
#include <stdint.h>

typedef unsigned short u16;
typedef unsigned long long u64;
typedef short s16x4 __attribute__((ext_vector_type(4)));
typedef __bf16 bf16x2 __attribute__((ext_vector_type(2)));
typedef __bf16 bf16x4 __attribute__((ext_vector_type(4)));
typedef __bf16 bf16x8 __attribute__((ext_vector_type(8)));
typedef float f32x2 __attribute__((ext_vector_type(2)));
typedef float f32x4 __attribute__((ext_vector_type(4)));
typedef unsigned u32x2 __attribute__((ext_vector_type(2)));
typedef unsigned u32x4 __attribute__((ext_vector_type(4)));
#define LAS __attribute__((address_space(3)))
#define GAS __attribute__((address_space(1)))
#define DI __device__ __forceinline__

constexpr int S = 2048, D = 512, F = 1376;
constexpr float EPS = 1e-5f;
constexpr unsigned HT_BYTES = 32u * 64u * 1024u;
constexpr unsigned W1T_BYTES = 86u * 64u * 1024u;
constexpr unsigned G_BYTES = 32u * 43u * 1024u;
constexpr unsigned O_BYTES = 128u * 16u * 1024u;
constexpr size_t WS_EPOCH = 0;
constexpr size_t WS_F1 = 4096;
constexpr size_t WS_F2 = WS_F1 + 256 * 32;
constexpr size_t WS_F3 = WS_F2 + 256 * 128;
constexpr size_t WS_HT = 131072;
constexpr size_t WS_W1T = WS_HT + HT_BYTES;
constexpr size_t WS_G = WS_W1T + W1T_BYTES;
constexpr size_t WS_O = WS_G + G_BYTES;
constexpr int SC1 = 16;

typedef __amdgpu_buffer_rsrc_t rsrc_t;
DI rsrc_t mk_rsrc(const void* p, unsigned bytes) { return __builtin_amdgcn_make_buffer_rsrc((void*)p, 0, bytes, 0x00020000); }
DI u32x4 mk_srd(const void* p, unsigned bytes) {
  const uint64_t a = (uint64_t)p;
  u32x4 r = {(unsigned)a, (unsigned)(a >> 32) & 0xffffu, bytes, 0x00020000u};
  return r;
}
DI void glds16(u32x4 srd, unsigned voff, unsigned soff, unsigned lds) {
  asm volatile("s_mov_b32 m0, %3\n\ts_nop 0\n\tbuffer_load_dwordx4 %0, %1, %2 offen lds" ::"v"(voff), "s"(srd), "s"(soff), "s"(lds) : "memory");
}
#define WAITV(n) asm volatile("s_waitcnt vmcnt(" #n ")" ::: "memory")
#define FENCE() asm volatile("" ::: "memory")
#define SCHED() __builtin_amdgcn_sched_barrier(0)
#define BAR() do { asm volatile("s_waitcnt lgkmcnt(0)" ::: "memory"); __builtin_amdgcn_s_barrier(); FENCE(); } while (0)

DI bf16x8 lds_b128(unsigned a) { return *(const LAS bf16x8*)(uintptr_t)a; }
DI f32x4 lds_f4(unsigned a) { return *(const LAS f32x4*)(uintptr_t)a; }
DI bf16x4 lds_tr(unsigned a) { return __builtin_amdgcn_ds_read_tr16_b64_v4bf16((LAS bf16x4*)(uintptr_t)a); }
DI bf16x8 cat8(bf16x4 a, bf16x4 b) { return __builtin_shufflevector(a, b, 0, 1, 2, 3, 4, 5, 6, 7); }
DI f32x4 mfma32(bf16x8 a, bf16x8 b, f32x4 c) { return __builtin_amdgcn_mfma_f32_16x16x32_bf16(a, b, c, 0, 0, 0); }
DI f32x4 mfma16(bf16x4 a, bf16x4 b, f32x4 c) { return __builtin_amdgcn_mfma_f32_16x16x16bf16_1k(__builtin_bit_cast(s16x4, a), __builtin_bit_cast(s16x4, b), c, 0, 0, 0); }
DI unsigned pk2(float a, float b) { f32x2 f = {a, b}; return __builtin_bit_cast(unsigned, __builtin_convertvector(f, bf16x2)); }
DI bf16x4 pk4(float a, float b, float c, float d) { u32x2 u = {pk2(a, b), pk2(c, d)}; return __builtin_bit_cast(bf16x4, u); }
DI float bflo(unsigned u) { return __uint_as_float(u << 16); }
DI float bfhi(unsigned u) { return __uint_as_float(u & 0xffff0000u); }
DI float dot2(unsigned a, unsigned b, float c) { return __builtin_amdgcn_fdot2_f32_bf16(__builtin_bit_cast(bf16x2, a), __builtin_bit_cast(bf16x2, b), c, false); }
template <int CTRL> DI float dpp(float v) { return __uint_as_float(__builtin_amdgcn_update_dpp(0u, __float_as_uint(v), CTRL, 0xf, 0xf, true)); }
DI float sum8(float v) { v += dpp<0xB1>(v); v += dpp<0x4E>(v); v += dpp<0x141>(v); return v; }
DI float sum16(float v) { v = sum8(v); v += dpp<0x140>(v); return v; }
DI float xsum16(float v) { auto r = __builtin_amdgcn_permlane16_swap(__float_as_uint(v), __float_as_uint(v), false, false); return __uint_as_float(r[0]) + __uint_as_float(r[1]); }
DI float xsum32(float v) { auto r = __builtin_amdgcn_permlane32_swap(__float_as_uint(v), __float_as_uint(v), false, false); return __uint_as_float(r[0]) + __uint_as_float(r[1]); }
DI float xmax16(float v) { auto r = __builtin_amdgcn_permlane16_swap(__float_as_uint(v), __float_as_uint(v), false, false); return fmaxf(__uint_as_float(r[0]), __uint_as_float(r[1])); }
DI float xmax32(float v) { auto r = __builtin_amdgcn_permlane32_swap(__float_as_uint(v), __float_as_uint(v), false, false); return fmaxf(__uint_as_float(r[0]), __uint_as_float(r[1])); }
DI float sum64(float v) { return xsum32(xsum16(sum16(v))); }
DI u32x4 bload(rsrc_t r, unsigned v, unsigned s) { return __builtin_amdgcn_raw_buffer_load_b128(r, v, s, 0); }
DI u32x4 bload1(rsrc_t r, unsigned v, unsigned s) { return __builtin_amdgcn_raw_buffer_load_b128(r, v, s, SC1); }
DI bf16x8 as_bf(u32x4 v) { return __builtin_bit_cast(bf16x8, v); }

constexpr unsigned LDS_BYTES = 163840;
constexpr unsigned QST = 34816;
constexpr unsigned QPRE = 3 * QST;
constexpr unsigned QPRE_X = QPRE + 22528;
constexpr unsigned P2STG = 0;
constexpr unsigned P2TMP = 114688;
constexpr unsigned E2SCR = 114688;
constexpr unsigned QSTAT = LDS_BYTES - 1024;
constexpr unsigned SY = QSTAT + 640;
enum { Y_FULL = 0, Y_FREE = 16, Y_H = 32, Y_W1 = 36, Y_PRE = 40, Y_F1 = 44, Y_QS = 48, Y_F2 = 52, Y_ISS = 56, Y_E2 = 60, Y_F3 = 64, Y_TAGOK = 68, Y_TAG = 72 };
constexpr unsigned SPIN_LDS = 1u << 22, SPIN_GLB = 1u << 18;

DI unsigned lds_ld(unsigned a) { return __hip_atomic_load((LAS unsigned*)(uintptr_t)a, __ATOMIC_RELAXED, __HIP_MEMORY_SCOPE_WORKGROUP); }
DI void lds_st(unsigned a, unsigned v) { __hip_atomic_store((LAS unsigned*)(uintptr_t)a, v, __ATOMIC_RELAXED, __HIP_MEMORY_SCOPE_WORKGROUP); }
DI void lds_arrive(unsigned a, int lane) { FENCE(); if (lane == 0) __hip_atomic_fetch_add((LAS unsigned*)(uintptr_t)a, 1u, __ATOMIC_RELAXED, __HIP_MEMORY_SCOPE_WORKGROUP); FENCE(); }
DI void lds_wait(unsigned a, unsigned target) {
  FENCE();
#pragma unroll 1
  for (unsigned n = 0; n < SPIN_LDS; n++) { if ((unsigned)__builtin_amdgcn_readfirstlane(lds_ld(a)) >= target) break; __builtin_amdgcn_s_sleep(1); }
  FENCE();
}
DI void publish(unsigned cnt, unsigned nwaves, char* flag, u64 tag, int lane) {
  WAITV(0);
  if (lane == 0) {
    const unsigned old = __hip_atomic_fetch_add((LAS unsigned*)(uintptr_t)cnt, 1u, __ATOMIC_RELAXED, __HIP_MEMORY_SCOPE_WORKGROUP);
    if (old == nwaves - 1) __hip_atomic_store((GAS u64*)flag, tag, __ATOMIC_RELAXED, __HIP_MEMORY_SCOPE_AGENT);
  }
  FENCE();
}
DI void poll(const char* flag, u64 tag) {
  FENCE();
#pragma unroll 1
  for (unsigned n = 0; n < SPIN_GLB; n++) {
    const u64 v = __hip_atomic_load((const GAS u64*)flag, __ATOMIC_RELAXED, __HIP_MEMORY_SCOPE_AGENT);
    if (__all(v == tag)) break;
    __builtin_amdgcn_s_sleep(1);
  }
  FENCE();
}
DI u64 get_tag(unsigned L0) {
  lds_wait(L0 + SY + Y_TAGOK, 1);
  const unsigned lo = __builtin_amdgcn_readfirstlane(lds_ld(L0 + SY + Y_TAG)), hi = __builtin_amdgcn_readfirstlane(lds_ld(L0 + SY + Y_TAG + 4));
  return ((u64)hi << 32) | lo;
}

#define ST(i)
#define STDECL
#define STFLUSH()

__global__ __launch_bounds__(512, 2) void fused(const u16* __restrict__ x, const u16* __restrict__ wqkv, const u16* __restrict__ wo, const u16* __restrict__ w1,
                                                const u16* __restrict__ b1, const u16* __restrict__ w2, const u16* __restrict__ b2, char* __restrict__ ws,
                                                u16* __restrict__ out) {
  extern __shared__ __attribute__((aligned(16))) char smem[];
  const unsigned L0 = (unsigned)(uintptr_t)(LAS char*)smem;
  const int tid = threadIdx.x, lane = tid & 63;
  const int W = __builtin_amdgcn_readfirstlane(tid >> 6), w = W & 3;
  const int xcd = blockIdx.x & 7, idx = blockIdx.x >> 3;
  const int sq = xcd >> 1, hh = xcd & 1;
  const int st = sq * 8 + (idx >> 2), ct = hh * 4 + (idx & 3);
  const int rg = sq * 64 + hh * 32 + idx;
  const int c = lane & 15, g = lane >> 4, r8 = lane >> 3, pc = lane & 7;
  const f32x4 z4 = {0.f, 0.f, 0.f, 0.f};
  STDECL ST(0);
  if (tid < 24) lds_st(L0 + SY + tid * 4, 0u);

  if (W < 4) {
    const rsrc_t rx = mk_rsrc(x, S * D * 2);
    u32x4 xr[2];
#pragma unroll
    for (int rr = 0; rr < 2; rr++) xr[rr] = bload(rx, lane * 16, (rg * 8 + 2 * w + rr) * 1024);
    u64 ep = 0;
    if (w == 0) ep = __hip_atomic_load((const GAS u64*)(ws + WS_EPOCH), __ATOMIC_RELAXED, __HIP_MEMORY_SCOPE_AGENT);
    BAR();
#pragma unroll
    for (int rr = 0; rr < 2; rr++) {
      float v[8];
#pragma unroll
      for (int j = 0; j < 4; j++) { v[2 * j] = bflo(xr[rr][j]); v[2 * j + 1] = bfhi(xr[rr][j]); }
      float s = 0;
#pragma unroll
      for (int j = 0; j < 8; j++) s += v[j];
      const float m = sum64(s) * (1.f / D);
      float q = 0;
#pragma unroll
      for (int j = 0; j < 8; j++) { v[j] -= m; q += v[j] * v[j]; }
      const float r = __builtin_amdgcn_rsqf(sum64(q) * (1.f / D) + EPS);
      float s2 = 0;
#pragma unroll
      for (int j = 0; j < 8; j++) { v[j] *= r; s2 += v[j]; }
      const float m2 = sum64(s2) * (1.f / D);
      float q2 = 0;
#pragma unroll
      for (int j = 0; j < 8; j++) { v[j] -= m2; q2 += v[j] * v[j]; }
      const float r2 = __builtin_amdgcn_rsqf(sum64(q2) * (1.f / D) + EPS);
      u32x4 o;
#pragma unroll
      for (int j = 0; j < 4; j++) o[j] = pk2(v[2 * j] * r2, v[2 * j + 1] * r2);
      *(LAS u32x4*)(uintptr_t)(L0 + QPRE_X + (2 * w + rr) * 1088 + lane * 16) = o;
    }
    lds_arrive(L0 + SY + Y_H, lane);
    if (w == 0 && lane == 0) { const u64 t = ep + 1; lds_st(L0 + SY + Y_TAG, (unsigned)t); lds_st(L0 + SY + Y_TAG + 4, (unsigned)(t >> 32)); FENCE(); lds_st(L0 + SY + Y_TAGOK, 1u); }
    ST(1);

    f32x4 aq[5], ak[5], av[5];
#pragma unroll
    for (int b = 0; b < 5; b++) { ak[b] = z4; av[b] = z4; aq[b] = z4; }
    float sa[3] = {0.f, 0.f, 0.f}, sb[3] = {0.f, 0.f, 0.f};
    const unsigned ax = c * 128 + ((g ^ (c >> 1)) << 4);
    const unsigned aw = 10240 + (8 * g + (c >> 2)) * 128 + ((w ^ ((c >> 3) | ((g & 1) << 1))) << 5) + (c & 3) * 8;
    const unsigned as2 = (w < 2 ? 8192 : 0) + tid * 16;
    bf16x8 xf[2][5], wf[2][3];
    auto rd = [&](int buf, unsigned b, int kk) {
#pragma unroll
      for (int sec = 0; sec < 3; sec++) wf[buf][sec] = cat8(lds_tr(b + aw + sec * 8192 + kk * 4096), lds_tr(b + aw + sec * 8192 + kk * 4096 + 512));
#pragma unroll
      for (int t = 0; t < 5; t++) xf[buf][t] = lds_b128(b + (ax ^ (kk * 64)) + t * 2048);
    };
    auto mm = [&](int buf) {
      u32x4 x0 = __builtin_bit_cast(u32x4, xf[buf][0]);
#pragma unroll
      for (int j = 0; j < 4; j++) x0[j] = c == 0 ? 0x3f803f80u : x0[j];
#pragma unroll
      for (int t = 4; t >= 0; t--) {
        const bf16x8 xx = t == 0 ? as_bf(x0) : xf[buf][t];
        ak[t] = mfma32(wf[buf][1], xx, ak[t]);
        av[t] = mfma32(xx, wf[buf][2], av[t]);
        aq[t] = mfma32(wf[buf][0], xx, aq[t]);
      }
    };
    auto wait_full = [&](unsigned n) {
      FENCE();
#pragma unroll 1
      for (unsigned i = 0; i < SPIN_LDS; i++) {
        const unsigned f0 = lds_ld(L0 + SY + Y_FULL), f1 = lds_ld(L0 + SY + Y_FULL + 4);
        if ((unsigned)__builtin_amdgcn_readfirstlane(min(f0, f1)) >= n) break;
        __builtin_amdgcn_s_sleep(1);
      }
      FENCE();
    };
    wait_full(1);
    ST(2);
    rd(0, L0, 0);
#pragma unroll 1
    for (int s = 0; s < 8; s++) {
      const unsigned b = L0 + (s & 3) * QST;
      rd(1, b, 1);
      u32x4 su[3];
      su[0] = *(const LAS u32x4*)(uintptr_t)(b + tid * 16);
      su[1] = *(const LAS u32x4*)(uintptr_t)(b + 4096 + tid * 16);
      su[2] = *(const LAS u32x4*)(uintptr_t)(b + as2);
      FENCE();
      if (lane == 0) lds_st(L0 + SY + Y_FREE + w * 4, s + 1);
      SCHED();
      mm(0);
      SCHED();
      if (s < 7) { wait_full(s + 2); rd(0, L0 + ((s + 1) & 3) * QST, 0); }
      SCHED();
      mm(1);
#pragma unroll
      for (int n = 0; n < 3; n++)
#pragma unroll
        for (int j = 0; j < 4; j++) { sa[n] = dot2(su[n][j], 0x3f803f80u, sa[n]); sb[n] = dot2(su[n][j], su[n][j], sb[n]); }
      SCHED();
    }
    ST(3);
#pragma unroll
    for (int n = 0; n < 3; n++) {
      const float m = sum8(sa[n]) * (1.f / D);
      const float var = fmaxf(sum8(sb[n]) * (1.f / D) - m * m, 0.f);
      const float r = __builtin_amdgcn_rsqf(var + EPS);
      if (pc == 0 && (n < 2 || w < 2)) { f32x2 o = {r, m * r}; *(LAS f32x2*)(uintptr_t)(L0 + QSTAT + (n * 32 + (tid >> 3)) * 8) = o; }
    }
    lds_arrive(L0 + SY + Y_QS, lane);
    float cq[4], ck[4];
#pragma unroll
    for (int j = 0; j < 4; j++) { cq[j] = dpp<0x150>(aq[0][j]); ck[j] = dpp<0x150>(ak[0][j]); }
    const float cv = xsum32(xsum16(g == 0 ? av[0][0] : 0.f));
    lds_wait(L0 + SY + Y_QS, 4);
    bf16x4 qf[4], kf[5], vf[5];
#pragma unroll
    for (int t = 0; t < 5; t++) {
      const f32x2 sc = *(const LAS f32x2*)(uintptr_t)(L0 + QSTAT + (16 * t + c) * 8);
      kf[t] = pk4(sc[0] * ak[t][0] - sc[1] * ck[0], sc[0] * ak[t][1] - sc[1] * ck[1], sc[0] * ak[t][2] - sc[1] * ck[2], sc[0] * ak[t][3] - sc[1] * ck[3]);
      if (t > 0) qf[t - 1] = pk4(sc[0] * aq[t][0] - sc[1] * cq[0], sc[0] * aq[t][1] - sc[1] * cq[1], sc[0] * aq[t][2] - sc[1] * cq[2], sc[0] * aq[t][3] - sc[1] * cq[3]);
      const f32x4 s0 = lds_f4(L0 + QSTAT + (16 * t + 4 * g) * 8), s1v = lds_f4(L0 + QSTAT + (16 * t + 4 * g) * 8 + 16);
      vf[t] = pk4(s0[0] * av[t][0] - s0[1] * cv, s0[2] * av[t][1] - s0[3] * cv, s1v[0] * av[t][2] - s1v[1] * cv, s1v[2] * av[t][3] - s1v[3] * cv);
    }
    u32x2 op[4];
    const bool odd = c & 1;
    const int k0 = 4 * g + (c & 1), k1 = k0 + 2;
    const bool cur0 = k0 <= c, cur1 = k1 <= c;
    constexpr float SC = 0.25f * 1.4426950408889634f;
#pragma unroll
    for (int t = 1; t < 5; t++) {
      const f32x4 sp = mfma16(kf[t - 1], qf[t - 1], z4), scu = mfma16(kf[t], qf[t - 1], z4);
      const float p0 = odd ? sp[1] : sp[0], p1 = odd ? sp[3] : sp[2];
      const float c0 = odd ? scu[1] : scu[0], c1 = odd ? scu[3] : scu[2];
      float l0 = (cur0 ? c0 : p0) * SC, l1 = (cur1 ? c1 : p1) * SC;
      if (t == 1 && st == 0) { l0 = cur0 ? l0 : -1e9f; l1 = cur1 ? l1 : -1e9f; }
      const float mx = xmax32(xmax16(fmaxf(l0, l1)));
      const float e0 = __builtin_amdgcn_exp2f(l0 - mx), e1 = __builtin_amdgcn_exp2f(l1 - mx);
      const float inv = __builtin_amdgcn_rcpf(xsum32(xsum16(e0 + e1)));
      const unsigned sh = odd ? 16 : 0;
      const unsigned b0 = (pk2(e0 * inv, 0.f) & 0xffffu) << sh, b1v = (pk2(e1 * inv, 0.f) & 0xffffu) << sh;
      const u32x4 pu = {cur0 ? 0u : b0, cur1 ? 0u : b1v, cur0 ? b0 : 0u, cur1 ? b1v : 0u};
      const f32x4 o = mfma32(cat8(vf[t - 1], vf[t]), as_bf(pu), z4);
      op[t - 1][0] = pk2(o[0], o[1]); op[t - 1][1] = pk2(o[2], o[3]);
    }
    const rsrc_t ro = mk_rsrc(ws + WS_O, O_BYTES);
#pragma unroll
    for (int t = 0; t < 4; t += 2) {
      const auto r0 = __builtin_amdgcn_permlane16_swap(op[t][0], op[t + 1][0], false, false);
      const auto r1 = __builtin_amdgcn_permlane16_swap(op[t][1], op[t + 1][1], false, false);
      const u32x4 v = {r0[0], r1[0], r0[1], r1[1]};
      const unsigned vo = (g & 1) * 16384 + ((g >> 1) * 16 + c) * 16;
      __builtin_amdgcn_raw_buffer_store_b128(v, ro, vo, ((4 * st + t) * 16 + 2 * ct + (w >> 1)) * 1024 + (w & 1) * 512, SC1);
    }
    const u64 tag = get_tag(L0);
    publish(L0 + SY + Y_F2, 4, ws + WS_F2 + (st * 8 + ct) * 128, tag, lane);
    ST(4);

    const rsrc_t rgg = mk_rsrc(ws + WS_G, G_BYTES), rwo = mk_rsrc(wo, D * D * 2), rw2 = mk_rsrc(w2, F * S * 2);
    lds_wait(L0 + SY + Y_ISS, 4);
    poll(ws + WS_F2 + (st * 8 + (lane & 7)) * 128, tag);
    ST(5);
    f32x4 acc[4][4];
#pragma unroll
    for (int i = 0; i < 4; i++)
#pragma unroll
      for (int j = 0; j < 4; j++) acc[i][j] = z4;
    const unsigned vo = lane * 16 + (4 * st * 16 + 4 * w) * 1024;
    const unsigned vg = lane * 16 + (4 * ct * 43 + 11 * w) * 1024;
    const unsigned vwo = (128 * w + r8) * 1024 + ct * 128 + pc * 16;
    const unsigned vw2 = (352 * w + r8) * 4096 + st * 128 + pc * 16;
    const unsigned lw = L0 + P2STG + w * 28672;
    const unsigned aw0 = lw + r8 * 128 + ((((pc >> 1) ^ ((r8 >> 1) & 1)) << 5) | ((pc & 1) << 4));
    const unsigned ar = lw + (8 * g + (c >> 2)) * 128 + ((((c >> 3) | ((g & 1) << 1))) << 5) + (c & 3) * 8;
    u32x4 ow[4][4], of[4][4], wr[11][4], gf[11][4];
    auto LO = [&](int k) {
#pragma unroll
      for (int j = 0; j < 4; j++) ow[k][j] = bload(rwo, vwo, (32 * k + 8 * j) * 1024);
#pragma unroll
      for (int j = 0; j < 4; j++) of[k][j] = bload1(ro, vo, ((w + j) & 3) * 16384 + k * 1024);
    };
    auto LW = [&](int k) {
#pragma unroll
      for (int j = 0; j < 4; j++) wr[k][j] = bload(rw2, vw2 + (32 * k + 8 * j) * 4096, 0);
    };
    auto LG = [&](int k) {
      const unsigned vgk = (k == 10 && w == 3) ? 0x80000000u : vg;
#pragma unroll
      for (int j = 0; j < 4; j++) gf[k][j] = bload1(rgg, vgk, j * 43 * 1024 + k * 1024);
    };
    auto SW = [&](const u32x4 (&v)[4], int slot) {
#pragma unroll
      for (int j = 0; j < 4; j++) *(LAS u32x4*)(uintptr_t)((aw0 ^ ((j & 1) << 6)) + j * 1024 + slot * 4096) = v[j];
    };
    auto CO = [&](int k) {
      SW(ow[k], 5 + (k & 1));
      bf16x8 tw[4];
#pragma unroll
      for (int e = 0; e < 4; e++) tw[e] = cat8(lds_tr((ar ^ (e << 5)) + (5 + (k & 1)) * 4096), lds_tr((ar ^ (e << 5)) + 512 + (5 + (k & 1)) * 4096));
#pragma unroll
      for (int i = 0; i < 4; i++)
#pragma unroll
        for (int j = 0; j < 4; j++) acc[i][j] = mfma32(tw[i], as_bf(of[k][j]), acc[i][j]);
    };
    unsigned arr[4];
#pragma unroll
    for (int j = 0; j < 4; j++) arr[j] = ar ^ (((w + j) & 3) << 5);
    auto TR = [&](bf16x8 (&t)[4], int slot) {
#pragma unroll
      for (int j = 0; j < 4; j++) t[j] = cat8(lds_tr(arr[j] + slot * 4096), lds_tr(arr[j] + 512 + slot * 4096));
    };
    LO(0); LO(1); LO(2); SCHED();
    CO(0); SCHED(); LO(3); SCHED();
    CO(1); SCHED(); LW(7); LW(8); SCHED();
    CO(2); SCHED(); LW(9); LW(10); SCHED();
    CO(3); SCHED(); LW(0); LW(1); SCHED();
    bf16x8 tk[4][4];
    SW(wr[7], 5); TR(tk[0], 5); SW(wr[8], 6); TR(tk[1], 6); SCHED(); LW(2); LW(3); SCHED();
    SW(wr[9], 5); TR(tk[2], 5); SW(wr[10], 6); TR(tk[3], 6); SCHED(); LW(4); LW(5); SCHED();
    SW(wr[0], 0); SW(wr[1], 1); SCHED(); LW(6);
    const int srow = 64 * st + 16 * w + c;
    u32x4 xv[2];
#pragma unroll
    for (int i = 0; i < 2; i++) xv[i] = bload(rx, (srow * D + 64 * ct + 32 * i + 16 * (g & 1) + 8 * (g >> 1)) * 2, 0);
    const float bias = bflo(b2[srow]);
    SCHED();
    SW(wr[2], 2); SW(wr[3], 3); SW(wr[4], 4); SW(wr[5], 5); SW(wr[6], 6);
    ST(6);
    {
      const int f0 = (352 * w) / 48 + (lane & 15);
      poll(ws + WS_F3 + (ct * 32 + min(f0, min(28, (352 * w + 351) / 48))) * 128, tag);
    }
    ST(7);
    LG(0); LG(1); LG(2); LG(3);
    bf16x8 tw[2][4];
    TR(tw[0], 0);
    SCHED();
#pragma unroll
    for (int k = 0; k < 11; k++) {
      if (k + 1 < 7) TR(tw[(k + 1) & 1], k + 1);
#pragma unroll
      for (int i = 0; i < 4; i++)
#pragma unroll
        for (int j = 0; j < 4; j++) acc[i][j] = mfma32(as_bf(gf[k][i]), k < 7 ? tw[k & 1][j] : tk[k < 7 ? 0 : k - 7][j], acc[i][j]);
      if (k + 4 < 11) LG(k + 4);
      SCHED();
    }
    ST(8);
    BAR();
#pragma unroll
    for (int j = 1; j < 4; j++)
#pragma unroll
      for (int i = 0; i < 4; i++) *(LAS f32x4*)(uintptr_t)(L0 + ((((w + j) & 3) * 3 + (j - 1)) * 4 + i) * 1024 + lane * 16) = acc[i][j];
    BAR();
    ST(9);
    f32x4 u[4];
#pragma unroll
    for (int i = 0; i < 4; i++) {
      u[i] = acc[i][0] + bias;
#pragma unroll
      for (int j = 0; j < 3; j++) u[i] += lds_f4(L0 + ((w * 3 + j) * 4 + i) * 1024 + lane * 16);
    }
#pragma unroll
    for (int i = 0; i < 4; i += 2) {
      float v[8];
#pragma unroll
      for (int r = 0; r < 4; r++) {
        const auto sw = __builtin_amdgcn_permlane16_swap(__float_as_uint(u[i][r]), __float_as_uint(u[i + 1][r]), false, false);
        v[r] = __uint_as_float(sw[0]); v[4 + r] = __uint_as_float(sw[1]);
      }
      u32x4 o;
#pragma unroll
      for (int j = 0; j < 4; j++) o[j] = pk2(v[2 * j] + bflo(xv[i >> 1][j]), v[2 * j + 1] + bfhi(xv[i >> 1][j]));
      *(u32x4*)(out + srow * D + 64 * ct + 16 * i + 16 * (g & 1) + 8 * (g >> 1)) = o;
    }
    ST(10);
  } else {
    if (w < 2) {
      const u32x4 sx = mk_srd(x, S * D * 2), sw = mk_srd(wqkv, D * 3 * D * 2);
      const unsigned vx0 = (unsigned)((64 * st - 16 + 8 * w + r8) * 1024 + ((pc ^ ((w << 2) | (r8 >> 1))) << 4));
      const unsigned vx1 = vx0 + 16384u;
      const unsigned vw = (8 * w + r8) * 3072 + ct * 128 + ((((pc >> 1) ^ (((r8 >> 1) & 1) | (w << 1))) << 5) | ((pc & 1) << 4));
      auto half0 = [&](int s) {
        const unsigned b = L0 + (s & 3) * QST + w * 1024;
        glds16(sx, vx0, s * 128, b);
#pragma unroll
        for (int j = 0; j < 4; j++) glds16(sx, vx1, s * 128 + j * 16384, b + 2048 + j * 2048);
#pragma unroll
        for (int sec = 0; sec < 3; sec++) glds16(sw, vw, s * 64 * 3072 + sec * 1024, b + 10240 + sec * 8192);
      };
      auto half1 = [&](int s) {
        const unsigned b = L0 + (s & 3) * QST + w * 1024;
#pragma unroll
        for (int j = 1; j < 4; j++)
#pragma unroll
          for (int sec = 0; sec < 3; sec++) glds16(sw, vw, s * 64 * 3072 + j * 16 * 3072 + sec * 1024, b + 10240 + sec * 8192 + j * 2048);
      };
      half0(0);
      BAR();
      half1(0);
#pragma unroll 1
      for (int s = 1; s < 8; s++) {
        if (s == 3) lds_wait(L0 + SY + Y_PRE, 2);
        if (s >= 4) {
          FENCE();
#pragma unroll 1
          for (unsigned i = 0; i < SPIN_LDS; i++) {
            const unsigned f = min(min(lds_ld(L0 + SY + Y_FREE), lds_ld(L0 + SY + Y_FREE + 4)), min(lds_ld(L0 + SY + Y_FREE + 8), lds_ld(L0 + SY + Y_FREE + 12)));
            if ((unsigned)__builtin_amdgcn_readfirstlane(f) >= (unsigned)(s - 3)) break;
            __builtin_amdgcn_s_sleep(1);
          }
          FENCE();
        }
        half0(s);
        if (s >= 2) { WAITV(25); if (lane == 0) lds_st(L0 + SY + Y_FULL + w * 4, s - 1); FENCE(); }
        half1(s);
        if (s == 2) { WAITV(17); if (lane == 0) lds_st(L0 + SY + Y_FULL + w * 4, 2); FENCE(); }
      }
      ST(1);
      WAITV(17); if (lane == 0) lds_st(L0 + SY + Y_FULL + w * 4, 7);
      WAITV(0); if (lane == 0) lds_st(L0 + SY + Y_FULL + w * 4, 8);
      FENCE();
    } else {
      const u32x4 s1 = mk_srd(w1, S * F * 2);
      const int v = w - 2;
#pragma unroll
      for (int n = 0; n < 11; n++) {
        const unsigned o = (v + 2 * n) * 1024 + lane * 16;
        glds16(s1, (n < 10 || o < 22016u) ? o : 0x80000000u, rg * 22016, L0 + QPRE + (v + 2 * n) * 1024);
      }
      BAR();
      WAITV(0);
      lds_arrive(L0 + SY + Y_W1, lane);
      const rsrc_t rht = mk_rsrc(ws + WS_HT, HT_BYTES), rw1t = mk_rsrc(ws + WS_W1T, W1T_BYTES);
      const int q4 = c >> 2, p4 = c & 3;
      const unsigned vo = (4 * v + g) * 65536 + (rg >> 2) * 1024 + ((rg & 3) * 16 + c) * 16;
      lds_wait(L0 + SY + Y_W1, 2);
      bf16x8 tv[15];
#pragma unroll
      for (int n = 0; n < 11; n++) {
        const unsigned a = L0 + QPRE + q4 * 2752 + ((v + 2 * n) * 4 + g) * 32 + p4 * 8;
        tv[n] = cat8(lds_tr(a), lds_tr(a + 4 * 2752));
      }
      lds_wait(L0 + SY + Y_H, 4);
#pragma unroll
      for (int n = 0; n < 4; n++) {
        const unsigned a = L0 + QPRE_X + q4 * 1088 + ((v + 2 * n) * 4 + g) * 32 + p4 * 8;
        tv[11 + n] = cat8(lds_tr(a), lds_tr(a + 4 * 1088));
      }
      lds_arrive(L0 + SY + Y_PRE, lane);
#pragma unroll
      for (int n = 0; n < 11; n++) __builtin_amdgcn_raw_buffer_store_b128(__builtin_bit_cast(u32x4, tv[n]), rw1t, vo + n * 8 * 65536, 0, SC1);
#pragma unroll
      for (int n = 0; n < 4; n++) __builtin_amdgcn_raw_buffer_store_b128(__builtin_bit_cast(u32x4, tv[11 + n]), rht, vo, n * 8 * 65536, SC1);
      ST(1);
      publish(L0 + SY + Y_F1, 2, ws + WS_F1 + rg * 32, get_tag(L0), lane);
    }
    ST(2);
    const int fl = idx >> 2;
    if (sq == 0 || fl < 7) {
      const u64 tag = get_tag(L0);
      const int ft = (sq == 0 ? 0 : 7 * sq + 1) + fl;
      const rsrc_t rht = mk_rsrc(ws + WS_HT, HT_BYTES), rw1t = mk_rsrc(ws + WS_W1T, W1T_BYTES);
      u32x2 bb[3];
#pragma unroll
      for (int i = 0; i < 3; i++) bb[i] = *(const u32x2*)(b1 + min(48 * ft + 16 * i + 4 * g, F - 4));
      poll(ws + WS_F1 + (64 * w + lane) * 32, tag);
      ST(3);
      constexpr int DEP = 4;
      u32x4 fa[DEP][3], fb[DEP][4];
      const unsigned va = lane * 16 + (3 * ft * 64 + 16 * w) * 1024, vb = lane * 16 + (4 * ct * 64 + 16 * w) * 1024;
      auto load = [&](int k) {
#pragma unroll
        for (int j = 0; j < 4; j++) fb[k % DEP][j] = bload1(rht, vb, ((w + j) & 3) * 65536 + k * 1024);
#pragma unroll
        for (int i = 0; i < 3; i++) fa[k % DEP][i] = bload1(rw1t, va + i * 65536, k * 1024);
      };
      f32x4 acc[3][4];
#pragma unroll
      for (int i = 0; i < 3; i++)
#pragma unroll
        for (int j = 0; j < 4; j++) acc[i][j] = z4;
#pragma unroll
      for (int k = 0; k < DEP; k++) load(k);
      SCHED();
#pragma unroll
      for (int k = 0; k < 16; k++) {
#pragma unroll
        for (int i = 0; i < 3; i++)
#pragma unroll
          for (int j = 0; j < 4; j++) acc[i][j] = mfma32(as_bf(fa[k % DEP][i]), as_bf(fb[k % DEP][j]), acc[i][j]);
        if (k + DEP < 16) load(k + DEP);
        if (k + DEP == 15) lds_arrive(L0 + SY + Y_ISS, lane);
        SCHED();
      }
      ST(4);
      lds_wait(L0 + SY + Y_QS, 4);
#pragma unroll
      for (int j = 1; j < 4; j++)
#pragma unroll
        for (int i = 0; i < 3; i++) *(LAS f32x4*)(uintptr_t)(L0 + E2SCR + ((((w + j) & 3) * 3 + (j - 1)) * 3 + i) * 1024 + lane * 16) = acc[i][j];
      lds_arrive(L0 + SY + Y_E2, lane);
      lds_wait(L0 + SY + Y_E2, 4);
      ST(5);
      u32x2 gp[3];
#pragma unroll
      for (int i = 0; i < 3; i++) {
        f32x4 u = acc[i][0];
#pragma unroll
        for (int j = 0; j < 3; j++) u += lds_f4(L0 + E2SCR + ((w * 3 + j) * 3 + i) * 1024 + lane * 16);
        float o[4];
        const float bv[4] = {bflo(bb[i][0]), bfhi(bb[i][0]), bflo(bb[i][1]), bfhi(bb[i][1])};
#pragma unroll
        for (int r = 0; r < 4; r++) {
          const float t = u[r] + bv[r];
          const float z = t * (1.f + 0.044715f * t * t) * (-2.f * 0.7978845608028654f * 1.4426950408889634f);
          o[r] = t * __builtin_amdgcn_rcpf(1.f + __builtin_amdgcn_exp2f(z));
        }
        gp[i][0] = pk2(o[0], o[1]); gp[i][1] = pk2(o[2], o[3]);
      }
      const rsrc_t rgg = mk_rsrc(ws + WS_G, G_BYTES);
      const auto r0 = __builtin_amdgcn_permlane16_swap(gp[0][0], gp[1][0], false, false);
      const auto r1 = __builtin_amdgcn_permlane16_swap(gp[0][1], gp[1][1], false, false);
      const u32x4 v = {r0[0], r1[0], r0[1], r1[1]};
      const int f8 = 6 * ft + 2 * (g & 1) + (g >> 1);
      __builtin_amdgcn_raw_buffer_store_b128(v, rgg, f8 * 256 + c * 16, (4 * ct + w) * 43 * 1024, SC1);
      const auto r2 = __builtin_amdgcn_permlane16_swap(gp[2][0], gp[2][0], false, false);
      const auto r3 = __builtin_amdgcn_permlane16_swap(gp[2][1], gp[2][1], false, false);
      const u32x4 v2 = {r2[0], r3[0], r2[1], r3[1]};
      const int f8b = 6 * ft + 4 + (g >> 1);
      __builtin_amdgcn_raw_buffer_store_b128(v2, rgg, ((g & 1) == 0 && f8b < 172) ? f8b * 256 + c * 16 : 0x80000000u, (4 * ct + w) * 43 * 1024, SC1);
      ST(6);
      publish(L0 + SY + Y_F3, 4, ws + WS_F3 + (ct * 32 + ft) * 128, tag, lane);
      if (blockIdx.x == 0 && w == 0 && lane == 0) __hip_atomic_store((GAS u64*)(ws + WS_EPOCH), tag, __ATOMIC_RELAXED, __HIP_MEMORY_SCOPE_AGENT);
      ST(7);
    } else {
      lds_arrive(L0 + SY + Y_ISS, lane);
    }
    BAR();
    BAR();
  }
  STFLUSH();
}

template <int NV>
__global__ __launch_bounds__(PTHREADS) void probe(float* sink) {
  const u64 t0 = __builtin_amdgcn_s_memrealtime();
  if (NV == 256) asm volatile("v_mov_b32 v250, 0" ::: "v250");
  if (NV == 512) asm volatile("v_accvgpr_write_b32 a250, 0\n\tv_mov_b32 v250, 0" ::: "v250", "a250");
  while (__builtin_amdgcn_s_memrealtime() < t0 + 10000) __builtin_amdgcn_s_sleep(2);
}
extern "C" void kernel_launch(void* const* d_in, const int* in_sizes, int n_in, void* d_out, int out_size, void* d_ws, size_t ws_size, hipStream_t stream) {
  (void)hipFuncSetAttribute((const void*)fused, hipFuncAttributeMaxDynamicSharedMemorySize, LDS_BYTES);
  (void)hipFuncSetAttribute((const void*)probe<PNV>, hipFuncAttributeMaxDynamicSharedMemorySize, 163840);
  probe<PNV><<<PBLOCKS, PTHREADS, PLDS, stream>>>((float*)d_ws);
  fused<<<256, 512, LDS_BYTES, stream>>>((const u16*)d_in[0], (const u16*)d_in[1], (const u16*)d_in[2], (const u16*)d_in[4], (const u16*)d_in[5],
                                         (const u16*)d_in[6], (const u16*)d_in[7], (char*)d_ws, (u16*)d_out);
}
```

```cpp
#include <hip/hip_runtime.h>
#include <stdint.h>

#define CODE_LEN 37888

typedef unsigned short u16;
typedef unsigned long long u64;
typedef short s16x4 __attribute__((ext_vector_type(4)));
typedef __bf16 bf16x2 __attribute__((ext_vector_type(2)));
typedef __bf16 bf16x4 __attribute__((ext_vector_type(4)));
typedef __bf16 bf16x8 __attribute__((ext_vector_type(8)));
typedef float f32x2 __attribute__((ext_vector_type(2)));
typedef float f32x4 __attribute__((ext_vector_type(4)));
typedef unsigned u32x2 __attribute__((ext_vector_type(2)));
typedef unsigned u32x4 __attribute__((ext_vector_type(4)));
#define LAS __attribute__((address_space(3)))
#define GAS __attribute__((address_space(1)))
#define DI __device__ __forceinline__

constexpr int S = 2048, D = 512, F = 1376;
constexpr float EPS = 1e-5f;
constexpr unsigned HT_BYTES = 32u * 64u * 1024u;
constexpr unsigned G_BYTES = 32u * 43u * 1024u;
constexpr unsigned O_BYTES = 128u * 16u * 1024u;
constexpr size_t WS_EPOCH = 0;
constexpr size_t WS_C1 = 4096;
constexpr size_t WS_C2 = WS_C1 + 256 * 128;
constexpr size_t WS_C3 = WS_C2 + 128 * 128;
constexpr size_t WS_C4 = WS_C3 + 256 * 128;
constexpr size_t WS_STAT = WS_C4 + 256 * 128;
constexpr size_t WS_HT = WS_STAT + 16384;
constexpr size_t WS_G = WS_HT + HT_BYTES;
constexpr size_t WS_O = WS_G + G_BYTES;
constexpr size_t WS_BASE = WS_O + O_BYTES;
constexpr int SC1 = 16;
constexpr unsigned OOB = 0x80000000u;

typedef __amdgpu_buffer_rsrc_t rsrc_t;
DI rsrc_t mk_rsrc(const void* p, unsigned bytes) { return __builtin_amdgcn_make_buffer_rsrc((void*)p, 0, bytes, 0x00020000); }
DI u32x4 mk_srd(const void* p, unsigned bytes) {
  const uint64_t a = (uint64_t)p;
  u32x4 r = {(unsigned)a, (unsigned)(a >> 32) & 0xffffu, bytes, 0x00020000u};
  return r;
}
DI void glds16(u32x4 srd, unsigned voff, unsigned soff, unsigned lds) {
  asm volatile("s_mov_b32 m0, %3\n\ts_nop 0\n\tbuffer_load_dwordx4 %0, %1, %2 offen lds" ::"v"(voff), "s"(srd), "s"(soff), "s"(lds) : "memory");
}
DI void glds16x3(u32x4 srd, unsigned voff, unsigned soff, unsigned lds) {
  asm volatile("s_mov_b32 m0, %3\n\ts_nop 0\n\tbuffer_load_dwordx4 %0, %1, %2 offen lds\n\tbuffer_load_dwordx4 %0, %1, %2 offen offset:1024 lds\n\t"
               "buffer_load_dwordx4 %0, %1, %2 offen offset:2048 lds" ::"v"(voff), "s"(srd), "s"(soff), "s"(lds) : "memory");
}
#define WAITV(n) asm volatile("s_waitcnt vmcnt(" #n ")" ::: "memory")
#define FENCE() asm volatile("" ::: "memory")
#define SCHED() __builtin_amdgcn_sched_barrier(0)
#define BAR() do { asm volatile("s_waitcnt lgkmcnt(0)" ::: "memory"); __builtin_amdgcn_s_barrier(); FENCE(); } while (0)

typedef LAS char* lp;
DI lp LP(unsigned a) { return (lp)(uintptr_t)a; }
DI bf16x8 cat8(bf16x4 a, bf16x4 b) { return __builtin_shufflevector(a, b, 0, 1, 2, 3, 4, 5, 6, 7); }
DI bf16x8 l128(lp p) { return *(const LAS bf16x8*)p; }
DI f32x4 lf4(lp p) { return *(const LAS f32x4*)p; }
DI bf16x4 ltr(lp p) { return __builtin_amdgcn_ds_read_tr16_b64_v4bf16((LAS bf16x4*)p); }
DI bf16x8 ltr2(lp p, int d) { return cat8(ltr(p), ltr(p + d)); }
DI f32x4 mfma32(bf16x8 a, bf16x8 b, f32x4 c) { return __builtin_amdgcn_mfma_f32_16x16x32_bf16(a, b, c, 0, 0, 0); }
DI f32x4 mfma16(u32x2 a, u32x2 b, f32x4 c) { return __builtin_amdgcn_mfma_f32_16x16x16bf16_1k(__builtin_bit_cast(s16x4, a), __builtin_bit_cast(s16x4, b), c, 0, 0, 0); }
DI unsigned pkv(f32x2 f) { return __builtin_bit_cast(unsigned, __builtin_convertvector(f, bf16x2)); }
DI unsigned pk2(float a, float b) { f32x2 f = {a, b}; return pkv(f); }
DI f32x2 lo2(f32x4 v) { return __builtin_shufflevector(v, v, 0, 1); }
DI f32x2 hi2(f32x4 v) { return __builtin_shufflevector(v, v, 2, 3); }
DI f32x2 fma2(f32x2 a, f32x2 b, f32x2 c) { return __builtin_elementwise_fma(a, b, c); }
DI f32x2 bc2(float a) { f32x2 r = {a, a}; return r; }
DI float bflo(unsigned u) { return __uint_as_float(u << 16); }
DI float bfhi(unsigned u) { return __uint_as_float(u & 0xffff0000u); }
template <int CTRL> DI float dpp(float v) { return __uint_as_float(__builtin_amdgcn_update_dpp(0u, __float_as_uint(v), CTRL, 0xf, 0xf, true)); }
DI float sum16(float v) { v += dpp<0xB1>(v); v += dpp<0x4E>(v); v += dpp<0x141>(v); v += dpp<0x140>(v); return v; }
DI f32x2 swap16(float a, float b) { auto r = __builtin_amdgcn_permlane16_swap(__float_as_uint(a), __float_as_uint(b), false, false); f32x2 o = {__uint_as_float(r[0]), __uint_as_float(r[1])}; return o; }
DI f32x2 swap32(float a, float b) { auto r = __builtin_amdgcn_permlane32_swap(__float_as_uint(a), __float_as_uint(b), false, false); f32x2 o = {__uint_as_float(r[0]), __uint_as_float(r[1])}; return o; }
DI u32x4 bload(rsrc_t r, unsigned v, unsigned s) { return __builtin_amdgcn_raw_buffer_load_b128(r, v, s, 0); }
DI u32x4 bload1(rsrc_t r, unsigned v, unsigned s) { return __builtin_amdgcn_raw_buffer_load_b128(r, v, s, SC1); }
DI void bstore1(u32x4 d, rsrc_t r, unsigned v, unsigned s) { __builtin_amdgcn_raw_buffer_store_b128(d, r, v, s, SC1); }
DI bf16x8 as_bf(u32x4 v) { return __builtin_bit_cast(bf16x8, v); }
DI u64 uni64(u64 v) { return ((u64)(unsigned)__builtin_amdgcn_readfirstlane((unsigned)(v >> 32)) << 32) | (unsigned)__builtin_amdgcn_readfirstlane((unsigned)v); }

constexpr unsigned LDS_BYTES = 163840;
constexpr unsigned QST = 34816;
constexpr unsigned QPRE_S = 4 * QST;
constexpr unsigned QSTAT = QPRE_S + 128;
constexpr unsigned QPRE_X = QPRE_S + 1024;
constexpr unsigned G1STG = QPRE_X;
constexpr unsigned P2STG = 0;
constexpr unsigned E2SCR = 98304;
constexpr unsigned SY = LDS_BYTES - 512;
enum { Y_FULL = 0, Y_FREE = 16, Y_H = 32, Y_HD = 48, Y_ST = 64, Y_QS = 80, Y_ISS = 96, Y_E2 = 112, Y_PA = 128, Y_OK2 = 144, Y_OK3 = 160, Y_OK4 = 176, Y_WO = 192,
       Y_N1 = 208, Y_N2 = 212, Y_N3 = 216, Y_TAG = 224 };
static_assert(G1STG + 16384 <= SY, "lds");
constexpr unsigned SPIN_GLB = 1u << 17;

template <int OFF> DI void sy_put(unsigned a, unsigned v) { asm volatile("ds_write_b32 %0, %1 offset:%2" ::"v"(a), "v"(v), "i"(OFF) : "memory"); }
template <int OFF> DI void sy_wait(unsigned a4, unsigned target) {
  unsigned t, n;
  asm volatile("s_mov_b32 %1, 0x200000\n"
               "1:\n\tds_read_b32 %0, %2 offset:%4\n\ts_waitcnt lgkmcnt(0)\n\tv_cmp_gt_u32 vcc, %3, %0\n\ts_cbranch_vccz 2f\n\t"
               "s_sleep 1\n\ts_sub_u32 %1, %1, 1\n\ts_cmp_lg_u32 %1, 0\n\ts_cbranch_scc1 1b\n"
               "2:" : "=&v"(t), "=&s"(n) : "v"(a4), "s"(target), "i"(OFF) : "vcc", "scc", "memory");
}
DI bool last_of(unsigned cnt, unsigned n, int lane) {
  unsigned old = 0;
  if (lane == 0) asm volatile("ds_add_rtn_u32 %0, %1, %2\n\ts_waitcnt lgkmcnt(0)" : "=&v"(old) : "v"(cnt), "v"(1u) : "memory");
  return (unsigned)__builtin_amdgcn_readfirstlane(old) == n - 1;
}
DI void gadd(char* p) { (void)__hip_atomic_fetch_add((GAS u64*)p, 1ull, __ATOMIC_RELAXED, __HIP_MEMORY_SCOPE_AGENT); }
DI u64 gpeek(const char* p) {
  u64 z = 0;
  asm volatile("" : "+v"(z));
  return __hip_atomic_fetch_add((GAS u64*)p, z, __ATOMIC_RELAXED, __HIP_MEMORY_SCOPE_AGENT);
}
template <int SL> DI void gwait(const char* p, bool on, u64 mask, u64 want) {
  FENCE();
  bool ok = !on;
#pragma unroll 1
  for (unsigned n = 0; n < SPIN_GLB; n++) {
    if (!ok) ok = (gpeek(p) & mask) == want;
    if (__all(ok)) break;
    __builtin_amdgcn_s_sleep(SL);
  }
  FENCE();
}

#define ST(i)
#define STDECL
#define STFLUSH()

__global__ __launch_bounds__(512, 2) void fused(const u16* __restrict__ x, const u16* __restrict__ wqkv, const u16* __restrict__ wo, const u16* __restrict__ w1,
                                                const u16* __restrict__ b1, const u16* __restrict__ w2, const u16* __restrict__ b2, char* __restrict__ ws,
                                                u16* __restrict__ out) {
  extern __shared__ __attribute__((aligned(16))) char smem[];
  uint64_t pc0; asm volatile("s_getpc_b64 %0" : "=s"(pc0));
  const unsigned L0 = (unsigned)(uintptr_t)(LAS char*)smem;
  const int tid = threadIdx.x, lane = tid & 63;
  const int W = __builtin_amdgcn_readfirstlane(tid >> 6), w = W & 3;
  const int xcd = blockIdx.x & 7, idx = blockIdx.x >> 3;
  const int sq = xcd >> 1, hh = xcd & 1;
  const int st = sq * 8 + (idx >> 2), ct = hh * 4 + (idx & 3);
  const int rg = sq * 64 + hh * 32 + idx;
  const int c = lane & 15, g = lane >> 4, r8 = lane >> 3, pc = lane & 7;
  const f32x4 z4 = {0.f, 0.f, 0.f, 0.f};
  STDECL ST(0);
  const unsigned pfc = __builtin_amdgcn_raw_buffer_load_b32(mk_rsrc((const void*)(pc0 & ~255ull), CODE_LEN), ((idx & 7) * 8 + W) * 1024 + (lane & 7) * 128, 0, 0);
  {
    const unsigned t5 = (tid >> 5) & 7;
    *(LAS unsigned*)LP(L0 + (t5 < 4 ? SY + t5 * 128 : (t5 - 4) * QST) + (tid & 31) * 4) = t5 < 4 ? 0u : 0x3f803f80u;
  }
  const unsigned a4 = L0 + SY + (lane & 3) * 4, a1 = L0 + SY + w * 4;
  asm volatile("" ::"s"(x), "s"(wqkv), "s"(wo), "s"(w1), "s"(b1), "s"(w2), "s"(b2), "s"(ws), "s"(out));
  BAR();

  if (W < 4) {
    const rsrc_t rx = mk_rsrc(x, S * D * 2);
    const u64 ep = __hip_atomic_load((const GAS u64*)(ws + WS_EPOCH), __ATOMIC_RELAXED, __HIP_MEMORY_SCOPE_AGENT);
    u32x4 xr[2];
#pragma unroll
    for (int rr = 0; rr < 2; rr++) xr[rr] = bload(rx, lane * 16, (rg * 8 + 2 * w + rr) * 1024);
    const unsigned pfw = __builtin_amdgcn_raw_buffer_load_b32(mk_rsrc(wqkv, D * 3 * D * 2), lane < 12 ? ((idx * 4 + sq) * 4 + w) * 3072 + (lane >> 2) * 1024 + hh * 512 + (lane & 3) * 128 : OOB, 0, 0);
    {
      f32x2 v[2][4];
      float p[2];
      auto red4 = [&](float a0, float a1v, float b0, float b1v, float (&o)[4]) {
        f32x2 t = swap16(a0, a1v), u = swap16(b0, b1v);
        t = swap32(t[0] + t[1], u[0] + u[1]);
        const float s = sum16(t[0] + t[1]);
#pragma unroll
        for (int k = 0; k < 4; k++) o[k] = __uint_as_float(__builtin_amdgcn_readlane(__float_as_uint(s), 16 * k));
      };
      float s1[2], s2[2], o[4];
#pragma unroll
      for (int rr = 0; rr < 2; rr++) {
        p[rr] = bflo(__builtin_amdgcn_readfirstlane(xr[rr][0]));
        f32x2 a = {0.f, 0.f}, b = {0.f, 0.f};
#pragma unroll
        for (int j = 0; j < 4; j++) { const f32x2 t = {bflo(xr[rr][j]), bfhi(xr[rr][j])}; v[rr][j] = t - bc2(p[rr]); a += v[rr][j]; b = fma2(v[rr][j], v[rr][j], b); }
        s1[rr] = a[0] + a[1]; s2[rr] = b[0] + b[1];
      }
      red4(s1[0], s1[1], s2[0], s2[1], o);
#pragma unroll
      for (int rr = 0; rr < 2; rr++) {
        const float m = o[rr] * (1.f / D);
        const float r = __builtin_amdgcn_rsqf(fmaxf(o[2 + rr] * (1.f / D) - m * m, 0.f) + EPS);
        *(LAS float*)LP(L0 + QPRE_S + (lane & 1) * 32 + (2 * w + rr) * 4) = (lane & 1) ? (m + p[rr]) * r : r;
        f32x2 a = {0.f, 0.f}, b = {0.f, 0.f};
#pragma unroll
        for (int j = 0; j < 4; j++) { v[rr][j] = fma2(v[rr][j], bc2(r), bc2(-m * r)); a += v[rr][j]; b = fma2(v[rr][j], v[rr][j], b); }
        s1[rr] = a[0] + a[1]; s2[rr] = b[0] + b[1];
      }
      red4(s1[0], s1[1], s2[0], s2[1], o);
#pragma unroll
      for (int rr = 0; rr < 2; rr++) {
        const float m2 = o[rr] * (1.f / D);
        const float r2 = __builtin_amdgcn_rsqf(fmaxf(o[2 + rr] * (1.f / D) - m2 * m2, 0.f) + EPS);
        u32x4 h;
#pragma unroll
        for (int j = 0; j < 4; j++) h[j] = pkv(fma2(v[rr][j], bc2(r2), bc2(-m2 * r2)));
        *(LAS u32x4*)LP(L0 + QPRE_X + (2 * w + rr) * 1088 + lane * 16) = h;
      }
    }
    {
      const u64 tag = (ep + 1) << 16;
      const u32x2 tg = {(unsigned)tag, (unsigned)(tag >> 32)};
      *(LAS u32x2*)LP(L0 + SY + Y_TAG) = tg;
      sy_put<Y_H>(a1, 1);
      const size_t o = w == 0 ? WS_C1 + blockIdx.x * 128 : w == 1 ? WS_C3 + (st * 8 + ct) * 128 : w == 2 ? WS_C4 + (st * 8 + ct) * 128 : WS_C2 + (blockIdx.x >> 1) * 128;
      if (lane == 0 && (w < 3 || (blockIdx.x & 1) == 0)) __hip_atomic_store((GAS u64*)(ws + o), tag, __ATOMIC_RELAXED, __HIP_MEMORY_SCOPE_AGENT);
    }
    ST(1);

    f32x4 aq[5], ak[5], av[5];
#pragma unroll
    for (int b = 0; b < 5; b++) { ak[b] = z4; av[b] = z4; aq[b] = z4; }
    const unsigned ax = L0 + c * 128 + ((g ^ (c >> 1)) << 4);
    const unsigned aw = L0 + 10240 + g * 3072 + (c >> 2) * 128 + ((w ^ ((c >> 3) | ((g & 1) << 1))) << 5) + (c & 3) * 8;
    bf16x8 xf[2][5];
    bf16x4 wl[2][3], wh[2][3];
    const lp pxa[2][2] = {{LP(ax), LP(ax ^ 64)}, {LP(ax + 2 * QST), LP((ax ^ 64) + 2 * QST)}}, pwa[2] = {LP(aw), LP(aw + 2 * QST)};
    auto RD = [&](int n, int i, int slot, int kk) {
      const lp px = pxa[slot >> 1][kk] + (slot & 1) * QST, pw = pwa[slot >> 1] + (slot & 1) * QST + kk * 12288;
      if (i == 0) wl[n][1] = ltr(pw + 1024);
      if (i == 1) wh[n][1] = ltr(pw + 1024 + 512);
      if (i == 2) xf[n][4] = l128(px + 8192);
      if (i == 3) wl[n][2] = ltr(pw + 2048);
      if (i == 4) wh[n][2] = ltr(pw + 2048 + 512);
      if (i == 5) wl[n][0] = ltr(pw);
      if (i == 6) wh[n][0] = ltr(pw + 512);
      if (i >= 7) xf[n][10 - i] = l128(px + (10 - i) * 2048);
    };
    auto MM = [&](int cur, int m) {
      const int t = 4 - m / 3;
      if (m % 3 == 0) ak[t] = mfma32(cat8(wl[cur][1], wh[cur][1]), xf[cur][t], ak[t]);
      if (m % 3 == 1) av[t] = mfma32(xf[cur][t], cat8(wl[cur][2], wh[cur][2]), av[t]);
      if (m % 3 == 2) aq[t] = mfma32(cat8(wl[cur][0], wh[cur][0]), xf[cur][t], aq[t]);
    };
    sy_wait<Y_FULL>(a4, 1);
    ST(2);
#pragma unroll
    for (int i = 0; i < 11; i++) RD(0, i, 0, 0);
    SCHED();
#pragma unroll
    for (int s = 0; s < 8; s++) {
      unsigned fl = 0;
#pragma unroll
      for (int m = 0; m < 14; m++) {
        MM(0, m); SCHED();
        if (m == 0 && s < 7) fl = *(volatile LAS unsigned*)(LP(a4) + Y_FULL);
        if (m >= 1 && m < 12) RD(1, m - 1, s & 3, 1);
        if (m == 12 && s < 4) { FENCE(); *(volatile LAS unsigned*)(LP(a1) + Y_FREE) = s + 1; }
        SCHED();
      }
      if (s == 1) { WAITV(0); sy_put<Y_HD>(a1, 1); }
      if (s == 3) ST(12);
      if (s == 5) ST(13);
      if (s < 7 && __builtin_expect(__any(fl < (unsigned)s + 2), 0)) sy_wait<Y_FULL>(a4, s + 2);
      MM(0, 14); SCHED();
#pragma unroll
      for (int m = 0; m < 15; m++) {
        MM(1, m); SCHED();
        if (m < 11 && s < 7) RD(0, m, (s + 1) & 3, 0);
        SCHED();
      }
    }
    sy_put<Y_QS>(a1, 1);
    ST(3);
    asm volatile("" ::"v"(pfc), "v"(pfw));
    f32x2 cq[2], ck[2];
#pragma unroll
    for (int j = 0; j < 4; j++) { cq[j >> 1][j & 1] = -dpp<0x150>(aq[0][j]); ck[j >> 1][j & 1] = -dpp<0x150>(ak[0][j]); }
    f32x2 cvt = swap16(av[0][0], av[0][0]);
    cvt = swap32(cvt[0], cvt[0]);
    const f32x2 cv = bc2(-cvt[0]);
    sy_wait<Y_ST>(a4, 1);
    u32x2 qf[4], kf[5], vf[5];
    {
      float sr[5], sm[5]; f32x4 vr[5], vm[5];
      const lp pcs = LP(L0 + QSTAT + (c >> 3) * 64 + (c & 7) * 4), prs = LP(L0 + QSTAT + (g >> 1) * 64 + (g & 1) * 16);
#pragma unroll
      for (int t = 0; t < 5; t++) {
        sr[t] = *(const LAS float*)(pcs + t * 128); sm[t] = *(const LAS float*)(pcs + t * 128 + 32);
        vr[t] = lf4(prs + t * 128); vm[t] = lf4(prs + t * 128 + 32);
      }
      SCHED();
#pragma unroll
      for (int t = 0; t < 5; t++) {
        const f32x2 r = bc2(sr[t]), m = bc2(sm[t]);
        kf[t][0] = pkv(fma2(r, lo2(ak[t]), m * ck[0])); kf[t][1] = pkv(fma2(r, hi2(ak[t]), m * ck[1]));
        if (t > 0) { qf[t - 1][0] = pkv(fma2(r, lo2(aq[t]), m * cq[0])); qf[t - 1][1] = pkv(fma2(r, hi2(aq[t]), m * cq[1])); }
        vf[t][0] = pkv(fma2(lo2(vr[t]), lo2(av[t]), lo2(vm[t]) * cv)); vf[t][1] = pkv(fma2(hi2(vr[t]), hi2(av[t]), hi2(vm[t]) * cv));
      }
    }
    u32x2 op[4];
    {
      const bool odd = c & 1;
      const bool cur0 = 4 * g + (c & 1) <= c, cur1 = 4 * g + (c & 1) + 2 <= c;
      constexpr float SC = 0.25f * 1.4426950408889634f;
      f32x4 sp[4], su[4];
#pragma unroll
      for (int t = 0; t < 4; t++) { sp[t] = mfma16(kf[t], qf[t], z4); su[t] = mfma16(kf[t + 1], qf[t], z4); }
      float l0[4], l1[4], mx[4];
#pragma unroll
      for (int t = 0; t < 4; t++) {
        const float p0 = odd ? sp[t][1] : sp[t][0], p1 = odd ? sp[t][3] : sp[t][2];
        const float u0 = odd ? su[t][1] : su[t][0], u1 = odd ? su[t][3] : su[t][2];
        l0[t] = cur0 ? u0 : p0; l1[t] = cur1 ? u1 : p1;
        if (t == 0 && st == 0) { l0[t] = cur0 ? l0[t] : -4e9f; l1[t] = cur1 ? l1[t] : -4e9f; }
        mx[t] = fmaxf(l0[t], l1[t]);
      }
      {
        const f32x2 a = swap16(mx[0], mx[1]), b = swap16(mx[2], mx[3]);
        const f32x2 d = swap32(fmaxf(a[0], a[1]), fmaxf(b[0], b[1]));
        const float m = fmaxf(d[0], d[1]) * -SC;
        const f32x2 e = swap32(m, m), f0 = swap16(e[0], e[0]), f1 = swap16(e[1], e[1]);
        mx[0] = f0[0]; mx[1] = f0[1]; mx[2] = f1[0]; mx[3] = f1[1];
      }
      const u32x4 ones = {0x3f803f80u, 0x3f803f80u, 0x3f803f80u, 0x3f803f80u};
      const unsigned keep = odd ? 0xffff0000u : 0x0000ffffu;
      const unsigned k00 = cur0 ? 0u : keep, k01 = cur1 ? 0u : keep, k10 = cur0 ? keep : 0u, k11 = cur1 ? keep : 0u;
      f32x4 o[4], den[4];
#pragma unroll
      for (int t = 0; t < 4; t++) {
        const float e0 = __builtin_amdgcn_exp2f(__builtin_fmaf(l0[t], SC, mx[t])), e1 = __builtin_amdgcn_exp2f(__builtin_fmaf(l1[t], SC, mx[t]));
        const unsigned b0 = pk2(e0, e0), b1v = pk2(e1, e1);
        const u32x4 pu = {b0 & k00, b1v & k01, b0 & k10, b1v & k11}, vv = {vf[t][0], vf[t][1], vf[t + 1][0], vf[t + 1][1]};
        o[t] = mfma32(as_bf(vv), as_bf(pu), z4);
        den[t] = mfma32(as_bf(ones), as_bf(pu), z4);
      }
#pragma unroll
      for (int t = 0; t < 4; t++) {
        const f32x2 inv = bc2(__builtin_amdgcn_rcpf(den[t][0]));
        op[t][0] = pkv(lo2(o[t]) * inv); op[t][1] = pkv(hi2(o[t]) * inv);
      }
    }
    {
      const rsrc_t ro = mk_rsrc(ws + WS_O, O_BYTES);
#pragma unroll
      for (int t = 0; t < 4; t += 2) {
        const auto r0 = __builtin_amdgcn_permlane16_swap(op[t][0], op[t + 1][0], false, false);
        const auto r1 = __builtin_amdgcn_permlane16_swap(op[t][1], op[t + 1][1], false, false);
        const u32x4 v = {r0[0], r1[0], r0[1], r1[1]};
        bstore1(v, ro, (g & 1) * 16384 + ((g >> 1) * 16 + c) * 16, ((4 * st + t) * 16 + 2 * ct + (w >> 1)) * 1024 + (w & 1) * 512);
      }
    }
    ST(11);
    WAITV(0);
    if (last_of(L0 + SY + Y_N2, 4, lane) && lane < 4) gadd(ws + WS_C2 + (((st >> 1) * 4 + lane) * 2 + (st & 1)) * 128);
    ST(4);

    const rsrc_t rgg = mk_rsrc(ws + WS_G, G_BYTES), rw2 = mk_rsrc(w2, F * S * 2);
    f32x4 acc[4][4];
#pragma unroll
    for (int i = 0; i < 4; i++)
#pragma unroll
      for (int j = 0; j < 4; j++) acc[i][j] = z4;
    const unsigned vg = lane * 16 + (4 * ct * 43 + 11 * w) * 1024;
    const unsigned vw2 = (352 * w + r8) * 4096 + st * 128 + pc * 16;
    const unsigned lw = L0 + P2STG + w * 24576;
    const unsigned aw0 = lw + r8 * 128 + ((((pc >> 1) ^ ((r8 >> 1) & 1)) << 5) | ((pc & 1) << 4));
    const lp pw0 = LP(aw0), pw1 = LP(aw0 ^ 64);
    const unsigned ar = lw + (8 * g + (c >> 2)) * 128 + ((((c >> 3) | ((g & 1) << 1))) << 5) + (c & 3) * 8;
    u32x4 wr[11][4], gf[11][4];
    auto LW = [&](int k) {
#pragma unroll
      for (int j = 0; j < 4; j++) wr[k][j] = bload(rw2, (k == 10 && w == 3) ? OOB : vw2, (32 * k + 8 * j) * 4096);
      SCHED();
    };
    auto LG1 = [&](int k, int j) { gf[k][j] = bload1(rgg, (k == 10 && w == 3) ? OOB : vg, j * 43 * 1024 + k * 1024); };
    auto SW = [&](int k, int slot) {
#pragma unroll
      for (int j = 0; j < 4; j++) *(LAS u32x4*)(((j & 1) ? pw1 : pw0) + j * 1024 + slot * 4096) = wr[k][j];
    };
    lp pr[4];
#pragma unroll
    for (int j = 0; j < 4; j++) pr[j] = LP(ar ^ (((w + j) & 3) << 5));
    bf16x8 tw[2][4];
    bf16x8 tk[4][4];
    auto TR = [&](bf16x8 (&t)[4], int slot) {
#pragma unroll
      for (int j = 0; j < 4; j++) t[j] = ltr2(pr[j] + slot * 4096, 512);
      SCHED();
    };
    if (idx >= 22) {
      sy_wait<Y_OK2>(a4, 1);
      unsigned pa = 0;
#pragma unroll
      for (int n = 0; n < 3; n++) { const int m = (idx - 22) * 4 + w + 40 * n; pa ^= __builtin_amdgcn_raw_buffer_load_b32(rw2, ((m * 2 + hh) * 8 + r8) * 4096 + sq * 1024 + pc * 128, 0, 0); }
      asm volatile("" ::"v"(pa));
    }
    sy_wait<Y_QS>(a4, 1);
    sy_wait<Y_ISS>(a4, 1);
    ST(5);
    LW(7); LW(8); LW(9); LW(10);
    SW(7, 0); TR(tk[0], 0); LW(0);
    SW(8, 1); TR(tk[1], 1); LW(1);
    SW(9, 2); TR(tk[2], 2); LW(2);
    SW(10, 3); TR(tk[3], 3); LW(3);
    SW(0, 0); TR(tw[0], 0); LW(4);
    SW(1, 1); LW(5);
    SW(2, 2); LW(6);
    SW(3, 3); SW(4, 4); SW(5, 5); SW(6, 0);
    ST(6);
    sy_wait<Y_OK3>(a4, 1);
    ST(7);
    constexpr int GD = 4;
#pragma unroll
    for (int k = 0; k < GD; k++)
#pragma unroll
      for (int j = 0; j < 4; j++) LG1(k, j);
    SCHED();
    u32x4 bv[2];
#pragma unroll
    for (int k = 0; k < 10; k++) {
      if (k + GD == 11) {
        sy_wait<Y_OK4>(a4, 1);
        const rsrc_t rb = mk_rsrc(ws + WS_BASE, S * D * 2);
#pragma unroll
        for (int i = 0; i < 2; i++) bv[i] = bload1(rb, lane * 16, ((4 * st + w) * 16 + 2 * ct + i) * 1024);
      }
#pragma unroll
      for (int i = 0; i < 4; i++) {
#pragma unroll
        for (int j = 0; j < 4; j++) {
          acc[i][j] = mfma32(as_bf(gf[k][i]), k < 7 ? tw[k & 1][j] : tk[k < 7 ? 0 : k - 7][j], acc[i][j]);
          if (j == 0 && k + 1 < 7) { SCHED(); tw[(k + 1) & 1][i] = ltr2(pr[i] + ((k + 1) % 6) * 4096, 512); SCHED(); }
        }
        SCHED();
        if (k + GD < 11) LG1(k + GD, i);
        SCHED();
      }
    }
#pragma unroll
    for (int jj = 1; jj <= 4; jj++) {
#pragma unroll
      for (int i = 0; i < 4; i++) {
        acc[i][jj & 3] = mfma32(as_bf(gf[10][i]), tk[3][jj & 3], acc[i][jj & 3]); SCHED();
        if (jj >= 2) *(LAS f32x4*)(LP(lw + lane * 16) + ((jj - 2) * 4 + i) * 1024) = acc[i][jj - 1];
        SCHED();
      }
    }
    ST(8);
    BAR();
    ST(9);
    f32x4 u[4];
#pragma unroll
    for (int i = 0; i < 4; i++) {
      u[i] = acc[i][0];
#pragma unroll
      for (int j = 1; j < 4; j++) u[i] += lf4(LP(L0 + P2STG + ((w - j) & 3) * 24576 + lane * 16) + ((j - 1) * 4 + i) * 1024);
    }
    const int srow = 64 * st + 16 * w + c;
#pragma unroll
    for (int i = 0; i < 4; i += 2) {
      float v[8];
#pragma unroll
      for (int r = 0; r < 4; r++) { const f32x2 sw = swap16(u[i][r], u[i + 1][r]); v[r] = sw[0]; v[4 + r] = sw[1]; }
      u32x4 o;
#pragma unroll
      for (int j = 0; j < 4; j++) o[j] = pk2(v[2 * j] + bflo(bv[i >> 1][j]), v[2 * j + 1] + bfhi(bv[i >> 1][j]));
      *(u32x4*)(out + srow * D + 64 * ct + 16 * i + 16 * (g & 1) + 8 * (g >> 1)) = o;
    }
    ST(10);
  } else {
    u64 tag;
    {
      const u32x4 sx = mk_srd(x, S * D * 2), sw = mk_srd(wqkv, D * 3 * D * 2);
      const unsigned vx0 = (unsigned)((64 * st - 16 + 8 * w + r8) * 1024 + ((pc ^ (((w & 1) << 2) | (r8 >> 1))) << 4));
      const unsigned vx1 = vx0 + 32768u;
      const unsigned vw = (8 * w + r8) * 3072 + ct * 128 + ((((pc >> 1) ^ (((r8 >> 1) & 1) | ((w & 1) << 1))) << 5) | ((pc & 1) << 4));
      auto head = [&](int s) { glds16x3(sw, vw, s * 64 * 3072, L0 + (s & 3) * QST + 10240 + w * 3072); };
      auto rest = [&](int s) {
        const unsigned b = L0 + (s & 3) * QST + w * 1024;
        if (lane >= 8 || w != 0) glds16(sx, vx0, s * 128, b);
        glds16(sx, vx1, s * 128, b + 4096);
        if (w < 2) glds16(sx, vx1, s * 128 + 32768, b + 8192);
        glds16x3(sw, vw, s * 64 * 3072 + 32 * 3072, b + 10240 + 12288 + w * 2048);
      };
#pragma unroll 1
      for (int s = 0; s < 3; s++) {
        head(s);
        if (s == 2) { WAITV(11); sy_put<Y_FULL>(a1, 1); }
        rest(s);
      }
      char* const c1p = ws + WS_C1 + ((rg >> 4) * 16 + c) * 128;
      u64 chk = 0;
      {
        WAITV(8); sy_put<Y_FULL>(a1, 2);
        sy_wait<Y_H>(a4, 1);
        bf16x8 tv[2];
#pragma unroll
        for (int n = 0; n < 2; n++) tv[n] = ltr2(LP(L0 + QPRE_X + (c >> 2) * 1088 + (w * 4 + g) * 32 + (c & 3) * 8) + n * 512, 4 * 1088);
        const u32x4 sv = *(const LAS u32x4*)LP(L0 + QPRE_S + (lane & 3) * 16);
        const u32x2 tg = *(const LAS u32x2*)LP(L0 + SY + Y_TAG);
        const rsrc_t rht = mk_rsrc(ws + WS_HT, HT_BYTES);
#pragma unroll
        for (int n = 0; n < 2; n++) bstore1(__builtin_bit_cast(u32x4, tv[n]), rht, g * 65536 + (rg >> 2) * 1024 + ((rg & 3) * 16 + c) * 16, (4 * w + n * 16) * 65536);
        if (w == 3) bstore1(sv, mk_rsrc(ws + WS_STAT, S * 8), lane < 4 ? rg * 64 + lane * 16 : OOB, 0);
        if (w == 2 && lane < 16) asm volatile("global_atomic_add_x2 %0, %1, %0, off sc0" : "+v"(chk) : "v"(c1p) : "memory");
        tag = ((u64)(unsigned)__builtin_amdgcn_readfirstlane(tg[1]) << 32) | (unsigned)__builtin_amdgcn_readfirstlane(tg[0]);
        ST(8);
      }
      head(3); rest(3);
      int ann = 2;
      unsigned fr = *(volatile LAS unsigned*)(LP(a4) + Y_FREE);
#pragma unroll 1
      for (int s = 4; s < 8; s++) {
        if (__any(fr < (unsigned)(s - 3))) {
          WAITV(8); sy_put<Y_FULL>(a1, s - 1); ann = s - 1;
          fr = *(volatile LAS unsigned*)(LP(a4) + Y_FREE);
          if (__any(fr < (unsigned)(s - 3))) { WAITV(0); sy_put<Y_FULL>(a1, s); ann = s; sy_wait<Y_FREE>(a4, s - 3); }
        }
        head(s);
        if (s == 4) WAITV(13); else WAITV(11);
        if (ann < s - 1) { ann = s - 1; sy_put<Y_FULL>(a1, ann); }
        if (s == 5) {
          if (w == 2) {
            asm volatile("" : "+v"(chk));
            if (__any(lane < 16 && (chk >> 16) != (tag >> 16))) gwait<2>(c1p, lane < 16, ~0xffffull, tag);
            sy_wait<Y_HD>(a4, 1);
          }
          if (last_of(L0 + SY + Y_N1, 4, lane) && lane < 16) gadd(c1p);
          ST(9);
        }
        fr = *(volatile LAS unsigned*)(LP(a4) + Y_FREE);
        rest(s);
      }
    }
    ST(1);
    const bool c1on = lane < (w == 3 ? 16 : 4);
    const char* const c1w = ws + WS_C1 + (((w == 3 ? 0 : 4 * w) + c) * 16 + (blockIdx.x & 15)) * 128;
    u64 c1v = tag + 16;
    if (c1on) { c1v = 0; asm volatile("global_atomic_add_x2 %0, %1, %0, off sc0" : "+v"(c1v) : "v"(c1w) : "memory"); }
    u32x4 stv4;
    auto c1_done = [&]() {
      asm volatile("" : "+v"(c1v));
      if (__any(c1v != tag + 16)) gwait<2>(c1w, c1on, ~0ull, tag + 16);
      if (w == 3) {
        stv4 = bload1(mk_rsrc(ws + WS_STAT, S * 8), (unsigned)((8 * st - 2) * 64 + min(lane, 39) * 16), 0);
        if (blockIdx.x == 0 && lane == 0) __hip_atomic_store((GAS u64*)(ws + WS_EPOCH), tag >> 16, __ATOMIC_RELAXED, __HIP_MEMORY_SCOPE_AGENT);
      }
    };
    auto put_stats = [&]() { if (w == 3) { *(LAS u32x4*)LP(L0 + QSTAT + min(lane, 39) * 16) = stv4; sy_put<Y_ST>(a4, 1); } };
    if (idx < 22) {
      const int ft = (22 * sq + idx) >> 2, dt = hh * 4 + ((22 * sq + idx) & 3);
      const rsrc_t rht = mk_rsrc(ws + WS_HT, HT_BYTES), r1 = mk_rsrc(w1, S * F * 2);
      constexpr int DEP = 3;
      u32x4 wr[16][4], fb[16][4];
      const unsigned vw = (ft == 21 && pc >= 4) ? OOB : (512 * w + r8) * 2752 + ft * 128 + pc * 16, vb = lane * 16 + (4 * dt * 64 + 16 * w) * 1024;
      const unsigned lb = L0 + G1STG + w * 4096;
      const unsigned aw0 = lb + r8 * 128 + ((((pc >> 1) ^ ((r8 >> 1) & 1)) << 5) | ((pc & 1) << 4));
      const lp pw0 = LP(aw0), pw1 = LP(aw0 ^ 64);
      const unsigned ar = lb + (8 * g + (c >> 2)) * 128 + ((((c >> 3) | ((g & 1) << 1))) << 5) + (c & 3) * 8;
      lp pe[4];
#pragma unroll
      for (int e = 0; e < 4; e++) pe[e] = LP(ar ^ (e << 5));
      auto LW = [&](int k, int j) { wr[k][j] = bload(r1, vw, (32 * k + 8 * j) * 2752); };
      auto LB = [&](int k, int j) { fb[k][j] = bload1(rht, vb, ((w + j) & 3) * 65536 + k * 1024); };
      auto SW = [&](int k, int j) { *(LAS u32x4*)(((j & 1) ? pw1 : pw0) + j * 1024) = wr[k][j]; };
      LW(0, 0); LW(0, 1);
      WAITV(11); sy_put<Y_FULL>(a1, 7);
      LW(0, 2); LW(0, 3); LW(1, 0); LW(1, 1);
      WAITV(7); sy_put<Y_FULL>(a1, 8);
      LW(1, 2); LW(1, 3);
      u32x2 bb[4];
      {
        const rsrc_t rb1 = mk_rsrc(b1, F * 2);
#pragma unroll
        for (int i = 0; i < 4; i++) bb[i] = __builtin_amdgcn_raw_buffer_load_b64(rb1, (64 * ft + 4 * g) * 2 + i * 32, 0, 0);
      }
      LW(2, 0); LW(2, 1); LW(2, 2); LW(2, 3);
      WAITV(16);
      ST(2);
      c1_done();
      ST(3);
#pragma unroll
      for (int k = 0; k < DEP; k++) { LB(k, 0); LB(k, 1); LB(k, 2); LB(k, 3); }
      put_stats();
      f32x4 acc[4][4];
#pragma unroll
      for (int i = 0; i < 4; i++) {
        acc[i][0] = (f32x4){bflo(bb[i][0]), bfhi(bb[i][0]), bflo(bb[i][1]), bfhi(bb[i][1])};
#pragma unroll
        for (int j = 1; j < 4; j++) acc[i][j] = z4;
      }
      sy_wait<Y_FULL>(a4, 4);
      bf16x4 al[2][4], ah[2][4];
      SW(0, 0); SW(0, 1); SW(0, 2); SW(0, 3);
#pragma unroll
      for (int e = 0; e < 4; e++) { al[0][e] = ltr(pe[e]); ah[0][e] = ltr(pe[e] + 512); }
      SCHED();
#pragma unroll
      for (int k = 0; k < 15; k++) {
#pragma unroll
        for (int m = 0; m < 16; m++) {
          const int i = m & 3, j = m >> 2;
          acc[i][j] = mfma32(cat8(al[k & 1][i], ah[k & 1][i]), as_bf(fb[k][j]), acc[i][j]);
          SCHED();
          if (m < 4) SW(k + 1, m);
          else if (m >= 6 && m < 14) { const int e = (m - 6) >> 1; if (m & 1) ah[(k + 1) & 1][e] = ltr(pe[e] + 512); else al[(k + 1) & 1][e] = ltr(pe[e]); }
          if (i == 1 && k + DEP < 16) LW(k + DEP, j);
          if (i == 3 && k + DEP < 16) LB(k + DEP, j);
          SCHED();
        }
        if (k == 8) sy_wait<Y_QS>(a4, 1);
        if (k + DEP == 15) { sy_put<Y_ISS>(a1, 1); __builtin_amdgcn_s_setprio(3); }
      }
#pragma unroll
      for (int jj = 1; jj <= 4; jj++) {
#pragma unroll
        for (int i = 0; i < 4; i++) {
          acc[i][jj & 3] = mfma32(cat8(al[1][i], ah[1][i]), as_bf(fb[15][jj & 3]), acc[i][jj & 3]); SCHED();
          if (jj >= 2) *(LAS f32x4*)((jj < 4 ? LP(L0 + E2SCR + w * 8192 + lane * 16) + (jj - 2) * 4096 : LP(lb + lane * 16)) + i * 1024) = acc[i][jj - 1];
          SCHED();
        }
      }
      sy_put<Y_E2>(a1, 1);
      ST(4);
      const lp pin[3] = {LP(L0 + E2SCR + ((w - 1) & 3) * 8192 + lane * 16), LP(L0 + E2SCR + ((w - 2) & 3) * 8192 + 4096 + lane * 16), LP(L0 + G1STG + ((w - 3) & 3) * 4096 + lane * 16)};
      sy_wait<Y_E2>(a4, 1);
      ST(5);
      f32x4 pt[4][3];
#pragma unroll
      for (int i = 0; i < 4; i++)
#pragma unroll
        for (int j = 0; j < 3; j++) pt[i][j] = lf4(pin[j] + i * 1024);
      SCHED();
      u32x2 gp[4];
#pragma unroll
      for (int i = 0; i < 4; i++) {
        const f32x4 u = (acc[i][0] + pt[i][0]) + (pt[i][1] + pt[i][2]);
#pragma unroll
        for (int hf = 0; hf < 2; hf++) {
          constexpr float K1 = -2.f * 0.7978845608028654f * 1.4426950408889634f, K2 = K1 * 0.044715f;
          const f32x2 t = hf ? hi2(u) : lo2(u);
          const f32x2 z = t * fma2(t * t, bc2(K2), bc2(K1));
          f32x2 e = {__builtin_amdgcn_exp2f(z[0]), __builtin_amdgcn_exp2f(z[1])};
          e += bc2(1.f);
          const f32x2 rc = {__builtin_amdgcn_rcpf(e[0]), __builtin_amdgcn_rcpf(e[1])};
          gp[i][hf] = pkv(t * rc);
        }
      }
      const rsrc_t rgg = mk_rsrc(ws + WS_G, G_BYTES);
#pragma unroll
      for (int i = 0; i < 4; i += 2) {
        const auto r0 = __builtin_amdgcn_permlane16_swap(gp[i][0], gp[i + 1][0], false, false);
        const auto r1v = __builtin_amdgcn_permlane16_swap(gp[i][1], gp[i + 1][1], false, false);
        const u32x4 v = {r0[0], r1v[0], r0[1], r1v[1]};
        if (i == 0 || ft < 21) bstore1(v, rgg, (2 * (g & 1) + (g >> 1)) * 256 + c * 16, ((4 * dt + w) * 43 + 2 * ft + (i >> 1)) * 1024);
      }
      ST(6);
      WAITV(0);
      if (last_of(L0 + SY + Y_N3, 4, lane) && lane < 32) gadd(ws + WS_C3 + (lane * 8 + dt) * 128);
      ST(7);
    } else {
      WAITV(9); sy_put<Y_FULL>(a1, 7);
      WAITV(1); sy_put<Y_FULL>(a1, 8);
      WAITV(0);
      c1_done();
      unsigned pv[6] = {0, 0, 0, 0, 0, 0};
      {
        const rsrc_t r1 = mk_rsrc(w1, S * F * 2);
        const unsigned c0 = ((22 * sq) >> 2) * 128;
        const int id = (idx - 22) * 4 + w;
#pragma unroll
        for (int n = 0; n < 6; n++) {
          const int u = id + 40 * n + 16;
          const unsigned row = (u & 3) * 512 + (u >> 2) * 8 + r8;
          pv[n] = __builtin_amdgcn_raw_buffer_load_b32(r1, pc < 7 ? ((row * 2752 + c0) & ~127u) + pc * 128 : OOB, 0, 0);
          if (n == 0) put_stats();
        }
      }
      if (idx < 30) {
        const int so = sq * 4 + ((idx - 22) >> 1), dq = hh * 2 + (idx & 1);
        const u32x4 swo = mk_srd(wo, D * D * 2);
        const unsigned vw = (8 * w + r8) * 1024 + dq * 256 + ((((pc >> 1) ^ (((r8 >> 1) & 1) | ((w & 1) << 1))) << 5) | ((pc & 1) << 4));
        sy_wait<Y_QS>(a4, 1);
#pragma unroll 1
        for (int n = 0; n < 16; n++) {
          glds16(swo, vw, n * 32768, L0 + (n * 4 + w) * 1024);
          glds16(swo, vw, n * 32768 + 128, L0 + 65536 + (n * 4 + w) * 1024);
        }
        const rsrc_t rx = mk_rsrc(x, S * D * 2), ro = mk_rsrc(ws + WS_O, O_BYTES), rb = mk_rsrc(ws + WS_BASE, S * D * 2);
        const int srow = 128 * so + 32 * w + c;
        const float bias0 = bflo(b2[srow]), bias1 = bflo(b2[srow + 16]);
        gwait<2>(ws + WS_C2 + ((so * 4 + dq) * 2 + (w >> 1)) * 128, lane == 0, ~0ull, tag + 8);
        WAITV(0);
        asm volatile("" ::"v"(pv[0]), "v"(pv[1]), "v"(pv[2]), "v"(pv[3]), "v"(pv[4]), "v"(pv[5]));
        sy_put<Y_WO>(a1, 1);
        ST(3);
        u32x4 of[16][2];
        const unsigned vo = lane * 16 + (8 * so + 2 * w) * 16384;
        constexpr int OD = 8;
#pragma unroll
        for (int k = 0; k < OD; k++) { of[k][0] = bload1(ro, vo, k * 1024); of[k][1] = bload1(ro, vo, 16384 + k * 1024); }
        sy_wait<Y_WO>(a4, 1);
        const unsigned ar = L0 + (8 * g + (c >> 2)) * 128 + ((((c >> 3) | ((g & 1) << 1))) << 5) + (c & 3) * 8;
        lp pe[8];
#pragma unroll
        for (int e = 0; e < 8; e++) pe[e] = LP((ar ^ ((e & 3) << 5)) + (e >> 2) * 65536);
        f32x4 acc[8][2];
#pragma unroll
        for (int i = 0; i < 8; i++) { acc[i][0] = (f32x4){bias0, bias0, bias0, bias0}; acc[i][1] = (f32x4){bias1, bias1, bias1, bias1}; }
        bf16x4 tl[2][8], th[2][8];
#pragma unroll
        for (int e = 0; e < 8; e++) { tl[0][e] = ltr(pe[e]); th[0][e] = ltr(pe[e] + 512); }
        u32x4 xv[2][4];
        SCHED();
#pragma unroll
        for (int k = 0; k < 16; k++) {
#pragma unroll
          for (int e = 0; e < 8; e++) {
            acc[e][0] = mfma32(cat8(tl[k & 1][e], th[k & 1][e]), as_bf(of[k][0]), acc[e][0]); SCHED();
            if (k < 15) tl[(k + 1) & 1][e] = ltr(pe[e] + (k + 1) * 4096);
            SCHED();
            acc[e][1] = mfma32(cat8(tl[k & 1][e], th[k & 1][e]), as_bf(of[k][1]), acc[e][1]); SCHED();
            if (k < 15) th[(k + 1) & 1][e] = ltr(pe[e] + (k + 1) * 4096 + 512);
            SCHED();
          }
          if (k + OD < 16) { of[k + OD][0] = bload1(ro, vo, (k + OD) * 1024); of[k + OD][1] = bload1(ro, vo, 16384 + (k + OD) * 1024); }
          if (k >= 8 && k < 12) {
#pragma unroll
            for (int j = 0; j < 2; j++) xv[j][k - 8] = bload(rx, (srow * D + 128 * dq + 16 * (g & 1) + 8 * (g >> 1)) * 2, j * 16384 + (k - 8) * 64);
          }
          if (k == 7) sy_put<Y_OK2>(a1, 1);
          if (k == 14) { FENCE(); sy_put<Y_ISS>(a1, 1); __builtin_amdgcn_s_setprio(3); }
        }
        ST(4);
#pragma unroll
        for (int j = 0; j < 2; j++)
#pragma unroll
          for (int i = 0; i < 4; i++) {
            float v[8];
#pragma unroll
            for (int r = 0; r < 4; r++) { const f32x2 sw = swap16(acc[2 * i][j][r], acc[2 * i + 1][j][r]); v[r] = sw[0]; v[4 + r] = sw[1]; }
            u32x4 o;
#pragma unroll
            for (int q = 0; q < 4; q++) o[q] = pk2(v[2 * q] + bflo(xv[j][i][q]), v[2 * q + 1] + bfhi(xv[j][i][q]));
            bstore1(o, rb, lane * 16, ((8 * so + 2 * w + j) * 16 + 4 * dq + i) * 1024);
          }
        ST(6);
        WAITV(0);
        if (last_of(L0 + SY + Y_N3, 4, lane) && lane < 4) gadd(ws + WS_C4 + ((2 * so + (lane >> 1)) * 8 + 2 * dq + (lane & 1)) * 128);
        ST(7);
      } else {
        asm volatile("" ::"v"(pv[0]), "v"(pv[1]), "v"(pv[2]), "v"(pv[3]), "v"(pv[4]), "v"(pv[5]));
        sy_put<Y_OK2>(a1, 1);
        sy_put<Y_ISS>(a1, 1);
      }
    }
    asm volatile("" ::"v"(pfc));
    if (w == 0) { gwait<2>(ws + WS_C3 + (st * 8 + ct) * 128, lane == 0, ~0ull, tag + 22); sy_put<Y_OK3>(a4, 1); }
    if (w == 1) { gwait<4>(ws + WS_C4 + (st * 8 + ct) * 128, lane == 0, ~0ull, tag + 1); sy_put<Y_OK4>(a4, 1); }
    BAR();
  }
  STFLUSH();
}

extern "C" void kernel_launch(void* const* d_in, const int* in_sizes, int n_in, void* d_out, int out_size, void* d_ws, size_t ws_size, hipStream_t stream) {
  (void)hipFuncSetAttribute((const void*)fused, hipFuncAttributeMaxDynamicSharedMemorySize, LDS_BYTES);
  fused<<<256, 512, LDS_BYTES, stream>>>((const u16*)d_in[0], (const u16*)d_in[1], (const u16*)d_in[2], (const u16*)d_in[4], (const u16*)d_in[5],
                                         (const u16*)d_in[6], (const u16*)d_in[7], (char*)d_ws, (u16*)d_out);
}
```

```cpp
#include <hip/hip_runtime.h>
#include <stdint.h>

#define CODE_LEN 38912

typedef unsigned short u16;
typedef unsigned long long u64;
typedef short s16x4 __attribute__((ext_vector_type(4)));
typedef __bf16 bf16x2 __attribute__((ext_vector_type(2)));
typedef __bf16 bf16x4 __attribute__((ext_vector_type(4)));
typedef __bf16 bf16x8 __attribute__((ext_vector_type(8)));
typedef float f32x2 __attribute__((ext_vector_type(2)));
typedef float f32x4 __attribute__((ext_vector_type(4)));
typedef unsigned u32x2 __attribute__((ext_vector_type(2)));
typedef unsigned u32x4 __attribute__((ext_vector_type(4)));
#define LAS __attribute__((address_space(3)))
#define GAS __attribute__((address_space(1)))
#define DI __device__ __forceinline__

constexpr int S = 2048, D = 512, F = 1376;
constexpr float EPS = 1e-5f;
constexpr unsigned HT_BYTES = 32u * 64u * 1024u;
constexpr unsigned G_BYTES = 32u * 43u * 1024u;
constexpr unsigned O_BYTES = 128u * 16u * 1024u;
constexpr size_t WS_EPOCH = 0;
constexpr size_t WS_C1 = 4096;
constexpr size_t WS_C2 = WS_C1 + 128 * 128;
constexpr size_t WS_C3 = WS_C2 + 256 * 128;
constexpr size_t WS_C4 = WS_C3 + 256 * 128;
constexpr size_t WS_STAT = WS_C4 + 256 * 128;
constexpr size_t WS_HT = WS_STAT + 16384;
constexpr size_t WS_G = WS_HT + HT_BYTES;
constexpr size_t WS_O = WS_G + G_BYTES;
constexpr size_t WS_BASE = WS_O + O_BYTES;
constexpr int SC1 = 16;
constexpr unsigned OOB = 0x80000000u;

typedef __amdgpu_buffer_rsrc_t rsrc_t;
DI rsrc_t mk_rsrc(const void* p, unsigned bytes) { return __builtin_amdgcn_make_buffer_rsrc((void*)p, 0, bytes, 0x00020000); }
DI u32x4 mk_srd(const void* p, unsigned bytes) {
  const uint64_t a = (uint64_t)p;
  u32x4 r = {(unsigned)a, (unsigned)(a >> 32) & 0xffffu, bytes, 0x00020000u};
  return r;
}
DI void glds16(u32x4 srd, unsigned voff, unsigned soff, unsigned lds) {
  asm volatile("s_mov_b32 m0, %3\n\ts_nop 0\n\tbuffer_load_dwordx4 %0, %1, %2 offen lds" ::"v"(voff), "s"(srd), "s"(soff), "s"(lds) : "memory");
}
DI void glds16x3(u32x4 srd, unsigned voff, unsigned soff, unsigned lds) {
  asm volatile("s_mov_b32 m0, %3\n\ts_nop 0\n\tbuffer_load_dwordx4 %0, %1, %2 offen lds\n\tbuffer_load_dwordx4 %0, %1, %2 offen offset:1024 lds\n\t"
               "buffer_load_dwordx4 %0, %1, %2 offen offset:2048 lds" ::"v"(voff), "s"(srd), "s"(soff), "s"(lds) : "memory");
}
#define WAITV(n) asm volatile("s_waitcnt vmcnt(" #n ")" ::: "memory")
#define FENCE() asm volatile("" ::: "memory")
#define SCHED() __builtin_amdgcn_sched_barrier(0)
#define BAR() do { asm volatile("s_waitcnt lgkmcnt(0)" ::: "memory"); __builtin_amdgcn_s_barrier(); FENCE(); } while (0)

typedef LAS char* lp;
DI lp LP(unsigned a) { return (lp)(uintptr_t)a; }
DI bf16x8 cat8(bf16x4 a, bf16x4 b) { return __builtin_shufflevector(a, b, 0, 1, 2, 3, 4, 5, 6, 7); }
DI bf16x8 l128(lp p) { return *(const LAS bf16x8*)p; }
DI f32x4 lf4(lp p) { return *(const LAS f32x4*)p; }
DI bf16x4 ltr(lp p) { return __builtin_amdgcn_ds_read_tr16_b64_v4bf16((LAS bf16x4*)p); }
DI bf16x8 ltr2(lp p, int d) { return cat8(ltr(p), ltr(p + d)); }
DI f32x4 mfma32(bf16x8 a, bf16x8 b, f32x4 c) { return __builtin_amdgcn_mfma_f32_16x16x32_bf16(a, b, c, 0, 0, 0); }
DI f32x4 mfma16(u32x2 a, u32x2 b, f32x4 c) { return __builtin_amdgcn_mfma_f32_16x16x16bf16_1k(__builtin_bit_cast(s16x4, a), __builtin_bit_cast(s16x4, b), c, 0, 0, 0); }
DI unsigned pkv(f32x2 f) { return __builtin_bit_cast(unsigned, __builtin_convertvector(f, bf16x2)); }
DI unsigned pk2(float a, float b) { f32x2 f = {a, b}; return pkv(f); }
DI f32x2 lo2(f32x4 v) { return __builtin_shufflevector(v, v, 0, 1); }
DI f32x2 hi2(f32x4 v) { return __builtin_shufflevector(v, v, 2, 3); }
DI f32x2 fma2(f32x2 a, f32x2 b, f32x2 c) { return __builtin_elementwise_fma(a, b, c); }
DI f32x2 bc2(float a) { f32x2 r = {a, a}; return r; }
DI float bflo(unsigned u) { return __uint_as_float(u << 16); }
DI float bfhi(unsigned u) { return __uint_as_float(u & 0xffff0000u); }
template <int CTRL> DI float dpp(float v) { return __uint_as_float(__builtin_amdgcn_update_dpp(0u, __float_as_uint(v), CTRL, 0xf, 0xf, true)); }
DI float sum16(float v) { v += dpp<0xB1>(v); v += dpp<0x4E>(v); v += dpp<0x141>(v); v += dpp<0x140>(v); return v; }
DI f32x2 swap16(float a, float b) { auto r = __builtin_amdgcn_permlane16_swap(__float_as_uint(a), __float_as_uint(b), false, false); f32x2 o = {__uint_as_float(r[0]), __uint_as_float(r[1])}; return o; }
DI f32x2 swap32(float a, float b) { auto r = __builtin_amdgcn_permlane32_swap(__float_as_uint(a), __float_as_uint(b), false, false); f32x2 o = {__uint_as_float(r[0]), __uint_as_float(r[1])}; return o; }
DI float sel(unsigned m, float a, float b) { return __uint_as_float((__float_as_uint(a) & m) | (__float_as_uint(b) & ~m)); }
DI u32x4 bload(rsrc_t r, unsigned v, unsigned s) { return __builtin_amdgcn_raw_buffer_load_b128(r, v, s, 0); }
DI u32x4 bload1(rsrc_t r, unsigned v, unsigned s) { return __builtin_amdgcn_raw_buffer_load_b128(r, v, s, SC1); }
DI void bstore1(u32x4 d, rsrc_t r, unsigned v, unsigned s) { __builtin_amdgcn_raw_buffer_store_b128(d, r, v, s, SC1); }
DI bf16x8 as_bf(u32x4 v) { return __builtin_bit_cast(bf16x8, v); }
DI u64 uni64(u64 v) { return ((u64)(unsigned)__builtin_amdgcn_readfirstlane((unsigned)(v >> 32)) << 32) | (unsigned)__builtin_amdgcn_readfirstlane((unsigned)v); }

constexpr unsigned LDS_BYTES = 163840;
constexpr unsigned QXB = 14336;
constexpr unsigned QST = QXB + 24576;
constexpr unsigned QSTAT = 4 * QST;
constexpr unsigned LNROW = 131072;
constexpr unsigned G1STG = 0;
constexpr unsigned E2SCR = 16384;
constexpr unsigned P2STG = 49152;
constexpr unsigned SY = LDS_BYTES - 512;
enum { Y_FULL = 0, Y_FREE = 16, Y_HA = 32, Y_HB = 48, Y_ST = 64, Y_QS = 80, Y_ISS = 96, Y_E2 = 112, Y_RA = 128, Y_OK2 = 144, Y_OK3 = 160, Y_OK4 = 176, Y_WO = 192,
       Y_N1 = 208, Y_N2 = 212, Y_N3 = 216, Y_RB = 224, Y_SIG = 240 };
static_assert(QSTAT + 896 <= SY && P2STG + 98304 <= SY, "lds");
constexpr unsigned SPIN_GLB = 1u << 17;

template <int OFF> DI void sy_put(unsigned a, unsigned v) { asm volatile("ds_write_b32 %0, %1 offset:%2" ::"v"(a), "v"(v), "i"(OFF) : "memory"); }
template <int OFF> DI void sy_wait(unsigned a4, unsigned target) {
  unsigned t, n;
  asm volatile("s_mov_b32 %1, 0x200000\n"
               "1:\n\tds_read_b32 %0, %2 offset:%4\n\ts_waitcnt lgkmcnt(0)\n\tv_cmp_gt_u32 vcc, %3, %0\n\ts_cbranch_vccz 2f\n\t"
               "s_sleep 1\n\ts_sub_u32 %1, %1, 1\n\ts_cmp_lg_u32 %1, 0\n\ts_cbranch_scc1 1b\n"
               "2:" : "=&v"(t), "=&s"(n) : "v"(a4), "s"(target), "i"(OFF) : "vcc", "scc", "memory");
}
DI bool last_of(unsigned cnt, unsigned n, int lane) {
  unsigned old = 0;
  if (lane == 0) asm volatile("ds_add_rtn_u32 %0, %1, %2\n\ts_waitcnt lgkmcnt(0)" : "=&v"(old) : "v"(cnt), "v"(1u) : "memory");
  return (unsigned)__builtin_amdgcn_readfirstlane(old) == n - 1;
}
DI void gadd(char* p) { (void)__hip_atomic_fetch_add((GAS u64*)p, 1ull, __ATOMIC_RELAXED, __HIP_MEMORY_SCOPE_AGENT); }
DI u64 gpeek(const char* p) {
  u64 z = 0;
  asm volatile("" : "+v"(z));
  return __hip_atomic_fetch_add((GAS u64*)p, z, __ATOMIC_RELAXED, __HIP_MEMORY_SCOPE_AGENT);
}
template <int SL> DI void gwait(const char* p, bool on, u64 mask, u64 want) {
  FENCE();
  bool ok = !on;
#pragma unroll 1
  for (unsigned n = 0; n < SPIN_GLB; n++) {
    if (!ok) ok = (gpeek(p) & mask) == want;
    if (__all(ok)) break;
    __builtin_amdgcn_s_sleep(SL);
  }
  FENCE();
}

#define ST(i)
#define STDECL
#define STFLUSH()

__global__ __launch_bounds__(512, 2) void fused(const u16* __restrict__ x, const u16* __restrict__ wqkv, const u16* __restrict__ wo, const u16* __restrict__ w1,
                                                const u16* __restrict__ b1, const u16* __restrict__ w2, const u16* __restrict__ b2, char* __restrict__ ws,
                                                u16* __restrict__ out) {
  extern __shared__ __attribute__((aligned(16))) char smem[];
  uint64_t pc0; asm volatile("s_getpc_b64 %0" : "=s"(pc0));
  const unsigned L0 = (unsigned)(uintptr_t)(LAS char*)smem;
  const int tid = threadIdx.x, lane = tid & 63;
  const int W = __builtin_amdgcn_readfirstlane(tid >> 6), w = W & 3;
  const int xcd = blockIdx.x & 7, idx = blockIdx.x >> 3;
  const int sq = xcd >> 1, hh = xcd & 1;
  const int st = sq * 8 + (idx >> 2), ct = hh * 4 + (idx & 3);
  const int o3 = ((idx & 3) + 1) >> 2;
  const int gi = (idx >> 2) * (3 - 2 * o3) + (idx & 3) + 21 * o3;
  const int rt = (22 * sq + gi) >> 2, hg = hh * 4 + ((22 * sq + gi) & 3);
  const int c = lane & 15, g = lane >> 4, r8 = lane >> 3, pc = lane & 7;
  const f32x4 z4 = {0.f, 0.f, 0.f, 0.f};
  STDECL ST(0);
  const unsigned pfc = __builtin_amdgcn_raw_buffer_load_b32(mk_rsrc((const void*)(pc0 & ~255ull), CODE_LEN), ((idx & 7) * 8 + W) * 1024 + (lane & 7) * 128, 0, 0);
  {
    const unsigned on = (W >> 1) & 1;
    *(LAS unsigned*)LP(L0 + (on ? 0 : SY) + ((tid >> 5) & 3) * (on ? QST : 128) + (tid & 31) * 4) = on ? 0x3f803f80u : 0u;
  }
  const unsigned a4 = L0 + SY + (lane & 3) * 4, a1 = L0 + SY + w * 4;
  asm volatile("" ::"s"(x), "s"(wqkv), "s"(wo), "s"(w1), "s"(b1), "s"(w2), "s"(b2), "s"(ws), "s"(out));
  const u32x4 sx = mk_srd(x, S * D * 2), sw = mk_srd(wqkv, D * 3 * D * 2);
  const unsigned vx0 = (unsigned)((96 * rt - 16 + 8 * w + r8) * 1024 + ((pc ^ (((w & 1) << 2) | (r8 >> 1))) << 4));
  const unsigned vsw = (((pc >> 1) ^ (((r8 >> 1) & 1) | ((w & 1) << 1))) << 5) | ((pc & 1) << 4);
  const unsigned vwq = (8 * w + r8) * 3072 + hg * 128 + vsw;
  auto head = [&](int s) { glds16x3(sw, vwq, s * 64 * 3072, L0 + (s & 3) * QST + QXB + w * 3072); };
  auto rest = [&](int s) {
    const unsigned b = L0 + (s & 3) * QST + w * 1024;
    if (lane >= 8 || w != 0) glds16(sx, vx0, s * 128, b);
    glds16(sx, vx0 + 32768u, s * 128, b + 4096);
    glds16(sx, vx0 + 65536u, s * 128, b + 8192);
    if (w < 2) glds16(sx, vx0 + 98304u, s * 128, b + 12288);
    glds16x3(sw, vwq, s * 64 * 3072 + 32 * 3072, b + QXB + 12288 + w * 2048);
  };
  const int un = sq * 16 + hh * 8 + gi - 24;
  u32x4 xr[4];
  ST(11);
  if (gi >= 24) {
    asm volatile("buffer_load_dwordx4 %0, %4, %5, %6 offen\n\tbuffer_load_dwordx4 %1, %4, %5, %6 offen offset:1024\n\t"
                 "buffer_load_dwordx4 %2, %4, %5, %6 offen offset:2048\n\tbuffer_load_dwordx4 %3, %4, %5, %6 offen offset:3072"
                 : "=&v"(xr[0]), "=&v"(xr[1]), "=&v"(xr[2]), "=&v"(xr[3]) : "v"(lane * 16), "s"(sx), "s"((un * 32 + 4 * W) * 1024) : "memory");
  } else {
    asm volatile("" : "=v"(xr[0]), "=v"(xr[1]), "=v"(xr[2]), "=v"(xr[3]));
    if (gi < 22 && W >= 4) { head(0); rest(0); }
  }
  BAR();
  u64 tag = 0;

  if (gi >= 24) {
    const u64 ep = __hip_atomic_load((const GAS u64*)(ws + WS_EPOCH), __ATOMIC_RELAXED, __HIP_MEMORY_SCOPE_AGENT);
    const unsigned pfw = *(const GAS unsigned*)((lane & 31) < 24 ? (const char*)wqkv + (un * 8 + W) * 3072 + (lane & 31) * 128 : (const char*)wo + (un * 8 + W) * 1024 + (lane & 7) * 128);
    WAITV(2);
    asm volatile("" : "+v"(xr[0]), "+v"(xr[1]), "+v"(xr[2]), "+v"(xr[3]));
    f32x2 v[4][4];
    float p[4], s1[4], s2[4], o1[4], o2[4];
    auto red4 = [&](const float (&a)[4], float (&o)[4]) {
      f32x2 t = swap16(a[0], a[1]), u = swap16(a[2], a[3]);
      t = swap32(t[0] + t[1], u[0] + u[1]);
      const float s = sum16(t[0] + t[1]);
#pragma unroll
      for (int k = 0; k < 4; k++) o[k] = __uint_as_float(__builtin_amdgcn_readlane(__float_as_uint(s), 16 * k));
    };
#pragma unroll
    for (int rr = 0; rr < 4; rr++) {
      p[rr] = bflo(__builtin_amdgcn_readfirstlane(xr[rr][0]));
      f32x2 a = {0.f, 0.f}, b = {0.f, 0.f};
#pragma unroll
      for (int j = 0; j < 4; j++) { const f32x2 t = {bflo(xr[rr][j]), bfhi(xr[rr][j])}; v[rr][j] = t - bc2(p[rr]); a += v[rr][j]; b = fma2(v[rr][j], v[rr][j], b); }
      s1[rr] = a[0] + a[1]; s2[rr] = b[0] + b[1];
    }
    if (W == 0) {
      const size_t o = lane < 2 ? WS_C1 + (un * 2 + lane) * 128 : WS_C2 + (un * 12 + lane - 2) * 128;
      u64 e = ep;
      asm volatile("" : "+v"(e));
      if (lane < 14) __hip_atomic_store((GAS u64*)(ws + o), (e + 1) << 16, __ATOMIC_RELAXED, __HIP_MEMORY_SCOPE_AGENT);
    }
    red4(s1, o1); red4(s2, o2);
    f32x4 sr, sm;
#pragma unroll
    for (int rr = 0; rr < 4; rr++) {
      const float m = o1[rr] * (1.f / D);
      const float r = __builtin_amdgcn_rsqf(fmaxf(o2[rr] * (1.f / D) - m * m, 0.f) + EPS);
      sr[rr] = r; sm[rr] = (m + p[rr]) * r;
      f32x2 a = {0.f, 0.f}, b = {0.f, 0.f};
#pragma unroll
      for (int j = 0; j < 4; j++) { v[rr][j] = fma2(v[rr][j], bc2(r), bc2(-m * r)); a += v[rr][j]; b = fma2(v[rr][j], v[rr][j], b); }
      s1[rr] = a[0] + a[1]; s2[rr] = b[0] + b[1];
    }
    *(LAS f32x4*)LP(L0 + QSTAT + (W >> 1) * 64 + (W & 1) * 16 + (lane & 1) * 32) = (lane & 1) ? sm : sr;
    red4(s1, o1); red4(s2, o2);
#pragma unroll
    for (int rr = 0; rr < 4; rr++) {
      const float m2 = o1[rr] * (1.f / D);
      const float r2 = __builtin_amdgcn_rsqf(fmaxf(o2[rr] * (1.f / D) - m2 * m2, 0.f) + EPS);
      u32x4 h;
#pragma unroll
      for (int j = 0; j < 4; j++) h[j] = pkv(fma2(v[rr][j], bc2(r2), bc2(-m2 * r2)));
      *(LAS u32x4*)LP(L0 + (4 * W + rr) * 1088 + ((W >> 1) & 1) * 32 + lane * 16) = h;
    }
    ST(12);
    if (W < 4) sy_put<Y_HA>(a1, 1); else sy_put<Y_HB>(a1, 1);
    char* const c1p = ws + WS_C1 + (sq * 32 + (lane & 31)) * 128;
    u64 chk = 0;
    if (W == 1 && lane < 32) asm volatile("global_atomic_add_x2 %0, %1, %0, off sc0" : "+v"(chk) : "v"(c1p) : "memory");
    sy_wait<Y_HA>(a4, 1); sy_wait<Y_HB>(a4, 1);
    {
      bf16x8 tv[4];
#pragma unroll
      for (int n = 0; n < 4; n++) tv[n] = ltr2(LP(L0 + (8 * g + (c >> 2)) * 1088 + (g & 1) * 32 + W * 128 + (c & 3) * 8) + n * 32, 4 * 1088);
      const u32x4 sv = *(const LAS u32x4*)LP(L0 + QSTAT + c * 16);
      const rsrc_t rht = mk_rsrc(ws + WS_HT, HT_BYTES);
#pragma unroll
      for (int n = 0; n < 4; n++) bstore1(__builtin_bit_cast(u32x4, tv[n]), rht, lane * 16, ((4 * W + n) * 64 + un) * 1024);
      if (W == 2) bstore1(sv, mk_rsrc(ws + WS_STAT, S * 8), lane < 16 ? un * 256 + lane * 16 : OOB, 0);
    }
    ST(1);
    WAITV(0);
    u64 e = ep;
    asm volatile("" : "+v"(e) : "v"(pfw));
    tag = (uni64(e) + 1) << 16;
    if (W == 1) { asm volatile("" : "+v"(chk)); if (__any(lane < 32 && (chk >> 16) != (tag >> 16))) gwait<2>(c1p, lane < 32, ~0xffffull, tag); }
    if (last_of(L0 + SY + Y_N1, 8, lane)) { if (lane < 32) gadd(c1p); sy_put<Y_SIG>(a4, 1); }
    ST(2);
  } else if (gi >= 22) {
    tag = (uni64(__hip_atomic_load((const GAS u64*)(ws + WS_EPOCH), __ATOMIC_RELAXED, __HIP_MEMORY_SCOPE_AGENT)) + 1) << 16;
    if (W == 0) {
      char* const c1p = ws + WS_C1 + (sq * 32 + (lane & 31)) * 128;
      __builtin_amdgcn_s_sleep(8);
      gwait<4>(c1p, lane < 32, ~0xffffull, tag);
      sy_wait<Y_HB>(a4, 1);
      if (lane < 32) gadd(c1p);
      sy_put<Y_SIG>(a4, 1);
    }
    if (W >= 4) sy_put<Y_HB>(a1, 1);
  }

  if (W < 4) {
    if (gi < 22) {
      f32x4 aq[7], ak[7], av[7];
#pragma unroll
      for (int b = 0; b < 7; b++) { ak[b] = z4; av[b] = z4; aq[b] = z4; }
      const unsigned ax = L0 + c * 128 + ((g ^ (c >> 1)) << 4);
      const unsigned aw = L0 + QXB + g * 3072 + (c >> 2) * 128 + ((w ^ ((c >> 3) | ((g & 1) << 1))) << 5) + (c & 3) * 8;
      bf16x8 xf[2][7];
      bf16x4 wl[2][3], wh[2][3];
      const lp pxa[2][2] = {{LP(ax), LP(ax ^ 64)}, {LP(ax + 2 * QST), LP((ax ^ 64) + 2 * QST)}}, pwa[2] = {LP(aw), LP(aw + 2 * QST)};
      auto RD = [&](int n, int i, int slot, int kk) {
        const lp px = pxa[slot >> 1][kk] + (slot & 1) * QST, pw = pwa[slot >> 1] + (slot & 1) * QST + kk * 12288;
        if (i == 0) wl[n][1] = ltr(pw + 1024);
        if (i == 1) wh[n][1] = ltr(pw + 1024 + 512);
        if (i == 2) xf[n][6] = l128(px + 12288);
        if (i == 3) wl[n][2] = ltr(pw + 2048);
        if (i == 4) wh[n][2] = ltr(pw + 2048 + 512);
        if (i == 5) wl[n][0] = ltr(pw);
        if (i == 6) wh[n][0] = ltr(pw + 512);
        if (i >= 7) xf[n][12 - i] = l128(px + (12 - i) * 2048);
      };
      auto MM = [&](int cur, int m) {
        const int t = 6 - m / 3;
        if (m % 3 == 0) ak[t] = mfma32(cat8(wl[cur][1], wh[cur][1]), xf[cur][t], ak[t]);
        if (m % 3 == 1) av[t] = mfma32(xf[cur][t], cat8(wl[cur][2], wh[cur][2]), av[t]);
        if (m % 3 == 2) aq[t] = mfma32(cat8(wl[cur][0], wh[cur][0]), xf[cur][t], aq[t]);
      };
      __builtin_amdgcn_s_setprio(2);
      sy_wait<Y_FULL>(a4, 1);
      ST(2);
#pragma unroll
      for (int i = 0; i < 13; i++) RD(0, i, 0, 0);
      SCHED();
#pragma unroll 1
      for (int s4 = 0; s4 < 8; s4 += 4) {
#pragma unroll
        for (int sl = 0; sl < 4; sl++) {
          unsigned fl = 0;
#pragma unroll
          for (int m = 0; m < 20; m++) {
            MM(0, m); SCHED();
            if (m < 13) RD(1, m, sl, 1);
            if (m == 13) { FENCE(); *(volatile LAS unsigned*)(LP(a1) + Y_FREE) = s4 + sl + 1; }
            if (m == 14) fl = *(volatile LAS unsigned*)(LP(a4) + Y_FULL);
            SCHED();
          }
          const unsigned need = min(s4 + sl + 2, 8);
          if (__builtin_expect(__any(fl < need), 0)) sy_wait<Y_FULL>(a4, need);
          MM(0, 20); SCHED();
#pragma unroll
          for (int m = 0; m < 21; m++) {
            MM(1, m); SCHED();
            if (m < 13) RD(0, m, (sl + 1) & 3, 0);
            SCHED();
          }
        }
      }
      sy_put<Y_QS>(a1, 1);
      ST(3);
      f32x2 cq[2], ck[2];
#pragma unroll
      for (int j = 0; j < 4; j++) { cq[j >> 1][j & 1] = -dpp<0x150>(aq[0][j]); ck[j >> 1][j & 1] = -dpp<0x150>(ak[0][j]); }
      f32x2 cvt = swap16(av[0][0], av[0][0]);
      cvt = swap32(cvt[0], cvt[0]);
      const f32x2 cv = bc2(-cvt[0]);
      sy_wait<Y_ST>(a4, 1);
      ST(12);
      u32x2 qf[6], kf[7], vf[7];
      {
        float sr[7], sm[7]; f32x4 vr[7], vm[7];
        const lp pcs = LP(L0 + QSTAT + (c >> 3) * 64 + (c & 7) * 4), prs = LP(L0 + QSTAT + (g >> 1) * 64 + (g & 1) * 16);
#pragma unroll
        for (int t = 0; t < 7; t++) {
          sr[t] = *(const LAS float*)(pcs + t * 128); sm[t] = *(const LAS float*)(pcs + t * 128 + 32);
          vr[t] = lf4(prs + t * 128); vm[t] = lf4(prs + t * 128 + 32);
        }
        SCHED();
#pragma unroll
        for (int t = 0; t < 7; t++) {
          const f32x2 r = bc2(sr[t]), m = bc2(sm[t]);
          f32x2 y[6] = {m * ck[0], m * ck[1], m * cq[0], m * cq[1], lo2(vm[t]) * cv, hi2(vm[t]) * cv};
          SCHED();
          y[0] = fma2(r, lo2(ak[t]), y[0]); y[1] = fma2(r, hi2(ak[t]), y[1]); y[2] = fma2(r, lo2(aq[t]), y[2]); y[3] = fma2(r, hi2(aq[t]), y[3]);
          y[4] = fma2(lo2(vr[t]), lo2(av[t]), y[4]); y[5] = fma2(hi2(vr[t]), hi2(av[t]), y[5]);
          SCHED();
          kf[t][0] = pkv(y[0]); kf[t][1] = pkv(y[1]);
          if (t > 0) { qf[t - 1][0] = pkv(y[2]); qf[t - 1][1] = pkv(y[3]); }
          vf[t][0] = pkv(y[4]); vf[t][1] = pkv(y[5]);
        }
        SCHED();
      }
      u32x2 op[6];
      {
        unsigned om = 0u - (unsigned)(c & 1);
        asm("" : "+v"(om));
        const bool cur0 = 4 * g + (c & 1) <= c, cur1 = 4 * g + (c & 1) + 2 <= c;
        constexpr float SC = 0.25f * 1.4426950408889634f;
        f32x4 sp[6], su[6];
#pragma unroll
        for (int t = 0; t < 6; t++) { sp[t] = mfma16(kf[t], qf[t], z4); su[t] = mfma16(kf[t + 1], qf[t], z4); }
        float l0[6], l1[6], mx[8];
#pragma unroll
        for (int t = 0; t < 6; t++) {
          const float p0 = sel(om, sp[t][1], sp[t][0]), p1 = sel(om, sp[t][3], sp[t][2]);
          const float u0 = sel(om, su[t][1], su[t][0]), u1 = sel(om, su[t][3], su[t][2]);
          l0[t] = cur0 ? u0 : p0; l1[t] = cur1 ? u1 : p1;
          if (t == 0 && rt == 0) { l0[t] = cur0 ? l0[t] : -4e9f; l1[t] = cur1 ? l1[t] : -4e9f; }
          mx[t] = fmaxf(l0[t], l1[t]);
        }
        {
          const f32x2 a = swap16(mx[0], mx[1]), b = swap16(mx[2], mx[3]), a2 = swap16(mx[4], mx[5]);
          const float n2 = fmaxf(a2[0], a2[1]);
          const f32x2 d = swap32(fmaxf(a[0], a[1]), fmaxf(b[0], b[1])), d2 = swap32(n2, n2);
          const float m = fmaxf(d[0], d[1]) * -SC, m2 = fmaxf(d2[0], d2[1]) * -SC;
          const f32x2 e = swap32(m, m), f0 = swap16(e[0], e[0]), f1 = swap16(e[1], e[1]), f2 = swap16(m2, m2);
          mx[0] = f0[0]; mx[1] = f0[1]; mx[2] = f1[0]; mx[3] = f1[1]; mx[4] = f2[0]; mx[5] = f2[1];
        }
        const u32x4 ones = {0x3f803f80u, 0x3f803f80u, 0x3f803f80u, 0x3f803f80u};
        const unsigned keep = om ^ 0x0000ffffu;
        const unsigned k00 = cur0 ? 0u : keep, k01 = cur1 ? 0u : keep, k10 = cur0 ? keep : 0u, k11 = cur1 ? keep : 0u;
        f32x4 o[6], den[6];
#pragma unroll
        for (int t = 0; t < 6; t++) {
          const float e0 = __builtin_amdgcn_exp2f(__builtin_fmaf(l0[t], SC, mx[t])), e1 = __builtin_amdgcn_exp2f(__builtin_fmaf(l1[t], SC, mx[t]));
          const unsigned b0 = pk2(e0, e0), b1v = pk2(e1, e1);
          const u32x4 pu = {b0 & k00, b1v & k01, b0 & k10, b1v & k11}, vv = {vf[t][0], vf[t][1], vf[t + 1][0], vf[t + 1][1]};
          o[t] = mfma32(as_bf(vv), as_bf(pu), z4);
          den[t] = mfma32(as_bf(ones), as_bf(pu), z4);
        }
#pragma unroll
        for (int t = 0; t < 6; t++) {
          const f32x2 inv = bc2(__builtin_amdgcn_rcpf(den[t][0]));
          op[t][0] = pkv(lo2(o[t]) * inv); op[t][1] = pkv(hi2(o[t]) * inv);
        }
      }
      {
        const rsrc_t ro = mk_rsrc(ws + WS_O, O_BYTES);
#pragma unroll
        for (int t = 0; t < 6; t += 2) {
          const auto r0 = __builtin_amdgcn_permlane16_swap(op[t][0], op[t + 1][0], false, false);
          const auto r1 = __builtin_amdgcn_permlane16_swap(op[t][1], op[t + 1][1], false, false);
          const u32x4 v = {r0[0], r1[0], r0[1], r1[1]};
          if (t == 0 || rt < 21) bstore1(v, ro, (g & 1) * 16384 + ((g >> 1) * 16 + c) * 16, ((6 * rt + t) * 16 + 2 * hg + (w >> 1)) * 1024 + (w & 1) * 512);
        }
      }
      __builtin_amdgcn_s_setprio(0);
      ST(13);
      WAITV(0);
      if (last_of(L0 + SY + Y_N2, 4, lane) && lane < (rt < 21 ? 12 : 4)) gadd(ws + WS_C2 + (12 * rt + lane) * 128);
      ST(4);
    } else {
      sy_wait<Y_OK2>(a4, 1);
      const rsrc_t rw2 = mk_rsrc(w2, F * S * 2);
      unsigned pv[7];
#pragma unroll
      for (int n = 0; n < 7; n++) {
        const int m = gi >= 24 ? gi - 24 : 8 + gi - 22 + 2 * n;
        pv[n] = __builtin_amdgcn_raw_buffer_load_b32(rw2, (gi < 24 || n == 0) ? ((44 * w + 2 * m + hh) * 8 + r8) * 4096 + sq * 1024 + pc * 128 : OOB, 0, 0);
      }
      asm volatile("" ::"v"(pv[0]), "v"(pv[1]), "v"(pv[2]), "v"(pv[3]), "v"(pv[4]), "v"(pv[5]), "v"(pv[6]));
      ST(4);
    }
    asm volatile("" ::"v"(pfc));

    const rsrc_t rgg = mk_rsrc(ws + WS_G, G_BYTES), rw2 = mk_rsrc(w2, F * S * 2);
    f32x4 acc[4][4];
#pragma unroll
    for (int i = 0; i < 4; i++)
#pragma unroll
      for (int j = 0; j < 4; j++) acc[i][j] = z4;
    const unsigned vg = lane * 16 + (4 * ct * 43 + 11 * w) * 1024;
    const unsigned vw2 = (352 * w + r8) * 4096 + st * 128 + pc * 16;
    const unsigned lw = L0 + P2STG + w * 24576;
    const unsigned aw0 = lw + r8 * 128 + ((((pc >> 1) ^ ((r8 >> 1) & 1)) << 5) | ((pc & 1) << 4));
    const lp pw0 = LP(aw0), pw1 = LP(aw0 ^ 64);
    const unsigned ar = lw + (8 * g + (c >> 2)) * 128 + ((((c >> 3) | ((g & 1) << 1))) << 5) + (c & 3) * 8;
    u32x4 wr[11][4], gf[11][4];
    auto LW = [&](int k) {
#pragma unroll
      for (int j = 0; j < 4; j++) wr[k][j] = bload(rw2, (k == 10 && w == 3) ? OOB : vw2, (32 * k + 8 * j) * 4096);
      SCHED();
    };
    auto LG1 = [&](int k, int j) { gf[k][j] = bload1(rgg, (k == 10 && w == 3) ? OOB : vg, j * 43 * 1024 + k * 1024); };
    auto SW = [&](int k, int slot) {
#pragma unroll
      for (int j = 0; j < 4; j++) *(LAS u32x4*)(((j & 1) ? pw1 : pw0) + j * 1024 + slot * 4096) = wr[k][j];
    };
    lp pr[4];
#pragma unroll
    for (int j = 0; j < 4; j++) pr[j] = LP(ar ^ (((w + j) & 3) << 5));
    bf16x8 tw[2][4];
    bf16x8 tk[4][4];
    auto TR = [&](bf16x8 (&t)[4], int slot) {
#pragma unroll
      for (int j = 0; j < 4; j++) t[j] = ltr2(pr[j] + slot * 4096, 512);
      SCHED();
    };
    if (gi < 22) sy_wait<Y_QS>(a4, 1);
    sy_wait<Y_ISS>(a4, 1);
    ST(5);
    LW(7); LW(8); LW(9); LW(10);
    SW(7, 0); TR(tk[0], 0); LW(0);
    SW(8, 1); TR(tk[1], 1); LW(1);
    SW(9, 2); TR(tk[2], 2); LW(2);
    SW(10, 3); TR(tk[3], 3); LW(3);
    SW(0, 0); TR(tw[0], 0); LW(4);
    SW(1, 1); LW(5);
    SW(2, 2); LW(6);
    SW(3, 3); SW(4, 4); SW(5, 5); SW(6, 0);
    const int srow = 64 * st + 16 * w + c;
    u32x4 xv[2];
#pragma unroll
    for (int i = 0; i < 2; i++) xv[i] = bload(mk_rsrc(x, S * D * 2), (srow * D + 64 * ct + 16 * (g & 1) + 8 * (g >> 1)) * 2, i * 64);
    ST(6);
    sy_wait<Y_OK3>(a4, 1);
    ST(7);
    constexpr int GD = 4;
#pragma unroll
    for (int k = 0; k < GD; k++)
#pragma unroll
      for (int j = 0; j < 4; j++) LG1(k, j);
    SCHED();
    u32x4 bv[2];
#pragma unroll
    for (int k = 0; k < 10; k++) {
      if (k + GD == 11) {
        sy_wait<Y_OK4>(a4, 1);
        const rsrc_t rb = mk_rsrc(ws + WS_BASE, S * D * 2);
#pragma unroll
        for (int i = 0; i < 2; i++) bv[i] = bload1(rb, lane * 16, ((4 * st + w) * 16 + 2 * ct + i) * 1024);
      }
#pragma unroll
      for (int i = 0; i < 4; i++) {
#pragma unroll
        for (int j = 0; j < 4; j++) {
          acc[i][j] = mfma32(as_bf(gf[k][i]), k < 7 ? tw[k & 1][j] : tk[k < 7 ? 0 : k - 7][j], acc[i][j]);
          if (j == 0 && k + 1 < 7) { SCHED(); tw[(k + 1) & 1][i] = ltr2(pr[i] + ((k + 1) % 6) * 4096, 512); SCHED(); }
        }
        SCHED();
        if (k + GD < 11) LG1(k + GD, i);
        SCHED();
      }
    }
#pragma unroll
    for (int jj = 1; jj <= 4; jj++) {
#pragma unroll
      for (int i = 0; i < 4; i++) {
        acc[i][jj & 3] = mfma32(as_bf(gf[10][i]), tk[3][jj & 3], acc[i][jj & 3]); SCHED();
        if (jj >= 2) *(LAS f32x4*)(LP(lw + lane * 16) + ((jj - 2) * 4 + i) * 1024) = acc[i][jj - 1];
        SCHED();
      }
    }
    ST(8);
    BAR();
    ST(9);
    f32x4 u[4];
#pragma unroll
    for (int i = 0; i < 4; i++) {
      u[i] = acc[i][0] + (f32x4){bflo(bv[i >> 1][2 * (i & 1)]), bfhi(bv[i >> 1][2 * (i & 1)]), bflo(bv[i >> 1][2 * (i & 1) + 1]), bfhi(bv[i >> 1][2 * (i & 1) + 1])};
#pragma unroll
      for (int j = 1; j < 4; j++) u[i] += lf4(LP(L0 + P2STG + ((w - j) & 3) * 24576 + lane * 16) + ((j - 1) * 4 + i) * 1024);
    }
#pragma unroll
    for (int i = 0; i < 4; i += 2) {
      float v[8];
#pragma unroll
      for (int r = 0; r < 4; r++) { const f32x2 sw = swap16(u[i][r], u[i + 1][r]); v[r] = sw[0]; v[4 + r] = sw[1]; }
      u32x4 o;
#pragma unroll
      for (int j = 0; j < 4; j++) o[j] = pk2(v[2 * j] + bflo(xv[i >> 1][j]), v[2 * j + 1] + bfhi(xv[i >> 1][j]));
      *(u32x4*)(out + srow * D + 64 * ct + 16 * i + 16 * (g & 1) + 8 * (g >> 1)) = o;
    }
    ST(10);
  } else {
    if (gi < 22) {
      {
        head(1); rest(1);
        ST(13);
        WAITV(9); sy_put<Y_FULL>(a1, 1);
        int ann = 1;
        unsigned fr = 0;
#pragma unroll 1
        for (int s = 2; s < 8; s++) {
          if (s >= 4 && __any(fr < (unsigned)(s - 3))) {
            WAITV(9); sy_put<Y_FULL>(a1, s - 1); ann = s - 1;
            fr = *(volatile LAS unsigned*)(LP(a4) + Y_FREE);
            if (__any(fr < (unsigned)(s - 3))) { WAITV(0); sy_put<Y_FULL>(a1, s); ann = s; sy_wait<Y_FREE>(a4, s - 3); }
          }
          head(s);
          if (ann < s - 1) { WAITV(12); ann = s - 1; sy_put<Y_FULL>(a1, ann); }
          rest(s);
          fr = *(volatile LAS unsigned*)(LP(a4) + Y_FREE);
        }
      }
      ST(1);
      const bool c1on = lane < (w == 3 ? 4 : 1);
      const char* const c1w = ws + WS_C1 + (((w == 3 ? 0 : w) + (lane & 3)) * 32 + (blockIdx.x & 31)) * 128;
      u64 c1v = 0;
      const u64 ep = __hip_atomic_load((const GAS u64*)(ws + WS_EPOCH), __ATOMIC_RELAXED, __HIP_MEMORY_SCOPE_AGENT);
      if (c1on) asm volatile("global_atomic_add_x2 %0, %1, %0, off sc0" : "+v"(c1v) : "v"(c1w) : "memory");
      u32x4 stv4;
      auto c1_done = [&]() {
        asm volatile("" : "+v"(c1v));
        tag = (uni64(ep) + 1) << 16;
        if (__any(c1on && c1v != tag + 20)) gwait<2>(c1w, c1on, ~0ull, tag + 20);
        if (w == 3) stv4 = bload1(mk_rsrc(ws + WS_STAT, S * 8), (unsigned)((12 * rt - 2) * 64 + min(lane, 55) * 16), 0);
      };
      auto put_stats = [&]() { if (w == 3) { *(LAS u32x4*)LP(L0 + QSTAT + min(lane, 55) * 16) = stv4; sy_put<Y_ST>(a4, 1); } };
      const int ft = rt, dt = hg;
      const rsrc_t rht = mk_rsrc(ws + WS_HT, HT_BYTES), r1 = mk_rsrc(w1, S * F * 2);
      constexpr int DEP = 3;
      u32x4 wr[16][4], fb[16][4];
      const unsigned vw = (ft == 21 && pc >= 4) ? OOB : (512 * w + r8) * 2752 + ft * 128 + pc * 16, vb = lane * 16 + (4 * dt * 64 + 16 * w) * 1024;
      const unsigned lb = L0 + G1STG + w * 4096;
      const unsigned aw0 = lb + r8 * 128 + ((((pc >> 1) ^ ((r8 >> 1) & 1)) << 5) | ((pc & 1) << 4));
      const lp pw0 = LP(aw0), pw1 = LP(aw0 ^ 64);
      const unsigned ar = lb + (8 * g + (c >> 2)) * 128 + ((((c >> 3) | ((g & 1) << 1))) << 5) + (c & 3) * 8;
      lp pe[4];
#pragma unroll
      for (int e = 0; e < 4; e++) pe[e] = LP(ar ^ (e << 5));
      auto LW = [&](int k, int j) { wr[k][j] = bload(r1, vw, (32 * k + 8 * j) * 2752); };
      auto LB = [&](int k, int j) { fb[k][j] = bload1(rht, vb, ((w + j) & 3) * 65536 + k * 1024); };
      auto SW = [&](int k, int j) { *(LAS u32x4*)(((j & 1) ? pw1 : pw0) + j * 1024) = wr[k][j]; };
      LW(0, 0); LW(0, 1);
      WAITV(13); sy_put<Y_FULL>(a1, 7);
      LW(0, 2); LW(0, 3); LW(1, 0); LW(1, 1);
      WAITV(8); sy_put<Y_FULL>(a1, 8);
      LW(1, 2); LW(1, 3);
      WAITV(8);
      ST(2);
      c1_done();
      ST(3);
      u32x2 bb[4];
      {
        const rsrc_t rb1 = mk_rsrc(b1, F * 2);
#pragma unroll
        for (int i = 0; i < 4; i++) bb[i] = __builtin_amdgcn_raw_buffer_load_b64(rb1, (64 * ft + 4 * g) * 2 + i * 32, 0, 0);
      }
      LB(0, 0); LB(0, 1); LB(0, 2); LB(0, 3);
      LB(1, 0); LB(1, 1); LB(1, 2); LB(1, 3);
      put_stats();
      LW(2, 0); LW(2, 1); LW(2, 2); LW(2, 3);
      LB(2, 0); LB(2, 1); LB(2, 2); LB(2, 3);
      f32x4 acc[4][4];
#pragma unroll
      for (int i = 0; i < 4; i++) {
        acc[i][0] = (f32x4){bflo(bb[i][0]), bfhi(bb[i][0]), bflo(bb[i][1]), bfhi(bb[i][1])};
#pragma unroll
        for (int j = 1; j < 4; j++) acc[i][j] = z4;
      }
      sy_wait<Y_FREE>(a4, 5);
      bf16x4 al[2][4], ah[2][4];
      SW(0, 0); SW(0, 1); SW(0, 2); SW(0, 3);
#pragma unroll
      for (int e = 0; e < 4; e++) { al[0][e] = ltr(pe[e]); ah[0][e] = ltr(pe[e] + 512); }
      SCHED();
#pragma unroll
      for (int k = 0; k < 15; k++) {
#pragma unroll
        for (int m = 0; m < 16; m++) {
          const int i = m & 3, j = m >> 2;
          acc[i][j] = mfma32(cat8(al[k & 1][i], ah[k & 1][i]), as_bf(fb[k][j]), acc[i][j]);
          SCHED();
          if (m < 4) SW(k + 1, m);
          else if (m >= 6 && m < 14) { const int e = (m - 6) >> 1; if (m & 1) ah[(k + 1) & 1][e] = ltr(pe[e] + 512); else al[(k + 1) & 1][e] = ltr(pe[e]); }
          if (i == 1 && k + DEP < 16) LW(k + DEP, j);
          if (i == 3 && k + DEP < 16) LB(k + DEP, j);
          SCHED();
        }
        if (k == 3) ST(8);
        if (k == 7) ST(9);
        if (k == 11) ST(10);
        if (k == 8) sy_wait<Y_QS>(a4, 1);
        if (k + DEP == 15) { sy_put<Y_ISS>(a1, 1); __builtin_amdgcn_s_setprio(3); }
      }
#pragma unroll
      for (int jj = 1; jj <= 4; jj++) {
#pragma unroll
        for (int i = 0; i < 4; i++) {
          acc[i][jj & 3] = mfma32(cat8(al[1][i], ah[1][i]), as_bf(fb[15][jj & 3]), acc[i][jj & 3]); SCHED();
          if (jj >= 2) *(LAS f32x4*)((jj < 4 ? LP(L0 + E2SCR + w * 8192 + lane * 16) + (jj - 2) * 4096 : LP(lb + lane * 16)) + i * 1024) = acc[i][jj - 1];
          SCHED();
        }
      }
      sy_put<Y_E2>(a1, 1);
      ST(4);
      const lp pin[3] = {LP(L0 + E2SCR + ((w - 1) & 3) * 8192 + lane * 16), LP(L0 + E2SCR + ((w - 2) & 3) * 8192 + 4096 + lane * 16), LP(L0 + G1STG + ((w - 3) & 3) * 4096 + lane * 16)};
      sy_wait<Y_E2>(a4, 1);
      ST(5);
      f32x4 pt[4][3];
#pragma unroll
      for (int i = 0; i < 4; i++)
#pragma unroll
        for (int j = 0; j < 3; j++) pt[i][j] = lf4(pin[j] + i * 1024);
      SCHED();
      u32x2 gp[4];
#pragma unroll
      for (int i = 0; i < 4; i++) {
        const f32x4 u = (acc[i][0] + pt[i][0]) + (pt[i][1] + pt[i][2]);
#pragma unroll
        for (int hf = 0; hf < 2; hf++) {
          constexpr float K1 = -2.f * 0.7978845608028654f * 1.4426950408889634f, K2 = K1 * 0.044715f;
          const f32x2 t = hf ? hi2(u) : lo2(u);
          const f32x2 z = t * fma2(t * t, bc2(K2), bc2(K1));
          f32x2 e = {__builtin_amdgcn_exp2f(z[0]), __builtin_amdgcn_exp2f(z[1])};
          e += bc2(1.f);
          const f32x2 rc = {__builtin_amdgcn_rcpf(e[0]), __builtin_amdgcn_rcpf(e[1])};
          gp[i][hf] = pkv(t * rc);
        }
      }
      const rsrc_t rgg = mk_rsrc(ws + WS_G, G_BYTES);
#pragma unroll
      for (int i = 0; i < 4; i += 2) {
        const auto r0 = __builtin_amdgcn_permlane16_swap(gp[i][0], gp[i + 1][0], false, false);
        const auto r1v = __builtin_amdgcn_permlane16_swap(gp[i][1], gp[i + 1][1], false, false);
        const u32x4 v = {r0[0], r1v[0], r0[1], r1v[1]};
        if (i == 0 || ft < 21) bstore1(v, rgg, (2 * (g & 1) + (g >> 1)) * 256 + c * 16, ((4 * dt + w) * 43 + 2 * ft + (i >> 1)) * 1024);
      }
      ST(6);
      WAITV(0);
      if (last_of(L0 + SY + Y_N3, 4, lane) && lane < 32) gadd(ws + WS_C3 + (lane * 8 + dt) * 128);
      ST(7);
    } else {
      unsigned pv[7];
      sy_wait<Y_SIG>(a4, 1);
      {
        const rsrc_t r1 = mk_rsrc(w1, S * F * 2);
        const unsigned c0 = ((22 * sq) >> 2) * 128;
#pragma unroll
        for (int n = 0; n < 7; n++) {
          const int m = n < 6 ? 2 * (gi - 22) + 20 * (n >> 1) + ((n & 1) ^ hh) : 60 + gi - 22;
          const unsigned row = w * 512 + m * 8 + r8;
          pv[n] = __builtin_amdgcn_raw_buffer_load_b32(r1, (m < 64 && pc < 6 + (r8 & 1)) ? ((row * 2752 + c0) & ~127u) + pc * 128 : OOB, 0, 0);
        }
      }
      if (gi >= 24) {
        const int so = sq * 4 + ((gi - 24) >> 1), dq = hh * 2 + (gi & 1);
        const u32x4 swo = mk_srd(wo, D * D * 2);
        const unsigned vwo = (8 * w + r8) * 1024 + dq * 256 + vsw;
#pragma unroll 1
        for (int n = 0; n < 16; n++) {
          glds16(swo, vwo, n * 32768, L0 + (n * 4 + w) * 1024);
          glds16(swo, vwo, n * 32768 + 128, L0 + 65536 + (n * 4 + w) * 1024);
        }
        const rsrc_t ro = mk_rsrc(ws + WS_O, O_BYTES), rb = mk_rsrc(ws + WS_BASE, S * D * 2);
        const int srow = 128 * so + 32 * w + c;
        const float bias0 = bflo(b2[srow]), bias1 = bflo(b2[srow + 16]);
        sy_put<Y_OK2>(a1, 1);
        WAITV(0);
        asm volatile("" ::"v"(pv[0]), "v"(pv[1]), "v"(pv[2]), "v"(pv[3]), "v"(pv[4]), "v"(pv[5]), "v"(pv[6]));
        sy_put<Y_WO>(a1, 1);
        ST(8);
        gwait<4>(ws + WS_C2 + ((so * 4 + w) * 4 + dq) * 128, lane == 0, ~0ull, tag + 8);
        ST(3);
        u32x4 of[16][2];
        const unsigned vo = lane * 16 + (8 * so + 2 * w) * 16384;
        constexpr int OD = 8;
#pragma unroll
        for (int k = 0; k < OD; k++) { of[k][0] = bload1(ro, vo, k * 1024); of[k][1] = bload1(ro, vo, 16384 + k * 1024); }
        sy_wait<Y_WO>(a4, 1);
        const unsigned ar = L0 + (8 * g + (c >> 2)) * 128 + ((((c >> 3) | ((g & 1) << 1))) << 5) + (c & 3) * 8;
        lp pe[8];
#pragma unroll
        for (int e = 0; e < 8; e++) pe[e] = LP((ar ^ ((e & 3) << 5)) + (e >> 2) * 65536);
        f32x4 acc[8][2];
#pragma unroll
        for (int i = 0; i < 8; i++) { acc[i][0] = (f32x4){bias0, bias0, bias0, bias0}; acc[i][1] = (f32x4){bias1, bias1, bias1, bias1}; }
        bf16x4 tl[2][8], th[2][8];
#pragma unroll
        for (int e = 0; e < 8; e++) { tl[0][e] = ltr(pe[e]); th[0][e] = ltr(pe[e] + 512); }
        SCHED();
#pragma unroll
        for (int k = 0; k < 16; k++) {
#pragma unroll
          for (int e = 0; e < 8; e++) {
            acc[e][0] = mfma32(cat8(tl[k & 1][e], th[k & 1][e]), as_bf(of[k][0]), acc[e][0]); SCHED();
            if (k < 15) tl[(k + 1) & 1][e] = ltr(pe[e] + (k + 1) * 4096);
            SCHED();
            acc[e][1] = mfma32(cat8(tl[k & 1][e], th[k & 1][e]), as_bf(of[k][1]), acc[e][1]); SCHED();
            if (k < 15) th[(k + 1) & 1][e] = ltr(pe[e] + (k + 1) * 4096 + 512);
            SCHED();
          }
          if (k + OD < 16) { of[k + OD][0] = bload1(ro, vo, (k + OD) * 1024); of[k + OD][1] = bload1(ro, vo, 16384 + (k + OD) * 1024); }
          if (k == 14) { FENCE(); sy_put<Y_ISS>(a1, 1); __builtin_amdgcn_s_setprio(3); }
        }
        ST(4);
#pragma unroll
        for (int j = 0; j < 2; j++)
#pragma unroll
          for (int i = 0; i < 4; i++) {
            const u32x4 o = {pkv(lo2(acc[2 * i][j])), pkv(hi2(acc[2 * i][j])), pkv(lo2(acc[2 * i + 1][j])), pkv(hi2(acc[2 * i + 1][j]))};
            bstore1(o, rb, lane * 16, ((8 * so + 2 * w + j) * 16 + 4 * dq + i) * 1024);
          }
        ST(6);
        WAITV(0);
        if (last_of(L0 + SY + Y_N3, 4, lane) && lane < 4) gadd(ws + WS_C4 + ((2 * so + (lane >> 1)) * 8 + 2 * dq + (lane & 1)) * 128);
        ST(7);
      } else {
        sy_put<Y_OK2>(a1, 1);
        sy_put<Y_ISS>(a1, 1);
        asm volatile("" ::"v"(pv[0]), "v"(pv[1]), "v"(pv[2]), "v"(pv[3]), "v"(pv[4]), "v"(pv[5]), "v"(pv[6]));
        __builtin_amdgcn_s_sleep(127);
      }
    }
    asm volatile("" ::"v"(pfc));
    if (w == 0) { gwait<2>(ws + WS_C3 + (st * 8 + ct) * 128, lane == 0, ~0ull, tag + 22); sy_put<Y_OK3>(a4, 1); }
    if (w == 1) { gwait<4>(ws + WS_C4 + (st * 8 + ct) * 128, lane == 0, ~0ull, tag + 1); sy_put<Y_OK4>(a4, 1); }
    if (w == 2 && blockIdx.x == 0) {
      sy_wait<Y_OK3>(a4, 1);
      gwait<4>(ws + WS_C3 + (lane & 7) * 128, lane < 8, ~0ull, tag + 22);
      if (lane == 0) __hip_atomic_store((GAS u64*)(ws + WS_EPOCH), tag >> 16, __ATOMIC_RELAXED, __HIP_MEMORY_SCOPE_AGENT);
    }
    BAR();
  }
  STFLUSH();
}

extern "C" void kernel_launch(void* const* d_in, const int* in_sizes, int n_in, void* d_out, int out_size, void* d_ws, size_t ws_size, hipStream_t stream) {
  (void)hipFuncSetAttribute((const void*)fused, hipFuncAttributeMaxDynamicSharedMemorySize, LDS_BYTES);
  fused<<<256, 512, LDS_BYTES, stream>>>((const u16*)d_in[0], (const u16*)d_in[1], (const u16*)d_in[2], (const u16*)d_in[4], (const u16*)d_in[5],
                                         (const u16*)d_in[6], (const u16*)d_in[7], (char*)d_ws, (u16*)d_out);
}
```

```cpp
#define NO_PFH
#include <hip/hip_runtime.h>
#include <stdint.h>

#define ISSK 12
#define CODE_LEN 33792

typedef unsigned short u16;
typedef unsigned long long u64;
typedef short s16x4 __attribute__((ext_vector_type(4)));
typedef __bf16 bf16x2 __attribute__((ext_vector_type(2)));
typedef __bf16 bf16x4 __attribute__((ext_vector_type(4)));
typedef __bf16 bf16x8 __attribute__((ext_vector_type(8)));
typedef float f32x2 __attribute__((ext_vector_type(2)));
typedef float f32x4 __attribute__((ext_vector_type(4)));
typedef unsigned u32x2 __attribute__((ext_vector_type(2)));
typedef unsigned u32x4 __attribute__((ext_vector_type(4)));
#define LAS __attribute__((address_space(3)))
#define GAS __attribute__((address_space(1)))
#define DI __device__ __forceinline__

constexpr int S = 2048, D = 512, F = 1376;
constexpr float EPS = 1e-5f;
constexpr unsigned HT_BYTES = 32u * 64u * 1024u;
constexpr unsigned G_BYTES = 32u * 43u * 1024u;
constexpr unsigned O_BYTES = 128u * 16u * 1024u;
constexpr size_t WS_EPOCH = 0;
constexpr size_t WS_C1 = 4096;
constexpr size_t WS_C2 = WS_C1 + 128 * 128;
constexpr size_t WS_C3 = WS_C2 + 256 * 128;
constexpr size_t WS_C4 = WS_C3 + 1024 * 128;
constexpr size_t WS_C5 = WS_C4 + 256 * 128;
constexpr size_t WS_STAT = WS_C5 + 1024;
constexpr size_t WS_HT = WS_STAT + 16384;
constexpr size_t WS_G = WS_HT + HT_BYTES;
constexpr size_t WS_O = WS_G + G_BYTES;
constexpr size_t WS_BASE = WS_O + O_BYTES;
constexpr int SC1 = 16;
constexpr unsigned OOB = 0x80000000u;

typedef __amdgpu_buffer_rsrc_t rsrc_t;
DI rsrc_t mk_rsrc(const void* p, unsigned bytes) { return __builtin_amdgcn_make_buffer_rsrc((void*)p, 0, bytes, 0x00020000); }
DI u32x4 mk_srd(const void* p, unsigned bytes) {
  const uint64_t a = (uint64_t)p;
  u32x4 r = {(unsigned)a, (unsigned)(a >> 32) & 0xffffu, bytes, 0x00020000u};
  return r;
}
DI void glds16(u32x4 srd, unsigned voff, unsigned soff, unsigned lds) {
  asm volatile("s_mov_b32 m0, %3\n\ts_nop 0\n\tbuffer_load_dwordx4 %0, %1, %2 offen lds" ::"v"(voff), "s"(srd), "s"(soff), "s"(lds) : "memory");
}
DI void glds16x3(u32x4 srd, unsigned voff, unsigned soff, unsigned lds) {
  asm volatile("s_mov_b32 m0, %3\n\ts_nop 0\n\tbuffer_load_dwordx4 %0, %1, %2 offen lds\n\tbuffer_load_dwordx4 %0, %1, %2 offen offset:1024 lds\n\t"
               "buffer_load_dwordx4 %0, %1, %2 offen offset:2048 lds" ::"v"(voff), "s"(srd), "s"(soff), "s"(lds) : "memory");
}
#define WAITV(n) asm volatile("s_waitcnt vmcnt(" #n ")" ::: "memory")
#define FENCE() asm volatile("" ::: "memory")
#define SCHED() __builtin_amdgcn_sched_barrier(0)
#define DRAIN() __builtin_amdgcn_s_waitcnt(0x0F70)
#define RBAR() do { FENCE(); __builtin_amdgcn_s_barrier(); FENCE(); } while (0)
#define BAR() do { asm volatile("s_waitcnt lgkmcnt(0)" ::: "memory"); __builtin_amdgcn_s_barrier(); FENCE(); } while (0)

typedef LAS char* lp;
DI lp LP(unsigned a) { return (lp)(uintptr_t)a; }
DI bf16x8 cat8(bf16x4 a, bf16x4 b) { return __builtin_shufflevector(a, b, 0, 1, 2, 3, 4, 5, 6, 7); }
DI bf16x8 l128(lp p) { return *(const LAS bf16x8*)p; }
DI f32x4 lf4(lp p) { return *(const LAS f32x4*)p; }
DI bf16x4 ltr(lp p) { return __builtin_amdgcn_ds_read_tr16_b64_v4bf16((LAS bf16x4*)p); }
DI bf16x8 ltr2(lp p, int d) { return cat8(ltr(p), ltr(p + d)); }
DI f32x4 mfma32(bf16x8 a, bf16x8 b, f32x4 c) { return __builtin_amdgcn_mfma_f32_16x16x32_bf16(a, b, c, 0, 0, 0); }
DI void mfma_ip(f32x4& c, bf16x8 a, bf16x8 b) { asm volatile("v_mfma_f32_16x16x32_bf16 %0, %1, %2, %0" : "+v"(c) : "v"(a), "v"(b)); }
DI void mfma_k(bool first, f32x4& c, bf16x8 a, bf16x8 b) { if (first) c = mfma32(a, b, c); else mfma_ip(c, a, b); }
#define MFMA_DONE() asm volatile("s_nop 7\n\ts_nop 3" ::: "memory")
DI f32x4 mfma16(u32x2 a, u32x2 b, f32x4 c) { return __builtin_amdgcn_mfma_f32_16x16x16bf16_1k(__builtin_bit_cast(s16x4, a), __builtin_bit_cast(s16x4, b), c, 0, 0, 0); }
DI unsigned pkv(f32x2 f) { return __builtin_bit_cast(unsigned, __builtin_convertvector(f, bf16x2)); }
DI unsigned pk2(float a, float b) { f32x2 f = {a, b}; return pkv(f); }
DI f32x2 lo2(f32x4 v) { return __builtin_shufflevector(v, v, 0, 1); }
DI f32x2 hi2(f32x4 v) { return __builtin_shufflevector(v, v, 2, 3); }
DI f32x2 fma2(f32x2 a, f32x2 b, f32x2 c) { return __builtin_elementwise_fma(a, b, c); }
DI f32x2 bc2(float a) { f32x2 r = {a, a}; return r; }
DI float bflo(unsigned u) { return __uint_as_float(u << 16); }
DI float bfhi(unsigned u) { return __uint_as_float(u & 0xffff0000u); }
template <int CTRL> DI float dpp(float v) { return __uint_as_float(__builtin_amdgcn_update_dpp(0u, __float_as_uint(v), CTRL, 0xf, 0xf, true)); }
DI float sum16(float v) { v += dpp<0xB1>(v); v += dpp<0x4E>(v); v += dpp<0x141>(v); v += dpp<0x140>(v); return v; }
DI f32x2 swap16(float a, float b) { auto r = __builtin_amdgcn_permlane16_swap(__float_as_uint(a), __float_as_uint(b), false, false); f32x2 o = {__uint_as_float(r[0]), __uint_as_float(r[1])}; return o; }
DI f32x2 swap32(float a, float b) { auto r = __builtin_amdgcn_permlane32_swap(__float_as_uint(a), __float_as_uint(b), false, false); f32x2 o = {__uint_as_float(r[0]), __uint_as_float(r[1])}; return o; }
DI float sel(unsigned m, float a, float b) { return __uint_as_float((__float_as_uint(a) & m) | (__float_as_uint(b) & ~m)); }
DI u32x4 bload(rsrc_t r, unsigned v, unsigned s) { return __builtin_amdgcn_raw_buffer_load_b128(r, v, s, 0); }
DI u32x4 bload1(rsrc_t r, unsigned v, unsigned s) { return __builtin_amdgcn_raw_buffer_load_b128(r, v, s, SC1); }
DI void bstore1(u32x4 d, rsrc_t r, unsigned v, unsigned s) { __builtin_amdgcn_raw_buffer_store_b128(d, r, v, s, SC1); }
DI bf16x8 as_bf(u32x4 v) { return __builtin_bit_cast(bf16x8, v); }
DI u64 uni64(u64 v) { return ((u64)(unsigned)__builtin_amdgcn_readfirstlane((unsigned)(v >> 32)) << 32) | (unsigned)__builtin_amdgcn_readfirstlane((unsigned)v); }

constexpr unsigned LDS_BYTES = 163840;
constexpr unsigned QXB = 14336;
constexpr unsigned QST = QXB + 24576;
constexpr unsigned QSTAT = 4 * QST;
constexpr unsigned LNROW = 131072;
constexpr unsigned G1STG = 0;
constexpr unsigned P2STG = 49152;
constexpr unsigned SY = LDS_BYTES - 512;
enum { Y_FULL = 0, Y_FREE = 16, Y_HA = 32, Y_HB = 48, Y_ST = 64, Y_QS = 80, Y_ISS = 96, Y_E2 = 112, Y_RA = 128, Y_OK2 = 144, Y_OK3 = 160, Y_OK4 = 176, Y_WO = 192,
       Y_N1 = 208, Y_N2 = 212, Y_N3 = 216, Y_RB = 224, Y_SIG = 240 };
static_assert(QSTAT + 896 <= SY && P2STG + 98304 <= SY, "lds");
constexpr unsigned SPIN_GLB = 1u << 17;

template <int OFF> DI void sy_put(unsigned a, unsigned v) { asm volatile("ds_write_b32 %0, %1 offset:%2" ::"v"(a), "v"(v), "i"(OFF) : "memory"); }
template <int OFF> DI void sy_wait(unsigned a4, unsigned target) {
  unsigned t, n;
  asm volatile("s_mov_b32 %1, 0x200000\n"
               "1:\n\tds_read_b32 %0, %2 offset:%4\n\ts_waitcnt lgkmcnt(0)\n\tv_cmp_gt_u32 vcc, %3, %0\n\ts_cbranch_vccz 2f\n\t"
               "s_sleep 1\n\ts_sub_u32 %1, %1, 1\n\ts_cmp_lg_u32 %1, 0\n\ts_cbranch_scc1 1b\n"
               "2:" : "=&v"(t), "=&s"(n) : "v"(a4), "s"(target), "i"(OFF) : "vcc", "scc", "memory");
}
DI bool last_of(unsigned cnt, unsigned n, int lane) {
  unsigned old = 0;
  if (lane == 0) asm volatile("ds_add_rtn_u32 %0, %1, %2\n\ts_waitcnt lgkmcnt(0)" : "=&v"(old) : "v"(cnt), "v"(1u) : "memory");
  return (unsigned)__builtin_amdgcn_readfirstlane(old) == n - 1;
}
DI void gadd(char* p) { (void)__hip_atomic_fetch_add((GAS u64*)p, 1ull, __ATOMIC_RELAXED, __HIP_MEMORY_SCOPE_AGENT); }
DI u64 gpeek(const char* p) {
  u64 z = 0;
  asm volatile("" : "+v"(z));
  return __hip_atomic_fetch_add((GAS u64*)p, z, __ATOMIC_RELAXED, __HIP_MEMORY_SCOPE_AGENT);
}
template <int SL> DI void gwait(const char* p, bool on, u64 mask, u64 want) {
  FENCE();
  bool ok = !on;
#pragma unroll 1
  for (unsigned n = 0; n < SPIN_GLB; n++) {
    if (!ok) ok = (gpeek(p) & mask) == want;
    if (__all(ok)) break;
    __builtin_amdgcn_s_sleep(SL);
  }
  FENCE();
}

#define ST(i)
#define STDECL
#define STFLUSH()

__global__ __launch_bounds__(512, 2) void fused(const u16* __restrict__ x, const u16* __restrict__ wqkv, const u16* __restrict__ wo, const u16* __restrict__ w1,
                                                const u16* __restrict__ b1, const u16* __restrict__ w2, const u16* __restrict__ b2, char* __restrict__ ws,
                                                u16* __restrict__ out) {
  extern __shared__ __attribute__((aligned(16))) char smem[];
  uint64_t pc0; asm volatile("s_getpc_b64 %0" : "=s"(pc0));
  const unsigned L0 = (unsigned)(uintptr_t)(LAS char*)smem;
  const int tid = threadIdx.x, lane = tid & 63;
  const int W = __builtin_amdgcn_readfirstlane(tid >> 6), w = W & 3;
  const int xcd = blockIdx.x & 7, idx = blockIdx.x >> 3;
  const int sq = xcd >> 1, hh = xcd & 1;
  const int st = sq * 8 + (idx >> 2), ct = hh * 4 + (idx & 3);
  const int gi = idx;
  const int rt = (22 * sq + gi) >> 2, hg = hh * 4 + ((22 * sq + gi) & 3);
  const int c = lane & 15, g = lane >> 4, r8 = lane >> 3, pc = lane & 7;
  const f32x4 z4 = {0.f, 0.f, 0.f, 0.f};
  STDECL ST(0);
  const unsigned pfc = __builtin_amdgcn_raw_buffer_load_b32(mk_rsrc((const void*)(pc0 & ~255ull), CODE_LEN), ((idx & 7) * 8 + W) * 1024 + (lane & 7) * 128, 0, 0);
  {
    const unsigned on = (W >> 1) & 1;
    *(LAS unsigned*)LP(L0 + (on ? 0 : SY) + ((tid >> 5) & 3) * (on ? QST : 128) + (tid & 31) * 4) = on ? 0x3f803f80u : 0u;
  }
  const unsigned a4 = L0 + SY + (lane & 3) * 4, a1 = L0 + SY + w * 4;
  asm volatile("" ::"s"(x), "s"(wqkv), "s"(wo), "s"(w1), "s"(b1), "s"(w2), "s"(b2), "s"(ws), "s"(out));
  const u32x4 sx = mk_srd(x, S * D * 2), sw = mk_srd(wqkv, D * 3 * D * 2);
  const unsigned vx0 = (unsigned)((96 * rt - 16 + 8 * w + r8) * 1024 + ((pc ^ (((w & 1) << 2) | (r8 >> 1))) << 4));
  const unsigned vsw = (((pc >> 1) ^ (((r8 >> 1) & 1) | ((w & 1) << 1))) << 5) | ((pc & 1) << 4);
  const unsigned vwq = (8 * w + r8) * 3072 + hg * 128 + vsw;
  auto head = [&](int s) { glds16x3(sw, vwq, s * 64 * 3072, L0 + (s & 3) * QST + QXB + w * 3072); };
  auto rest = [&](int s) {
    const unsigned b = L0 + (s & 3) * QST + w * 1024;
    if (lane >= 8 || w != 0) glds16(sx, vx0, s * 128, b);
    glds16(sx, vx0 + 32768u, s * 128, b + 4096);
    glds16(sx, vx0 + 65536u, s * 128, b + 8192);
    if (w < 2) glds16(sx, vx0 + 98304u, s * 128, b + 12288);
    glds16x3(sw, vwq, s * 64 * 3072 + 32 * 3072, b + QXB + 12288 + w * 2048);
  };
  const int un = sq * 16 + hh * 8 + gi - 24;
  u32x4 xr[4];
  ST(11);
  if (gi >= 24) {
    asm volatile("buffer_load_dwordx4 %0, %4, %5, %6 offen\n\tbuffer_load_dwordx4 %1, %4, %5, %6 offen offset:1024\n\t"
                 "buffer_load_dwordx4 %2, %4, %5, %6 offen offset:2048\n\tbuffer_load_dwordx4 %3, %4, %5, %6 offen offset:3072"
                 : "=&v"(xr[0]), "=&v"(xr[1]), "=&v"(xr[2]), "=&v"(xr[3]) : "v"(lane * 16), "s"(sx), "s"((un * 32 + 4 * W) * 1024) : "memory");
  } else {
    asm volatile("" : "=v"(xr[0]), "=v"(xr[1]), "=v"(xr[2]), "=v"(xr[3]));
    if (gi < 22 && W >= 4) { head(0); rest(0); }
  }
  BAR();
  u64 tag = 0;

  if (gi >= 24) {
    const u64 ep = __hip_atomic_load((const GAS u64*)(ws + WS_EPOCH), __ATOMIC_RELAXED, __HIP_MEMORY_SCOPE_AGENT);
    const unsigned pfw = *(const GAS unsigned*)((lane & 31) < 24 ? (const char*)wqkv + (un * 8 + W) * 3072 + (lane & 31) * 128 : (const char*)wo + (un * 8 + W) * 1024 + (lane & 7) * 128);
    WAITV(2);
    asm volatile("" : "+v"(xr[0]), "+v"(xr[1]), "+v"(xr[2]), "+v"(xr[3]));
    f32x2 v[4][4];
    float p[4], s1[4], s2[4];
    auto red4 = [&](const float (&a)[4]) {
      f32x2 t = swap16(a[0], a[1]), u = swap16(a[2], a[3]);
      t = swap32(t[0] + t[1], u[0] + u[1]);
      return sum16(t[0] + t[1]);
    };
#pragma unroll
    for (int rr = 0; rr < 4; rr++) {
      p[rr] = bflo(__builtin_amdgcn_readfirstlane(xr[rr][0]));
      f32x2 a = {0.f, 0.f}, b = {0.f, 0.f};
#pragma unroll
      for (int j = 0; j < 4; j++) { const f32x2 t = {bflo(xr[rr][j]), bfhi(xr[rr][j])}; v[rr][j] = t - bc2(p[rr]); a += v[rr][j]; b = fma2(v[rr][j], v[rr][j], b); }
      s1[rr] = a[0] + a[1]; s2[rr] = b[0] + b[1];
    }
    if (W == 0) {
      const size_t o = lane < 2 ? WS_C1 + (un * 2 + lane) * 128 : WS_C2 + (un * 24 + lane - 2) * 128;
      u64 e = ep;
      asm volatile("" : "+v"(e));
      if (lane < (un == 63 ? 27 : 26)) __hip_atomic_store((GAS u64*)(ws + o), (e + 1) << 16, __ATOMIC_RELAXED, __HIP_MEMORY_SCOPE_AGENT);
    }
    {
      const float m = red4(s1) * (1.f / D), var = fmaxf(red4(s2) * (1.f / D) - m * m, 0.f);
      const float r = __builtin_amdgcn_rsqf(var + EPS);
      const float a = r * __builtin_amdgcn_rsqf(var * r * r + EPS), ma = -m * a;
#pragma unroll
      for (int rr = 0; rr < 4; rr++) {
        const f32x2 av = bc2(__uint_as_float(__builtin_amdgcn_readlane(__float_as_uint(a), 16 * rr))), cv = bc2(__uint_as_float(__builtin_amdgcn_readlane(__float_as_uint(ma), 16 * rr)));
        u32x4 h;
#pragma unroll
        for (int j = 0; j < 4; j++) h[j] = pkv(fma2(v[rr][j], av, cv));
        *(LAS u32x4*)LP(L0 + (4 * W + rr) * 1088 + ((W >> 1) & 1) * 32 + lane * 16) = h;
      }
      const float pg = g == 0 ? p[0] : g == 1 ? p[1] : g == 2 ? p[2] : p[3];
      *(LAS float*)LP(L0 + QSTAT + (W >> 1) * 64 + (W & 1) * 16 + g * 4 + (lane & 1) * 32) = (lane & 1) ? (m + pg) * r : r;
    }
    ST(12);
    {
      u64 e = ep;
      asm volatile("" : "+v"(e) : "v"(pfw));
      tag = (uni64(e) + 1) << 16;
    }
    char* const c1p = ws + WS_C1 + (sq * 32 + (lane & 31)) * 128;
    u64 chk = 0;
    if (W == 1 && lane < 32) asm volatile("global_atomic_add_x2 %0, %1, %0, off sc0" : "+v"(chk) : "v"(c1p) : "memory");
    BAR();
    {
      bf16x8 tv[4];
#pragma unroll
      for (int n = 0; n < 4; n++) tv[n] = ltr2(LP(L0 + (8 * g + (c >> 2)) * 1088 + (g & 1) * 32 + W * 128 + (c & 3) * 8) + n * 32, 4 * 1088);
      const u32x4 sv = *(const LAS u32x4*)LP(L0 + QSTAT + c * 16);
      const rsrc_t rht = mk_rsrc(ws + WS_HT, HT_BYTES);
#pragma unroll
      for (int n = 0; n < 4; n++) bstore1(__builtin_bit_cast(u32x4, tv[n]), rht, lane * 16, ((4 * W + n) * 64 + un) * 1024);
      if (W == 2) bstore1(sv, mk_rsrc(ws + WS_STAT, S * 8), lane < 16 ? un * 256 + lane * 16 : OOB, 0);
    }
    ST(1);
    if (W >= 4) {
      const u32x4 swo = mk_srd(wo, D * D * 2);
#pragma unroll 1
      for (int n = 0; n < 16; n++) glds16(swo, (8 * w + r8) * 1024 + (hh * 2 + (gi & 1)) * 256 + vsw, n * 32768 + 128, L0 + 65536 + (n * 4 + w) * 1024);
      WAITV(16);
    } else WAITV(0);
    if (W == 1) { asm volatile("" : "+v"(chk)); if (__any(lane < 32 && (chk >> 16) != (tag >> 16))) gwait<2>(c1p, lane < 32, ~0xffffull, tag); }
    if (last_of(L0 + SY + Y_N1, 8, lane)) { if (lane < 32) gadd(c1p); sy_put<Y_SIG>(a4, 1); }
    ST(2);
  } else if (gi >= 22) {
    unsigned p3[3] = {0, 0, 0};
    if (W >= 4) {
#pragma unroll
      for (int n = 0; n < 3; n++) p3[n] = __builtin_amdgcn_raw_buffer_load_b32(mk_rsrc(wqkv, D * 3 * D * 2), hh ? OOB : ((n * 8 + (gi - 22) * 4 + w) * 64 + lane) * 1024 + 384, 0, 0);
    } else if (W > 0) {
#pragma unroll
      for (int n = 0; n < 2; n++) p3[n] = __builtin_amdgcn_raw_buffer_load_b32(mk_rsrc(x, S * D * 2), (unsigned)((((22 * sq) >> 2) * 96 - 16 + (n * 6 + (gi - 22) * 3 + W - 1) * 52 + min(lane, 51)) * 1024 + 384), 0, 0);
    }
    tag = (uni64(__hip_atomic_load((const GAS u64*)(ws + WS_EPOCH), __ATOMIC_RELAXED, __HIP_MEMORY_SCOPE_AGENT)) + 1) << 16;
    if (W < 4) sy_put<Y_HA>(a1, 1);
    asm volatile("" ::"v"(p3[0]), "v"(p3[1]), "v"(p3[2]));
  }

  if (W < 4) {
    if (gi < 22) {
      f32x4 aq[7], ak[7], av[7];
#pragma unroll
      for (int b = 0; b < 7; b++) { ak[b] = z4; av[b] = z4; aq[b] = z4; }
      const unsigned ax = L0 + c * 128 + ((g ^ (c >> 1)) << 4);
      const unsigned aw = L0 + QXB + g * 3072 + (c >> 2) * 128 + ((w ^ ((c >> 3) | ((g & 1) << 1))) << 5) + (c & 3) * 8;
      bf16x8 xf[2][7];
      bf16x4 wl[2][3], wh[2][3];
      const lp pxa[2][2] = {{LP(ax), LP(ax ^ 64)}, {LP(ax + 2 * QST), LP((ax ^ 64) + 2 * QST)}}, pwa[2] = {LP(aw), LP(aw + 2 * QST)};
      auto RD = [&](int n, int i, int slot, int kk) {
        const lp px = pxa[slot >> 1][kk] + (slot & 1) * QST, pw = pwa[slot >> 1] + (slot & 1) * QST + kk * 12288;
        if (i == 0) wl[n][1] = ltr(pw + 1024);
        if (i == 1) wh[n][1] = ltr(pw + 1024 + 512);
        if (i == 2) xf[n][6] = l128(px + 12288);
        if (i == 3) wl[n][2] = ltr(pw + 2048);
        if (i == 4) wh[n][2] = ltr(pw + 2048 + 512);
        if (i == 5) wl[n][0] = ltr(pw);
        if (i == 6) wh[n][0] = ltr(pw + 512);
        if (i >= 7) xf[n][12 - i] = l128(px + (12 - i) * 2048);
      };
      auto MM = [&](int cur, int m) {
        const int t = 6 - m / 3;
        if (m % 3 == 0) mfma_ip(ak[t], cat8(wl[cur][1], wh[cur][1]), xf[cur][t]);
        if (m % 3 == 1) mfma_ip(av[t], xf[cur][t], cat8(wl[cur][2], wh[cur][2]));
        if (m % 3 == 2) mfma_ip(aq[t], cat8(wl[cur][0], wh[cur][0]), xf[cur][t]);
      };
      RBAR();
      ST(2);
#pragma unroll
      for (int i = 0; i < 13; i++) RD(0, i, 0, 0);
      SCHED();
#pragma unroll 1
      for (int s4 = 0; s4 < 8; s4 += 4) {
#pragma unroll
        for (int sl = 0; sl < 4; sl++) {
#pragma unroll
          for (int m = 0; m < 21; m++) {
            MM(0, m); SCHED();
            if (m < 13) RD(1, m, sl, 1);
            if (m == 19 && (sl < 3 || s4 == 0)) BAR();
            SCHED();
          }
#pragma unroll
          for (int m = 0; m < 21; m++) {
            MM(1, m); SCHED();
            if (m < 13) RD(0, m, (sl + 1) & 3, 0);
            SCHED();
          }
        }
      }
      MFMA_DONE();
      SCHED();
      ST(3);
      f32x2 cq[2], ck[2];
#pragma unroll
      for (int j = 0; j < 4; j++) { cq[j >> 1][j & 1] = -dpp<0x150>(aq[0][j]); ck[j >> 1][j & 1] = -dpp<0x150>(ak[0][j]); }
      f32x2 cvt = swap16(av[0][0], av[0][0]);
      cvt = swap32(cvt[0], cvt[0]);
      const f32x2 cv = bc2(-cvt[0]);
      sy_wait<Y_ST>(a4, 1);
      u32x2 qf[6], kf[7], vf[7];
      {
        float sr[7], sm[7]; f32x4 vr[7], vm[7];
        const lp pcs = LP(L0 + QSTAT + (c >> 3) * 64 + (c & 7) * 4), prs = LP(L0 + QSTAT + (g >> 1) * 64 + (g & 1) * 16);
#pragma unroll
        for (int t = 0; t < 7; t++) {
          sr[t] = *(const LAS float*)(pcs + t * 128); sm[t] = *(const LAS float*)(pcs + t * 128 + 32);
          vr[t] = lf4(prs + t * 128); vm[t] = lf4(prs + t * 128 + 32);
        }
        SCHED();
#pragma unroll
        for (int t = 0; t < 7; t++) {
          const f32x2 r = bc2(sr[t]), m = bc2(sm[t]);
          f32x2 y[6] = {m * ck[0], m * ck[1], m * cq[0], m * cq[1], lo2(vm[t]) * cv, hi2(vm[t]) * cv};
          SCHED();
          y[0] = fma2(r, lo2(ak[t]), y[0]); y[1] = fma2(r, hi2(ak[t]), y[1]); y[2] = fma2(r, lo2(aq[t]), y[2]); y[3] = fma2(r, hi2(aq[t]), y[3]);
          y[4] = fma2(lo2(vr[t]), lo2(av[t]), y[4]); y[5] = fma2(hi2(vr[t]), hi2(av[t]), y[5]);
          SCHED();
          kf[t][0] = pkv(y[0]); kf[t][1] = pkv(y[1]);
          if (t > 0) { qf[t - 1][0] = pkv(y[2]); qf[t - 1][1] = pkv(y[3]); }
          vf[t][0] = pkv(y[4]); vf[t][1] = pkv(y[5]);
        }
        SCHED();
      }
      u32x2 op[6];
      {
        unsigned om = 0u - (unsigned)(c & 1);
        asm("" : "+v"(om));
        const bool cur0 = 4 * g + (c & 1) <= c, cur1 = 4 * g + (c & 1) + 2 <= c;
        constexpr float SC = 0.25f * 1.4426950408889634f;
        f32x4 sp[6], su[6];
#pragma unroll
        for (int t = 0; t < 6; t++) { sp[t] = mfma16(kf[t], qf[t], z4); su[t] = mfma16(kf[t + 1], qf[t], z4); }
        float l0[6], l1[6], mx[8];
#pragma unroll
        for (int t = 0; t < 6; t++) {
          const float p0 = sel(om, sp[t][1], sp[t][0]), p1 = sel(om, sp[t][3], sp[t][2]);
          const float u0 = sel(om, su[t][1], su[t][0]), u1 = sel(om, su[t][3], su[t][2]);
          l0[t] = cur0 ? u0 : p0; l1[t] = cur1 ? u1 : p1;
          if (t == 0 && rt == 0) { l0[t] = cur0 ? l0[t] : -4e9f; l1[t] = cur1 ? l1[t] : -4e9f; }
          mx[t] = fmaxf(l0[t], l1[t]);
        }
        {
          const f32x2 a = swap16(mx[0], mx[1]), b = swap16(mx[2], mx[3]), a2 = swap16(mx[4], mx[5]);
          const float n2 = fmaxf(a2[0], a2[1]);
          const f32x2 d = swap32(fmaxf(a[0], a[1]), fmaxf(b[0], b[1])), d2 = swap32(n2, n2);
          const float m = fmaxf(d[0], d[1]) * -SC, m2 = fmaxf(d2[0], d2[1]) * -SC;
          const f32x2 e = swap32(m, m), f0 = swap16(e[0], e[0]), f1 = swap16(e[1], e[1]), f2 = swap16(m2, m2);
          mx[0] = f0[0]; mx[1] = f0[1]; mx[2] = f1[0]; mx[3] = f1[1]; mx[4] = f2[0]; mx[5] = f2[1];
        }
        const u32x4 ones = {0x3f803f80u, 0x3f803f80u, 0x3f803f80u, 0x3f803f80u};
        const unsigned keep = om ^ 0x0000ffffu;
        const unsigned k00 = cur0 ? 0u : keep, k01 = cur1 ? 0u : keep, k10 = cur0 ? keep : 0u, k11 = cur1 ? keep : 0u;
        f32x4 o[6], den[6];
#pragma unroll
        for (int t = 0; t < 6; t++) {
          const float e0 = __builtin_amdgcn_exp2f(__builtin_fmaf(l0[t], SC, mx[t])), e1 = __builtin_amdgcn_exp2f(__builtin_fmaf(l1[t], SC, mx[t]));
          const unsigned b0 = pk2(e0, e0), b1v = pk2(e1, e1);
          const u32x4 pu = {b0 & k00, b1v & k01, b0 & k10, b1v & k11}, vv = {vf[t][0], vf[t][1], vf[t + 1][0], vf[t + 1][1]};
          o[t] = mfma32(as_bf(vv), as_bf(pu), z4);
          den[t] = mfma32(as_bf(ones), as_bf(pu), z4);
        }
#pragma unroll
        for (int t = 0; t < 6; t++) {
          const f32x2 inv = bc2(__builtin_amdgcn_rcpf(den[t][0]));
          op[t][0] = pkv(lo2(o[t]) * inv); op[t][1] = pkv(hi2(o[t]) * inv);
        }
      }
      {
        const rsrc_t ro = mk_rsrc(ws + WS_O, O_BYTES);
#pragma unroll
        for (int t = 0; t < 6; t += 2) {
          const auto r0 = __builtin_amdgcn_permlane16_swap(op[t][0], op[t + 1][0], false, false);
          const auto r1 = __builtin_amdgcn_permlane16_swap(op[t][1], op[t + 1][1], false, false);
          const u32x4 v = {r0[0], r1[0], r0[1], r1[1]};
          if (t == 0 || rt < 21) bstore1(v, ro, (g & 1) * 16384 + ((g >> 1) * 16 + c) * 16, ((6 * rt + t) * 16 + 2 * hg + (w >> 1)) * 1024 + (w & 1) * 512);
        }
      }
      ST(13);
      sy_put<Y_QS>(a1, 1);
      WAITV(0);
      if (last_of(L0 + SY + Y_N2, 4, lane) && lane < (rt < 21 ? 12 : 4)) gadd(ws + WS_C2 + (12 * rt + lane) * 128);
      ST(4);
    } else {
      const rsrc_t rw2 = mk_rsrc(w2, F * S * 2);
      if (gi >= 24) {
        sy_wait<Y_OK2>(a4, 1);
        const int m = gi - 24 + 8 * r8;
        const unsigned t = __builtin_amdgcn_raw_buffer_load_b32(rw2, m < 22 ? ((44 * w + 2 * m + (hh ^ 1)) * 8 + pc) * 4096 + sq * 1024 + 384 : OOB, 0, 0);
        asm volatile("" ::"v"(t));
      } else {
        unsigned pv[5];
#pragma unroll
        for (int n = 0; n < 5; n++) pv[n] = __builtin_amdgcn_raw_buffer_load_b32(rw2, ((44 * w + 2 * (gi - 22 + 2 * n) + hh) * 8 + r8) * 4096 + sq * 1024 + pc * 128, 0, 0);
        asm volatile("" ::"v"(pv[0]), "v"(pv[1]), "v"(pv[2]), "v"(pv[3]), "v"(pv[4]));
      }
      ST(4);
    }
    asm volatile("" ::"v"(pfc));

    const rsrc_t rgg = mk_rsrc(ws + WS_G, G_BYTES);
    const unsigned vg = lane * 16 + (4 * ct * 43 + 11 * w) * 1024;
    const u32x4 sw2 = mk_srd(w2, F * S * 2);
    const unsigned vw2 = (352 * w + r8) * 4096 + st * 128 + ((((pc >> 1) ^ ((r8 >> 1) & 1)) << 5) | ((pc & 1) << 4));
    const unsigned lw = L0 + P2STG + w * 24576;
    const unsigned ar = lw + (8 * g + (c >> 2)) * 128 + ((((c >> 3) | ((g & 1) << 1))) << 5) + (c & 3) * 8;
    u32x4 gf[11][4];
    auto GW = [&](int k, int slot) {
#pragma unroll
      for (int j = 0; j < 4; j++) glds16(sw2, (k == 10 && w == 3) ? OOB : vw2 ^ ((j & 1) << 6), (32 * k + 8 * j) * 4096, lw + slot * 4096 + j * 1024);
    };
    auto LG1 = [&](int k, int j) { gf[k][j] = bload1(rgg, (k == 10 && w == 3) ? OOB : vg, j * 43 * 1024 + k * 1024); };
    lp pr[4];
#pragma unroll
    for (int j = 0; j < 4; j++) pr[j] = LP(ar ^ (((w + j) & 3) << 5));
    bf16x8 tw[2][4];
    bf16x8 tk[4][4];
    auto TR = [&](bf16x8 (&t)[4], int slot) {
#pragma unroll
      for (int j = 0; j < 4; j++) t[j] = ltr2(pr[j] + slot * 4096, 512);
      asm volatile("" ::"v"(t[0]), "v"(t[1]), "v"(t[2]), "v"(t[3]) : "memory");
    };
    if (gi < 22) sy_wait<Y_QS>(a4, 1);
    sy_wait<Y_ISS>(a4, 1);
    ST(5);
    const int srow = 64 * st + 16 * w + c;
    u32x4 xq[2];
    asm volatile("buffer_load_dwordx4 %0, %2, %3, 0 offen\n\tbuffer_load_dwordx4 %1, %2, %3, 0 offen offset:64"
                 : "=&v"(xq[0]), "=&v"(xq[1]) : "v"((srow * D + 64 * ct + 16 * (g & 1) + 8 * (g >> 1)) * 2), "s"(sx) : "memory");
    GW(7, 0); GW(8, 1);
    sy_wait<Y_ISS>(a4, 2);
    GW(9, 2); GW(10, 3); GW(4, 4); GW(5, 5);
    WAITV(20); TR(tk[0], 0); GW(0, 0);
    WAITV(20); TR(tk[1], 1); GW(1, 1);
    WAITV(20); TR(tk[2], 2); GW(2, 2);
    WAITV(20); TR(tk[3], 3); GW(3, 3);
    WAITV(12); TR(tw[0], 0); GW(6, 0);
    asm volatile("" : "+v"(xq[0]), "+v"(xq[1]));
    u32x2 xv[4];
#pragma unroll
    for (int i = 0; i < 2; i++) {
      const auto r0 = __builtin_amdgcn_permlane16_swap(xq[i][0], xq[i][2], false, false), r1 = __builtin_amdgcn_permlane16_swap(xq[i][1], xq[i][3], false, false);
      xv[2 * i][0] = r0[0]; xv[2 * i][1] = r1[0]; xv[2 * i + 1][0] = r0[1]; xv[2 * i + 1][1] = r1[1];
    }
    f32x4 acc[4][4];
#pragma unroll
    for (int i = 0; i < 4; i++) {
      acc[i][0] = (f32x4){bflo(xv[i][0]), bfhi(xv[i][0]), bflo(xv[i][1]), bfhi(xv[i][1])};
#pragma unroll
      for (int j = 1; j < 4; j++) acc[i][j] = z4;
      asm volatile("" : "+v"(acc[i][0]));
    }
    ST(6);
    sy_wait<Y_OK3>(a1, 1);
    ST(7);
    constexpr int GD = 4;
#pragma unroll
    for (int k = 0; k < GD; k++)
#pragma unroll
      for (int j = 0; j < 4; j++) LG1(k, j);
    WAITV(16);
    SCHED();
    u32x4 bv[2];
#pragma unroll
    for (int k = 0; k < 10; k++) {
      if (k + GD == 11) {
        sy_wait<Y_OK4>(a4, 1);
        const rsrc_t rb = mk_rsrc(ws + WS_BASE, S * D * 2);
#pragma unroll
        for (int i = 0; i < 2; i++) bv[i] = bload1(rb, lane * 16, ((4 * st + w) * 16 + 2 * ct + i) * 1024);
      }
#pragma unroll
      for (int i = 0; i < 4; i++) {
#pragma unroll
        for (int j = 0; j < 4; j++) {
          mfma_k(k == 0, acc[i][j], as_bf(gf[k][i]), k < 7 ? tw[k & 1][j] : tk[k < 7 ? 0 : k - 7][j]);
          if (j == 0 && k + 1 < 7) { SCHED(); tw[(k + 1) & 1][i] = ltr2(pr[i] + ((k + 1) % 6) * 4096, 512); SCHED(); }
        }
        SCHED();
        if (k + GD < 11) LG1(k + GD, i);
        SCHED();
      }
    }
#pragma unroll
    for (int jj = 1; jj <= 4; jj++) {
#pragma unroll
      for (int i = 0; i < 4; i++) {
        acc[i][jj & 3] = mfma32(as_bf(gf[10][i]), tk[3][jj & 3], acc[i][jj & 3]); SCHED();
        if (jj >= 2) *(LAS f32x4*)(LP(lw + lane * 16) + ((jj - 2) * 4 + i) * 1024) = acc[i][jj - 1];
        SCHED();
      }
    }
    ST(8);
    BAR();
    ST(9);
    f32x4 u[4];
#pragma unroll
    for (int i = 0; i < 4; i++) {
      u[i] = acc[i][0] + (f32x4){bflo(bv[i >> 1][2 * (i & 1)]), bfhi(bv[i >> 1][2 * (i & 1)]), bflo(bv[i >> 1][2 * (i & 1) + 1]), bfhi(bv[i >> 1][2 * (i & 1) + 1])};
#pragma unroll
      for (int j = 1; j < 4; j++) u[i] += lf4(LP(L0 + P2STG + ((w - j) & 3) * 24576 + lane * 16) + ((j - 1) * 4 + i) * 1024);
    }
#pragma unroll
    for (int i = 0; i < 4; i += 2) {
      const auto r0 = __builtin_amdgcn_permlane16_swap(pkv(lo2(u[i])), pkv(lo2(u[i + 1])), false, false);
      const auto r1 = __builtin_amdgcn_permlane16_swap(pkv(hi2(u[i])), pkv(hi2(u[i + 1])), false, false);
      const u32x4 o = {r0[0], r1[0], r0[1], r1[1]};
      *(u32x4*)(out + srow * D + 64 * ct + 16 * i + 16 * (g & 1) + 8 * (g >> 1)) = o;
    }
    ST(10);
  } else {
    if (gi < 22) {
      const bool c1on = lane < (w == 3 ? 4 : 1);
      const char* const c1w = ws + WS_C1 + (((w == 3 ? 0 : w) + (lane & 3)) * 32 + (blockIdx.x & 31)) * 128;
      u64 c1v = 0, ep = 0;
      head(1); rest(1);
#pragma unroll 1
      for (int s = 2; s < 8; s++) {
        head(s);
        WAITV(12); RBAR();
        rest(s);
      }
      ST(1);
      u32x4 stv4;
      auto c1_done = [&]() {
        asm volatile("" : "+v"(c1v), "+v"(ep));
        tag = (uni64(ep) + 1) << 16;
        if (__any(c1on && c1v != tag + 16)) gwait<2>(c1w, c1on, ~0ull, tag + 16);
        if (w == 3) stv4 = bload1(mk_rsrc(ws + WS_STAT, S * 8), (unsigned)((12 * rt - 2) * 64 + min(lane, 55) * 16), 0);
      };
      auto put_stats = [&]() { if (w == 3) { *(LAS u32x4*)LP(L0 + QSTAT + min(lane, 55) * 16) = stv4; sy_put<Y_ST>(a4, 1); } };
      const int ft = rt, dt = hg;
      const rsrc_t rht = mk_rsrc(ws + WS_HT, HT_BYTES);
      const u32x4 s1 = mk_srd(w1, S * F * 2);
      u32x4 fb[16][4];
      const unsigned sz = (((pc >> 1) ^ ((r8 >> 1) & 1)) << 5) | ((pc & 1) << 4), vwb = (512 * w + r8) * 2752 + ft * 128;
      const unsigned vw0 = (ft == 21 && sz >= 64) ? OOB : vwb + sz, vw1 = (ft == 21 && sz < 64) ? OOB : vwb + (sz ^ 64);
      const unsigned vb = lane * 16 + (4 * dt * 64 + 16 * w) * 1024;
      const unsigned lb = L0 + G1STG + w * 4096;
      const unsigned ar = lb + (8 * g + (c >> 2)) * 128 + ((((c >> 3) | ((g & 1) << 1))) << 5) + (c & 3) * 8;
      lp pe[4];
#pragma unroll
      for (int e = 0; e < 4; e++) pe[e] = LP(ar ^ (e << 5));
      auto GW = [&](int k, int j) { glds16(s1, (j & 1) ? vw1 : vw0, (32 * k + 8 * j) * 2752, lb + (k % 3) * 16384 + j * 1024); };
      auto LB = [&](int k, int j) { fb[k][j] = bload1(rht, vb, ((w + j) & 3) * 65536 + k * 1024); };
      asm volatile("global_load_dwordx2 %0, %1, off sc1" : "=v"(ep) : "v"(ws + WS_EPOCH) : "memory");
      if (c1on) asm volatile("global_atomic_add_x2 %0, %1, %0, off sc0" : "+v"(c1v) : "v"(c1w) : "memory");
      GW(0, 0); GW(0, 1);
      WAITV(13); RBAR();
      GW(0, 2); GW(0, 3); GW(1, 0); GW(1, 1);
      WAITV(8); RBAR();
      GW(1, 2); GW(1, 3);
      WAITV(8);
      ST(2);
      c1_done();
      ST(3);
      u32x2 bb[4];
      {
        const rsrc_t rb1 = mk_rsrc(b1, F * 2);
#pragma unroll
        for (int i = 0; i < 4; i++) bb[i] = __builtin_amdgcn_raw_buffer_load_b64(rb1, (64 * ft + 4 * g) * 2 + i * 32, 0, 0);
      }
      LB(0, 0); LB(0, 1); LB(0, 2); LB(0, 3);
      GW(2, 0); GW(2, 1); GW(2, 2); GW(2, 3);
      LB(1, 0); LB(1, 1); LB(1, 2); LB(1, 3);
      put_stats();
      LB(2, 0); LB(2, 1); LB(2, 2); LB(2, 3);
      LB(3, 0); LB(3, 1); LB(3, 2); LB(3, 3);
      WAITV(28);
      f32x4 acc[4][4];
#pragma unroll
      for (int i = 0; i < 4; i++) {
        acc[i][0] = (f32x4){bflo(bb[i][0]), bfhi(bb[i][0]), bflo(bb[i][1]), bfhi(bb[i][1])};
#pragma unroll
        for (int j = 1; j < 4; j++) acc[i][j] = z4;
      }
      bf16x4 al[2][4], ah[2][4];
#pragma unroll
      for (int e = 0; e < 4; e++) { al[0][e] = ltr(pe[e]); ah[0][e] = ltr(pe[e] + 512); }
      SCHED();
#pragma unroll
      for (int k = 0; k < 15; k++) {
#pragma unroll
        for (int m = 0; m < 16; m++) {
          const int i = m & 3, j = m >> 2;
          mfma_k(k == 0, acc[i][j], cat8(al[k & 1][i], ah[k & 1][i]), as_bf(fb[k][j]));
          SCHED();
          if (m == 5) {
            if (k == 0) WAITV(26); else if (k == 1) WAITV(22); else if (k < 12) WAITV(11); else if (k == 12) WAITV(10); else if (k == 13) WAITV(5); else WAITV(0);
          }
          if (m >= 6 && m < 14) {
            const int e = (m - 6) >> 1;
            if (m & 1) ah[(k + 1) & 1][e] = ltr(pe[e] + ((k + 1) % 3) * 16384 + 512); else al[(k + 1) & 1][e] = ltr(pe[e] + ((k + 1) % 3) * 16384);
          }
          if ((m == 4 || m == 6 || m == 9 || m == 13) && k + 3 < 16) GW(k + 3, m == 4 ? 0 : m == 6 ? 1 : m == 9 ? 2 : 3);
          if (i == 3 && k + 4 < 16) LB(k + 4, j);
          SCHED();
        }
        if (k == 3) ST(8);
        if (k == 7) ST(9);
        if (k == 11) ST(10);
        if (k == ISSK) sy_put<Y_ISS>(a1, 1);
      }
#pragma unroll
      for (int jj = 1; jj <= 4; jj++) {
#pragma unroll
        for (int i = 0; i < 4; i++) {
          acc[i][jj & 3] = mfma32(cat8(al[1][i], ah[1][i]), as_bf(fb[15][jj & 3]), acc[i][jj & 3]); SCHED();
          if (jj >= 2) *(LAS f32x4*)(LP(lb + lane * 16) + (jj - 2) * 16384 + i * 1024) = acc[i][jj - 1];
          SCHED();
        }
      }
      sy_put<Y_E2>(a1, 1);
      sy_put<Y_ISS>(a1, 2);
      __builtin_amdgcn_s_setprio(3);
      ST(4);
      const lp pin[3] = {LP(L0 + G1STG + ((w - 1) & 3) * 4096 + lane * 16), LP(L0 + G1STG + 16384 + ((w - 2) & 3) * 4096 + lane * 16), LP(L0 + G1STG + 32768 + ((w - 3) & 3) * 4096 + lane * 16)};
      sy_wait<Y_E2>(a4, 1);
      ST(5);
      f32x4 pt[4][3];
#pragma unroll
      for (int i = 0; i < 4; i++)
#pragma unroll
        for (int j = 0; j < 3; j++) pt[i][j] = lf4(pin[j] + i * 1024);
      SCHED();
      u32x2 gp[4];
#pragma unroll
      for (int i = 0; i < 4; i++) {
        const f32x4 u = (acc[i][0] + pt[i][0]) + (pt[i][1] + pt[i][2]);
#pragma unroll
        for (int hf = 0; hf < 2; hf++) {
          constexpr float K1 = -2.f * 0.7978845608028654f * 1.4426950408889634f, K2 = K1 * 0.044715f;
          const f32x2 t = hf ? hi2(u) : lo2(u);
          const f32x2 z = t * fma2(t * t, bc2(K2), bc2(K1));
          f32x2 e = {__builtin_amdgcn_exp2f(z[0]), __builtin_amdgcn_exp2f(z[1])};
          e += bc2(1.f);
          const f32x2 rc = {__builtin_amdgcn_rcpf(e[0]), __builtin_amdgcn_rcpf(e[1])};
          gp[i][hf] = pkv(t * rc);
        }
      }
      const rsrc_t rgg = mk_rsrc(ws + WS_G, G_BYTES);
#pragma unroll
      for (int i = 0; i < 4; i += 2) {
        const auto r0 = __builtin_amdgcn_permlane16_swap(gp[i][0], gp[i + 1][0], false, false);
        const auto r1v = __builtin_amdgcn_permlane16_swap(gp[i][1], gp[i + 1][1], false, false);
        const u32x4 v = {r0[0], r1v[0], r0[1], r1v[1]};
        if (i == 0 || ft < 21) bstore1(v, rgg, (2 * (g & 1) + (g >> 1)) * 256 + c * 16, ((4 * dt + w) * 43 + 2 * ft + (i >> 1)) * 1024);
      }
      ST(6);
      WAITV(0);
      DRAIN();
      {
        const int sl0 = (2 * ft) / 11, sl1 = (2 * ft + 1) / 11;
        if (last_of(L0 + SY + Y_N3, 4, lane) && (lane < 32 || sl1 != sl0)) gadd(ws + WS_C3 + (((lane & 31) * 8 + dt) * 4 + (lane < 32 ? sl0 : sl1)) * 128);
        sy_put<Y_ISS>(a1, 3);
      }
      ST(7);
    } else {
      if (gi >= 24) {
        unsigned pv[8];
        sy_wait<Y_SIG>(a4, 1);
        {
          const rsrc_t r1 = mk_rsrc(w1, S * F * 2);
          const unsigned c0 = ((22 * sq) >> 2) * 128;
#pragma unroll
          for (int n = 0; n < 8; n++) {
            const unsigned row = w * 512 + (2 * (gi - 24) + 16 * (n >> 1) + ((n & 1) ^ hh)) * 8 + r8;
            pv[n] = __builtin_amdgcn_raw_buffer_load_b32(r1, pc < 6 + (r8 & 1) ? ((row * 2752 + c0) & ~127u) + pc * 128 : OOB, 0, 0);
          }
        }
        const int so = sq * 4 + ((gi - 24) >> 1), dq = hh * 2 + (gi & 1);
        const u32x4 swo = mk_srd(wo, D * D * 2);
        const unsigned vwo = (8 * w + r8) * 1024 + dq * 256 + vsw;
#pragma unroll 1
        for (int n = 0; n < 16; n++) glds16(swo, vwo, n * 32768, L0 + (n * 4 + w) * 1024);
        const rsrc_t ro = mk_rsrc(ws + WS_O, O_BYTES), rb = mk_rsrc(ws + WS_BASE, S * D * 2);
        const int srow = 128 * so + 32 * w + c;
        const float bias0 = bflo(b2[srow]), bias1 = bflo(b2[srow + 16]);
        WAITV(0);
        asm volatile("" ::"v"(pv[0]), "v"(pv[1]), "v"(pv[2]), "v"(pv[3]), "v"(pv[4]), "v"(pv[5]), "v"(pv[6]), "v"(pv[7]));
        sy_put<Y_WO>(a1, 1);
        ST(8);
        gwait<4>(ws + WS_C2 + ((so * 4 + w) * 4 + dq) * 128, lane == 0, ~0ull, tag + 8);
        ST(3);
        u32x4 of[16][2];
        const unsigned vo = lane * 16 + (8 * so + 2 * w) * 16384;
        constexpr int OD = 8;
#pragma unroll
        for (int k = 0; k < OD; k++) { of[k][0] = bload1(ro, vo, k * 1024); of[k][1] = bload1(ro, vo, 16384 + k * 1024); }
        sy_wait<Y_WO>(a4, 1);
        const unsigned ar = L0 + (8 * g + (c >> 2)) * 128 + ((((c >> 3) | ((g & 1) << 1))) << 5) + (c & 3) * 8;
        lp pe[8];
#pragma unroll
        for (int e = 0; e < 8; e++) pe[e] = LP((ar ^ ((e & 3) << 5)) + (e >> 2) * 65536);
        f32x4 acc[8][2];
#pragma unroll
        for (int i = 0; i < 8; i++) { acc[i][0] = (f32x4){bias0, bias0, bias0, bias0}; acc[i][1] = (f32x4){bias1, bias1, bias1, bias1}; }
        bf16x4 tl[2][8], th[2][8];
#pragma unroll
        for (int e = 0; e < 8; e++) { tl[0][e] = ltr(pe[e]); th[0][e] = ltr(pe[e] + 512); }
        SCHED();
#pragma unroll
        for (int k = 0; k < 16; k++) {
#pragma unroll
          for (int e = 0; e < 8; e++) {
            mfma_k(k == 0, acc[e][0], cat8(tl[k & 1][e], th[k & 1][e]), as_bf(of[k][0])); SCHED();
            if (k < 15) tl[(k + 1) & 1][e] = ltr(pe[e] + (k + 1) * 4096);
            SCHED();
            mfma_k(k == 0, acc[e][1], cat8(tl[k & 1][e], th[k & 1][e]), as_bf(of[k][1])); SCHED();
            if (k < 15) th[(k + 1) & 1][e] = ltr(pe[e] + (k + 1) * 4096 + 512);
            SCHED();
          }
          if (k + OD < 16) { of[k + OD][0] = bload1(ro, vo, (k + OD) * 1024); of[k + OD][1] = bload1(ro, vo, 16384 + (k + OD) * 1024); }
          if (k == 8) sy_put<Y_OK2>(a1, 1);
          if (k == 14) { FENCE(); sy_put<Y_ISS>(a1, 3); __builtin_amdgcn_s_setprio(3); }
        }
        MFMA_DONE();
        SCHED();
        ST(4);
#pragma unroll
        for (int j = 0; j < 2; j++)
#pragma unroll
          for (int i = 0; i < 4; i++) {
            const u32x4 o = {pkv(lo2(acc[2 * i][j])), pkv(hi2(acc[2 * i][j])), pkv(lo2(acc[2 * i + 1][j])), pkv(hi2(acc[2 * i + 1][j]))};
            bstore1(o, rb, lane * 16, ((8 * so + 2 * w + j) * 16 + 4 * dq + i) * 1024);
          }
        ST(6);
        WAITV(0);
        if (last_of(L0 + SY + Y_N3, 4, lane) && lane < 4) gadd(ws + WS_C4 + ((2 * so + (lane >> 1)) * 8 + 2 * dq + (lane & 1)) * 128);
        ST(7);
      } else {
        const rsrc_t rw2 = mk_rsrc(w2, F * S * 2);
        unsigned pv[6];
#pragma unroll
        for (int n = 0; n < 6; n++) pv[n] = __builtin_amdgcn_raw_buffer_load_b32(rw2, ((44 * w + 2 * (10 + gi - 22 + 2 * n) + hh) * 8 + r8) * 4096 + sq * 1024 + pc * 128, 0, 0);
        asm volatile("" ::"v"(pv[0]), "v"(pv[1]), "v"(pv[2]), "v"(pv[3]), "v"(pv[4]), "v"(pv[5]));
        sy_put<Y_ISS>(a1, 3);
        __builtin_amdgcn_s_sleep(64);
      }
      DRAIN();
    }
    asm volatile("" ::"v"(pfc));
    gwait<1>(ws + WS_C3 + ((st * 8 + ct) * 4 + w) * 128, lane == 0, ~0ull, tag + 6);
    sy_put<Y_OK3>(a1, 1);
    if (w == 1) { gwait<4>(ws + WS_C4 + (st * 8 + ct) * 128, lane == 0, ~0ull, tag + 1); sy_put<Y_OK4>(a4, 1); }
    if (w == 2 && (gi == 22 || gi == 23)) { sy_wait<Y_HA>(a4, 1); sy_wait<Y_OK3>(a4, 1); if (lane == 0) gadd(ws + WS_C5); }
    if (w == 2 && blockIdx.x == 0) {
      gwait<4>(ws + (lane < 32 ? WS_C3 + lane * 128 : WS_C5), lane <= 32, ~0ull, tag + (lane < 32 ? 6 : 16));
      if (lane == 0) __hip_atomic_store((GAS u64*)(ws + WS_EPOCH), tag >> 16, __ATOMIC_RELAXED, __HIP_MEMORY_SCOPE_AGENT);
    }
    BAR();
  }
  STFLUSH();
}

extern "C" void kernel_launch(void* const* d_in, const int* in_sizes, int n_in, void* d_out, int out_size, void* d_ws, size_t ws_size, hipStream_t stream) {
  (void)hipFuncSetAttribute((const void*)fused, hipFuncAttributeMaxDynamicSharedMemorySize, LDS_BYTES);
  fused<<<256, 512, LDS_BYTES, stream>>>((const u16*)d_in[0], (const u16*)d_in[1], (const u16*)d_in[2], (const u16*)d_in[4], (const u16*)d_in[5],
                                         (const u16*)d_in[6], (const u16*)d_in[7], (char*)d_ws, (u16*)d_out);
}
```

```cpp
#define PAUSE
#define NO_PFH
#include <hip/hip_runtime.h>
#include <stdint.h>

#define ISSK 12
#define CODE_LEN 33792

typedef unsigned short u16;
typedef unsigned long long u64;
typedef short s16x4 __attribute__((ext_vector_type(4)));
typedef __bf16 bf16x2 __attribute__((ext_vector_type(2)));
typedef __bf16 bf16x4 __attribute__((ext_vector_type(4)));
typedef __bf16 bf16x8 __attribute__((ext_vector_type(8)));
typedef float f32x2 __attribute__((ext_vector_type(2)));
typedef float f32x4 __attribute__((ext_vector_type(4)));
typedef unsigned u32x2 __attribute__((ext_vector_type(2)));
typedef unsigned u32x4 __attribute__((ext_vector_type(4)));
#define LAS __attribute__((address_space(3)))
#define GAS __attribute__((address_space(1)))
#define DI __device__ __forceinline__

constexpr int S = 2048, D = 512, F = 1376;
constexpr float EPS = 1e-5f;
constexpr unsigned HT_BYTES = 32u * 64u * 1024u;
constexpr unsigned G_BYTES = 32u * 43u * 1024u;
constexpr unsigned O_BYTES = 128u * 16u * 1024u;
constexpr size_t WS_EPOCH = 0;
constexpr size_t WS_C1 = 4096;
constexpr size_t WS_C2 = WS_C1 + 128 * 128;
constexpr size_t WS_C3 = WS_C2 + 256 * 128;
constexpr size_t WS_C4 = WS_C3 + 1024 * 128;
constexpr size_t WS_C5 = WS_C4 + 256 * 128;
constexpr size_t WS_STAT = WS_C5 + 1024;
constexpr size_t WS_HT = WS_STAT + 16384;
constexpr size_t WS_G = WS_HT + HT_BYTES;
constexpr size_t WS_O = WS_G + G_BYTES;
constexpr size_t WS_BASE = WS_O + O_BYTES;
constexpr int SC1 = 16;
constexpr unsigned OOB = 0x80000000u;

typedef __amdgpu_buffer_rsrc_t rsrc_t;
DI rsrc_t mk_rsrc(const void* p, unsigned bytes) { return __builtin_amdgcn_make_buffer_rsrc((void*)p, 0, bytes, 0x00020000); }
DI u32x4 mk_srd(const void* p, unsigned bytes) {
  const uint64_t a = (uint64_t)p;
  u32x4 r = {(unsigned)a, (unsigned)(a >> 32) & 0xffffu, bytes, 0x00020000u};
  return r;
}
DI void glds16(u32x4 srd, unsigned voff, unsigned soff, unsigned lds) {
  asm volatile("s_mov_b32 m0, %3\n\ts_nop 0\n\tbuffer_load_dwordx4 %0, %1, %2 offen lds" ::"v"(voff), "s"(srd), "s"(soff), "s"(lds) : "memory");
}
DI void glds16x3(u32x4 srd, unsigned voff, unsigned soff, unsigned lds) {
  asm volatile("s_mov_b32 m0, %3\n\ts_nop 0\n\tbuffer_load_dwordx4 %0, %1, %2 offen lds\n\tbuffer_load_dwordx4 %0, %1, %2 offen offset:1024 lds\n\t"
               "buffer_load_dwordx4 %0, %1, %2 offen offset:2048 lds" ::"v"(voff), "s"(srd), "s"(soff), "s"(lds) : "memory");
}
#define WAITV(n) asm volatile("s_waitcnt vmcnt(" #n ")" ::: "memory")
#define FENCE() asm volatile("" ::: "memory")
#define SCHED() __builtin_amdgcn_sched_barrier(0)
#define DRAIN() __builtin_amdgcn_s_waitcnt(0x0F70)
#define RBAR() do { FENCE(); __builtin_amdgcn_s_barrier(); FENCE(); } while (0)
#define BAR() do { asm volatile("s_waitcnt lgkmcnt(0)" ::: "memory"); __builtin_amdgcn_s_barrier(); FENCE(); } while (0)

typedef LAS char* lp;
DI lp LP(unsigned a) { return (lp)(uintptr_t)a; }
DI bf16x8 cat8(bf16x4 a, bf16x4 b) { return __builtin_shufflevector(a, b, 0, 1, 2, 3, 4, 5, 6, 7); }
DI bf16x8 l128(lp p) { return *(const LAS bf16x8*)p; }
DI f32x4 lf4(lp p) { return *(const LAS f32x4*)p; }
DI bf16x4 ltr(lp p) { return __builtin_amdgcn_ds_read_tr16_b64_v4bf16((LAS bf16x4*)p); }
DI bf16x8 ltr2(lp p, int d) { return cat8(ltr(p), ltr(p + d)); }
DI f32x4 mfma32(bf16x8 a, bf16x8 b, f32x4 c) { return __builtin_amdgcn_mfma_f32_16x16x32_bf16(a, b, c, 0, 0, 0); }
DI void mfma_ip(f32x4& c, bf16x8 a, bf16x8 b) { asm volatile("v_mfma_f32_16x16x32_bf16 %0, %1, %2, %0" : "+v"(c) : "v"(a), "v"(b)); }
DI void mfma_k(bool first, f32x4& c, bf16x8 a, bf16x8 b) { if (first) c = mfma32(a, b, c); else mfma_ip(c, a, b); }
#define MFMA_DONE() asm volatile("s_nop 7\n\ts_nop 3" ::: "memory")
DI f32x4 mfma16(u32x2 a, u32x2 b, f32x4 c) { return __builtin_amdgcn_mfma_f32_16x16x16bf16_1k(__builtin_bit_cast(s16x4, a), __builtin_bit_cast(s16x4, b), c, 0, 0, 0); }
DI unsigned pkv(f32x2 f) { return __builtin_bit_cast(unsigned, __builtin_convertvector(f, bf16x2)); }
DI unsigned pk2(float a, float b) { f32x2 f = {a, b}; return pkv(f); }
DI f32x2 lo2(f32x4 v) { return __builtin_shufflevector(v, v, 0, 1); }
DI f32x2 hi2(f32x4 v) { return __builtin_shufflevector(v, v, 2, 3); }
DI f32x2 fma2(f32x2 a, f32x2 b, f32x2 c) { return __builtin_elementwise_fma(a, b, c); }
DI f32x2 bc2(float a) { f32x2 r = {a, a}; return r; }
DI float bflo(unsigned u) { return __uint_as_float(u << 16); }
DI float bfhi(unsigned u) { return __uint_as_float(u & 0xffff0000u); }
template <int CTRL> DI float dpp(float v) { return __uint_as_float(__builtin_amdgcn_update_dpp(0u, __float_as_uint(v), CTRL, 0xf, 0xf, true)); }
DI float sum16(float v) { v += dpp<0xB1>(v); v += dpp<0x4E>(v); v += dpp<0x141>(v); v += dpp<0x140>(v); return v; }
DI f32x2 swap16(float a, float b) { auto r = __builtin_amdgcn_permlane16_swap(__float_as_uint(a), __float_as_uint(b), false, false); f32x2 o = {__uint_as_float(r[0]), __uint_as_float(r[1])}; return o; }
DI f32x2 swap32(float a, float b) { auto r = __builtin_amdgcn_permlane32_swap(__float_as_uint(a), __float_as_uint(b), false, false); f32x2 o = {__uint_as_float(r[0]), __uint_as_float(r[1])}; return o; }
DI float sel(unsigned m, float a, float b) { return __uint_as_float((__float_as_uint(a) & m) | (__float_as_uint(b) & ~m)); }
DI u32x4 bload(rsrc_t r, unsigned v, unsigned s) { return __builtin_amdgcn_raw_buffer_load_b128(r, v, s, 0); }
DI u32x4 bload1(rsrc_t r, unsigned v, unsigned s) { return __builtin_amdgcn_raw_buffer_load_b128(r, v, s, SC1); }
DI void bstore1(u32x4 d, rsrc_t r, unsigned v, unsigned s) { __builtin_amdgcn_raw_buffer_store_b128(d, r, v + s, 0, SC1); }
DI bf16x8 as_bf(u32x4 v) { return __builtin_bit_cast(bf16x8, v); }
DI u64 uni64(u64 v) { return ((u64)(unsigned)__builtin_amdgcn_readfirstlane((unsigned)(v >> 32)) << 32) | (unsigned)__builtin_amdgcn_readfirstlane((unsigned)v); }

constexpr unsigned LDS_BYTES = 163840;
constexpr unsigned QXB = 14336;
constexpr unsigned QST = QXB + 24576;
constexpr unsigned QSTAT = 4 * QST;
constexpr unsigned LNROW = 131072;
constexpr unsigned G1STG = 0;
constexpr unsigned P2STG = 49152;
constexpr unsigned SY = LDS_BYTES - 512;
enum { Y_FULL = 0, Y_FREE = 16, Y_HA = 32, Y_HB = 48, Y_ST = 64, Y_QS = 80, Y_ISS = 96, Y_E2 = 112, Y_RA = 128, Y_OK2 = 144, Y_OK3 = 160, Y_OK4 = 176, Y_WO = 192,
       Y_N1 = 208, Y_N2 = 212, Y_N3 = 216, Y_RB = 224, Y_SIG = 240 };
static_assert(QSTAT + 896 <= SY && P2STG + 98304 <= SY, "lds");
constexpr unsigned SPIN_GLB = 1u << 17;

template <int OFF> DI void sy_put(unsigned a, unsigned v) { asm volatile("ds_write_b32 %0, %1 offset:%2" ::"v"(a), "v"(v), "i"(OFF) : "memory"); }
template <int OFF> DI void sy_wait(unsigned a4, unsigned target) {
  unsigned t, n;
  asm volatile("s_mov_b32 %1, 0x200000\n"
               "1:\n\tds_read_b32 %0, %2 offset:%4\n\ts_waitcnt lgkmcnt(0)\n\tv_cmp_gt_u32 vcc, %3, %0\n\ts_cbranch_vccz 2f\n\t"
               "s_sleep 1\n\ts_sub_u32 %1, %1, 1\n\ts_cmp_lg_u32 %1, 0\n\ts_cbranch_scc1 1b\n"
               "2:" : "=&v"(t), "=&s"(n) : "v"(a4), "s"(target), "i"(OFF) : "vcc", "scc", "memory");
}
DI bool last_of(unsigned cnt, unsigned n, int lane) {
  unsigned old = 0;
  if (lane == 0) asm volatile("ds_add_rtn_u32 %0, %1, %2\n\ts_waitcnt lgkmcnt(0)" : "=&v"(old) : "v"(cnt), "v"(1u) : "memory");
  return (unsigned)__builtin_amdgcn_readfirstlane(old) == n - 1;
}
DI void gadd(char* p) { (void)__hip_atomic_fetch_add((GAS u64*)p, 1ull, __ATOMIC_RELAXED, __HIP_MEMORY_SCOPE_AGENT); }
DI u64 gpeek(const char* p) {
  u64 z = 0;
  asm volatile("" : "+v"(z));
  return __hip_atomic_fetch_add((GAS u64*)p, z, __ATOMIC_RELAXED, __HIP_MEMORY_SCOPE_AGENT);
}
template <int SL> DI void gwait(const char* p, bool on, u64 mask, u64 want) {
  FENCE();
  bool ok = !on;
#pragma unroll 1
  for (unsigned n = 0; n < SPIN_GLB; n++) {
    if (!ok) ok = (gpeek(p) & mask) == want;
    if (__all(ok)) break;
    __builtin_amdgcn_s_sleep(SL);
  }
  FENCE();
}

#define ST(i)
#define STDECL
#define STFLUSH()

__global__ __launch_bounds__(512, 2) void fused(const u16* __restrict__ x, const u16* __restrict__ wqkv, const u16* __restrict__ wo, const u16* __restrict__ w1,
                                                const u16* __restrict__ b1, const u16* __restrict__ w2, const u16* __restrict__ b2, char* __restrict__ ws,
                                                u16* __restrict__ out) {
  extern __shared__ __attribute__((aligned(16))) char smem[];
  uint64_t pc0; asm volatile("s_getpc_b64 %0" : "=s"(pc0));
  const unsigned L0 = (unsigned)(uintptr_t)(LAS char*)smem;
  const int tid = threadIdx.x, lane = tid & 63;
  const int W = __builtin_amdgcn_readfirstlane(tid >> 6), w = W & 3;
  const int xcd = blockIdx.x & 7, idx = blockIdx.x >> 3;
  const int sq = xcd >> 1, hh = xcd & 1;
  const int st = sq * 8 + (idx >> 2), ct = hh * 4 + (idx & 3);
  const int gi = idx;
  const int rt = (22 * sq + gi) >> 2, hg = hh * 4 + ((22 * sq + gi) & 3);
  const int c = lane & 15, g = lane >> 4, r8 = lane >> 3, pc = lane & 7;
  const f32x4 z4 = {0.f, 0.f, 0.f, 0.f};
  STDECL ST(0);
  const unsigned pfc = __builtin_amdgcn_raw_buffer_load_b32(mk_rsrc((const void*)(pc0 & ~255ull), CODE_LEN), ((idx & 7) * 8 + W) * 1024 + (lane & 7) * 128, 0, 0);
  {
    const unsigned on = (W >> 1) & 1;
    *(LAS unsigned*)LP(L0 + (on ? 0 : SY) + ((tid >> 5) & 3) * (on ? QST : 128) + (tid & 31) * 4) = on ? 0x3f803f80u : 0u;
  }
  const unsigned a4 = L0 + SY + (lane & 3) * 4, a1 = L0 + SY + w * 4;
  asm volatile("" ::"s"(x), "s"(wqkv), "s"(wo), "s"(w1), "s"(b1), "s"(w2), "s"(b2), "s"(ws), "s"(out));
  const u32x4 sx = mk_srd(x, S * D * 2), sw = mk_srd(wqkv, D * 3 * D * 2);
  const unsigned vx0 = (unsigned)((96 * rt - 16 + 8 * w + r8) * 1024 + ((pc ^ (((w & 1) << 2) | (r8 >> 1))) << 4));
  const unsigned vsw = (((pc >> 1) ^ (((r8 >> 1) & 1) | ((w & 1) << 1))) << 5) | ((pc & 1) << 4);
  const unsigned vwq = (8 * w + r8) * 3072 + hg * 128 + vsw;
  auto head = [&](int s) { glds16x3(sw, vwq, s * 64 * 3072, L0 + (s & 3) * QST + QXB + w * 3072); };
  auto rest = [&](int s) {
    const unsigned b = L0 + (s & 3) * QST + w * 1024;
    if (lane >= 8 || w != 0) glds16(sx, vx0, s * 128, b);
    glds16(sx, vx0 + 32768u, s * 128, b + 4096);
    glds16(sx, vx0 + 65536u, s * 128, b + 8192);
    if (w < 2) glds16(sx, vx0 + 98304u, s * 128, b + 12288);
    glds16x3(sw, vwq, s * 64 * 3072 + 32 * 3072, b + QXB + 12288 + w * 2048);
  };
  const int un = sq * 16 + hh * 8 + gi - 24;
  u32x4 xr[4];
  ST(11);
  if (gi >= 24) {
    asm volatile("buffer_load_dwordx4 %0, %4, %5, %6 offen\n\tbuffer_load_dwordx4 %1, %4, %5, %6 offen offset:1024\n\t"
                 "buffer_load_dwordx4 %2, %4, %5, %6 offen offset:2048\n\tbuffer_load_dwordx4 %3, %4, %5, %6 offen offset:3072"
                 : "=&v"(xr[0]), "=&v"(xr[1]), "=&v"(xr[2]), "=&v"(xr[3]) : "v"(lane * 16), "s"(sx), "s"((un * 32 + 4 * W) * 1024) : "memory");
  } else {
    asm volatile("" : "=v"(xr[0]), "=v"(xr[1]), "=v"(xr[2]), "=v"(xr[3]));
    if (gi < 22 && W >= 4) { head(0); rest(0); }
  }
  BAR();
  u64 tag = 0;

  if (gi >= 24) {
    const u64 ep = __hip_atomic_load((const GAS u64*)(ws + WS_EPOCH), __ATOMIC_RELAXED, __HIP_MEMORY_SCOPE_AGENT);
    const unsigned pfw = *(const GAS unsigned*)((lane & 31) < 24 ? (const char*)wqkv + (un * 8 + W) * 3072 + (lane & 31) * 128 : (const char*)wo + (un * 8 + W) * 1024 + (lane & 7) * 128);
    WAITV(2);
    asm volatile("" : "+v"(xr[0]), "+v"(xr[1]), "+v"(xr[2]), "+v"(xr[3]));
    f32x2 v[4][4];
    float p[4], s1[4], s2[4];
    auto red4 = [&](const float (&a)[4]) {
      f32x2 t = swap16(a[0], a[1]), u = swap16(a[2], a[3]);
      t = swap32(t[0] + t[1], u[0] + u[1]);
      return sum16(t[0] + t[1]);
    };
#pragma unroll
    for (int rr = 0; rr < 4; rr++) {
      p[rr] = bflo(__builtin_amdgcn_readfirstlane(xr[rr][0]));
      f32x2 a = {0.f, 0.f}, b = {0.f, 0.f};
#pragma unroll
      for (int j = 0; j < 4; j++) { const f32x2 t = {bflo(xr[rr][j]), bfhi(xr[rr][j])}; v[rr][j] = t - bc2(p[rr]); a += v[rr][j]; b = fma2(v[rr][j], v[rr][j], b); }
      s1[rr] = a[0] + a[1]; s2[rr] = b[0] + b[1];
    }
    if (W == 0) {
      const size_t o = lane < 2 ? WS_C1 + (un * 2 + lane) * 128 : WS_C2 + (un * 24 + lane - 2) * 128;
      u64 e = ep;
      asm volatile("" : "+v"(e));
      if (lane < (un == 63 ? 27 : 26)) __hip_atomic_store((GAS u64*)(ws + o), (e + 1) << 16, __ATOMIC_RELAXED, __HIP_MEMORY_SCOPE_AGENT);
    }
    {
      const float m = red4(s1) * (1.f / D), var = fmaxf(red4(s2) * (1.f / D) - m * m, 0.f);
      const float r = __builtin_amdgcn_rsqf(var + EPS);
      const float a = r * __builtin_amdgcn_rsqf(var * r * r + EPS), ma = -m * a;
#pragma unroll
      for (int rr = 0; rr < 4; rr++) {
        const f32x2 av = bc2(__uint_as_float(__builtin_amdgcn_readlane(__float_as_uint(a), 16 * rr))), cv = bc2(__uint_as_float(__builtin_amdgcn_readlane(__float_as_uint(ma), 16 * rr)));
        u32x4 h;
#pragma unroll
        for (int j = 0; j < 4; j++) h[j] = pkv(fma2(v[rr][j], av, cv));
        *(LAS u32x4*)LP(L0 + (4 * W + rr) * 1088 + ((W >> 1) & 1) * 32 + lane * 16) = h;
      }
      const float pg = g == 0 ? p[0] : g == 1 ? p[1] : g == 2 ? p[2] : p[3];
      *(LAS float*)LP(L0 + QSTAT + (W >> 1) * 64 + (W & 1) * 16 + g * 4 + (lane & 1) * 32) = (lane & 1) ? (m + pg) * r : r;
    }
    ST(12);
    char* const c1p = ws + WS_C1 + (sq * 32 + (lane & 31)) * 128;
    u64 chk = 0;
    if (W == 1 && lane < 32) asm volatile("global_atomic_add_x2 %0, %1, %0, off sc0" : "+v"(chk) : "v"(c1p) : "memory");
    BAR();
    {
      bf16x8 tv[4];
#pragma unroll
      for (int n = 0; n < 4; n++) tv[n] = ltr2(LP(L0 + (8 * g + (c >> 2)) * 1088 + (g & 1) * 32 + W * 128 + (c & 3) * 8) + n * 32, 4 * 1088);
      const u32x4 sv = *(const LAS u32x4*)LP(L0 + QSTAT + c * 16);
      const rsrc_t rht = mk_rsrc(ws + WS_HT, HT_BYTES);
#pragma unroll
      for (int n = 0; n < 4; n++) bstore1(__builtin_bit_cast(u32x4, tv[n]), rht, lane * 16, ((4 * W + n) * 64 + un) * 1024);
      if (W == 2) bstore1(sv, mk_rsrc(ws + WS_STAT, S * 8), lane < 16 ? un * 256 + lane * 16 : OOB, 0);
    }
    ST(1);
    WAITV(0);
    DRAIN();
    u64 e = ep;
    asm volatile("" : "+v"(e) : "v"(pfw));
    tag = (uni64(e) + 1) << 16;
    if (W == 1) { asm volatile("" : "+v"(chk)); if (__any(lane < 32 && (chk >> 16) != (tag >> 16))) gwait<2>(c1p, lane < 32, ~0xffffull, tag); }
    if (last_of(L0 + SY + Y_N1, 8, lane)) { if (lane < 32) gadd(c1p); sy_put<Y_SIG>(a4, 1); }
    ST(2);
  } else if (gi >= 22) {
    unsigned p3[3] = {0, 0, 0};
    if (W >= 4) {
#pragma unroll
      for (int n = 0; n < 3; n++) p3[n] = __builtin_amdgcn_raw_buffer_load_b32(mk_rsrc(wqkv, D * 3 * D * 2), hh ? OOB : ((n * 8 + (gi - 22) * 4 + w) * 64 + lane) * 1024 + 384, 0, 0);
    } else if (W > 0) {
#pragma unroll
      for (int n = 0; n < 2; n++) p3[n] = __builtin_amdgcn_raw_buffer_load_b32(mk_rsrc(x, S * D * 2), (unsigned)((((22 * sq) >> 2) * 96 - 16 + (n * 6 + (gi - 22) * 3 + W - 1) * 52 + min(lane, 51)) * 1024 + 384), 0, 0);
    }
    tag = (uni64(__hip_atomic_load((const GAS u64*)(ws + WS_EPOCH), __ATOMIC_RELAXED, __HIP_MEMORY_SCOPE_AGENT)) + 1) << 16;
    if (W < 4) sy_put<Y_HA>(a1, 1);
    asm volatile("" ::"v"(p3[0]), "v"(p3[1]), "v"(p3[2]));
  }

  if (W < 4) {
    if (gi < 22) {
      f32x4 aq[7], ak[7], av[7];
#pragma unroll
      for (int b = 0; b < 7; b++) { ak[b] = z4; av[b] = z4; aq[b] = z4; }
      const unsigned ax = L0 + c * 128 + ((g ^ (c >> 1)) << 4);
      const unsigned aw = L0 + QXB + g * 3072 + (c >> 2) * 128 + ((w ^ ((c >> 3) | ((g & 1) << 1))) << 5) + (c & 3) * 8;
      bf16x8 xf[2][7];
      bf16x4 wl[2][3], wh[2][3];
      const lp pxa[2][2] = {{LP(ax), LP(ax ^ 64)}, {LP(ax + 2 * QST), LP((ax ^ 64) + 2 * QST)}}, pwa[2] = {LP(aw), LP(aw + 2 * QST)};
      auto RD = [&](int n, int i, int slot, int kk) {
        const lp px = pxa[slot >> 1][kk] + (slot & 1) * QST, pw = pwa[slot >> 1] + (slot & 1) * QST + kk * 12288;
        if (i == 0) wl[n][1] = ltr(pw + 1024);
        if (i == 1) wh[n][1] = ltr(pw + 1024 + 512);
        if (i == 2) xf[n][6] = l128(px + 12288);
        if (i == 3) wl[n][2] = ltr(pw + 2048);
        if (i == 4) wh[n][2] = ltr(pw + 2048 + 512);
        if (i == 5) wl[n][0] = ltr(pw);
        if (i == 6) wh[n][0] = ltr(pw + 512);
        if (i >= 7) xf[n][12 - i] = l128(px + (12 - i) * 2048);
      };
      auto MM = [&](int cur, int m) {
        const int t = 6 - m / 3;
        if (m % 3 == 0) mfma_ip(ak[t], cat8(wl[cur][1], wh[cur][1]), xf[cur][t]);
        if (m % 3 == 1) mfma_ip(av[t], xf[cur][t], cat8(wl[cur][2], wh[cur][2]));
        if (m % 3 == 2) mfma_ip(aq[t], cat8(wl[cur][0], wh[cur][0]), xf[cur][t]);
      };
      RBAR();
      ST(2);
#pragma unroll
      for (int i = 0; i < 13; i++) RD(0, i, 0, 0);
      SCHED();
#pragma unroll 1
      for (int s4 = 0; s4 < 8; s4 += 4) {
#pragma unroll
        for (int sl = 0; sl < 4; sl++) {
#pragma unroll
          for (int m = 0; m < 21; m++) {
            MM(0, m); SCHED();
            if (m < 13) RD(1, m, sl, 1);
            if (m == 19 && (sl < 3 || s4 == 0)) BAR();
            SCHED();
          }
#pragma unroll
          for (int m = 0; m < 21; m++) {
            MM(1, m); SCHED();
            if (m < 13) RD(0, m, (sl + 1) & 3, 0);
            SCHED();
          }
        }
      }
      MFMA_DONE();
      SCHED();
      ST(3);
      f32x2 cq[2], ck[2];
#pragma unroll
      for (int j = 0; j < 4; j++) { cq[j >> 1][j & 1] = -dpp<0x150>(aq[0][j]); ck[j >> 1][j & 1] = -dpp<0x150>(ak[0][j]); }
      f32x2 cvt = swap16(av[0][0], av[0][0]);
      cvt = swap32(cvt[0], cvt[0]);
      const f32x2 cv = bc2(-cvt[0]);
      sy_wait<Y_ST>(a4, 1);
      u32x2 qf[6], kf[7], vf[7];
      {
        float sr[7], sm[7]; f32x4 vr[7], vm[7];
        const lp pcs = LP(L0 + QSTAT + (c >> 3) * 64 + (c & 7) * 4), prs = LP(L0 + QSTAT + (g >> 1) * 64 + (g & 1) * 16);
#pragma unroll
        for (int t = 0; t < 7; t++) {
          sr[t] = *(const LAS float*)(pcs + t * 128); sm[t] = *(const LAS float*)(pcs + t * 128 + 32);
          vr[t] = lf4(prs + t * 128); vm[t] = lf4(prs + t * 128 + 32);
        }
        SCHED();
#pragma unroll
        for (int t = 0; t < 7; t++) {
          const f32x2 r = bc2(sr[t]), m = bc2(sm[t]);
          f32x2 y[6] = {m * ck[0], m * ck[1], m * cq[0], m * cq[1], lo2(vm[t]) * cv, hi2(vm[t]) * cv};
          SCHED();
          y[0] = fma2(r, lo2(ak[t]), y[0]); y[1] = fma2(r, hi2(ak[t]), y[1]); y[2] = fma2(r, lo2(aq[t]), y[2]); y[3] = fma2(r, hi2(aq[t]), y[3]);
          y[4] = fma2(lo2(vr[t]), lo2(av[t]), y[4]); y[5] = fma2(hi2(vr[t]), hi2(av[t]), y[5]);
          SCHED();
          kf[t][0] = pkv(y[0]); kf[t][1] = pkv(y[1]);
          if (t > 0) { qf[t - 1][0] = pkv(y[2]); qf[t - 1][1] = pkv(y[3]); }
          vf[t][0] = pkv(y[4]); vf[t][1] = pkv(y[5]);
        }
        SCHED();
      }
      u32x2 op[6];
      {
        unsigned om = 0u - (unsigned)(c & 1);
        asm("" : "+v"(om));
        const bool cur0 = 4 * g + (c & 1) <= c, cur1 = 4 * g + (c & 1) + 2 <= c;
        constexpr float SC = 0.25f * 1.4426950408889634f;
        f32x4 sp[6], su[6];
#pragma unroll
        for (int t = 0; t < 6; t++) { sp[t] = mfma16(kf[t], qf[t], z4); su[t] = mfma16(kf[t + 1], qf[t], z4); }
        float l0[6], l1[6], mx[8];
#pragma unroll
        for (int t = 0; t < 6; t++) {
          const float p0 = sel(om, sp[t][1], sp[t][0]), p1 = sel(om, sp[t][3], sp[t][2]);
          const float u0 = sel(om, su[t][1], su[t][0]), u1 = sel(om, su[t][3], su[t][2]);
          l0[t] = cur0 ? u0 : p0; l1[t] = cur1 ? u1 : p1;
          if (t == 0 && rt == 0) { l0[t] = cur0 ? l0[t] : -4e9f; l1[t] = cur1 ? l1[t] : -4e9f; }
          mx[t] = fmaxf(l0[t], l1[t]);
        }
        {
          const f32x2 a = swap16(mx[0], mx[1]), b = swap16(mx[2], mx[3]), a2 = swap16(mx[4], mx[5]);
          const float n2 = fmaxf(a2[0], a2[1]);
          const f32x2 d = swap32(fmaxf(a[0], a[1]), fmaxf(b[0], b[1])), d2 = swap32(n2, n2);
          const float m = fmaxf(d[0], d[1]) * -SC, m2 = fmaxf(d2[0], d2[1]) * -SC;
          const f32x2 e = swap32(m, m), f0 = swap16(e[0], e[0]), f1 = swap16(e[1], e[1]), f2 = swap16(m2, m2);
          mx[0] = f0[0]; mx[1] = f0[1]; mx[2] = f1[0]; mx[3] = f1[1]; mx[4] = f2[0]; mx[5] = f2[1];
        }
        const u32x4 ones = {0x3f803f80u, 0x3f803f80u, 0x3f803f80u, 0x3f803f80u};
        const unsigned keep = om ^ 0x0000ffffu;
        const unsigned k00 = cur0 ? 0u : keep, k01 = cur1 ? 0u : keep, k10 = cur0 ? keep : 0u, k11 = cur1 ? keep : 0u;
        f32x4 o[6], den[6];
#pragma unroll
        for (int t = 0; t < 6; t++) {
          const float e0 = __builtin_amdgcn_exp2f(__builtin_fmaf(l0[t], SC, mx[t])), e1 = __builtin_amdgcn_exp2f(__builtin_fmaf(l1[t], SC, mx[t]));
          const unsigned b0 = pk2(e0, e0), b1v = pk2(e1, e1);
          const u32x4 pu = {b0 & k00, b1v & k01, b0 & k10, b1v & k11}, vv = {vf[t][0], vf[t][1], vf[t + 1][0], vf[t + 1][1]};
          o[t] = mfma32(as_bf(vv), as_bf(pu), z4);
          den[t] = mfma32(as_bf(ones), as_bf(pu), z4);
        }
#pragma unroll
        for (int t = 0; t < 6; t++) {
          const f32x2 inv = bc2(__builtin_amdgcn_rcpf(den[t][0]));
          op[t][0] = pkv(lo2(o[t]) * inv); op[t][1] = pkv(hi2(o[t]) * inv);
        }
      }
      {
        const rsrc_t ro = mk_rsrc(ws + WS_O, O_BYTES);
#pragma unroll
        for (int t = 0; t < 6; t += 2) {
          const auto r0 = __builtin_amdgcn_permlane16_swap(op[t][0], op[t + 1][0], false, false);
          const auto r1 = __builtin_amdgcn_permlane16_swap(op[t][1], op[t + 1][1], false, false);
          const u32x4 v = {r0[0], r1[0], r0[1], r1[1]};
          if (t == 0 || rt < 21) bstore1(v, ro, (g & 1) * 16384 + ((g >> 1) * 16 + c) * 16, ((6 * rt + t) * 16 + 2 * hg + (w >> 1)) * 1024 + (w & 1) * 512);
        }
      }
      ST(13);
      sy_put<Y_QS>(a1, 1);
      WAITV(0);
      if (last_of(L0 + SY + Y_N2, 4, lane) && lane < (rt < 21 ? 12 : 4)) gadd(ws + WS_C2 + (12 * rt + lane) * 128);
      ST(4);
    } else {
      const rsrc_t rw2 = mk_rsrc(w2, F * S * 2);
      if (gi >= 24) {
        sy_wait<Y_OK2>(a4, 1);
        const int m = gi - 24 + 8 * r8;
        const unsigned t = __builtin_amdgcn_raw_buffer_load_b32(rw2, m < 22 ? ((44 * w + 2 * m + (hh ^ 1)) * 8 + pc) * 4096 + sq * 1024 + 384 : OOB, 0, 0);
        asm volatile("" ::"v"(t));
      } else {
        unsigned pv[5];
#pragma unroll
        for (int n = 0; n < 5; n++) pv[n] = __builtin_amdgcn_raw_buffer_load_b32(rw2, ((44 * w + 2 * (gi - 22 + 2 * n) + hh) * 8 + r8) * 4096 + sq * 1024 + pc * 128, 0, 0);
        asm volatile("" ::"v"(pv[0]), "v"(pv[1]), "v"(pv[2]), "v"(pv[3]), "v"(pv[4]));
      }
      ST(4);
    }
    asm volatile("" ::"v"(pfc));

    const rsrc_t rgg = mk_rsrc(ws + WS_G, G_BYTES);
    const unsigned vg = lane * 16 + (4 * ct * 43 + 11 * w) * 1024;
    const u32x4 sw2 = mk_srd(w2, F * S * 2);
    const unsigned vw2 = (352 * w + r8) * 4096 + st * 128 + ((((pc >> 1) ^ ((r8 >> 1) & 1)) << 5) | ((pc & 1) << 4));
    const unsigned lw = L0 + P2STG + w * 24576;
    const unsigned ar = lw + (8 * g + (c >> 2)) * 128 + ((((c >> 3) | ((g & 1) << 1))) << 5) + (c & 3) * 8;
    u32x4 gf[11][4];
    auto GW = [&](int k, int slot) {
#pragma unroll
      for (int j = 0; j < 4; j++) glds16(sw2, (k == 10 && w == 3) ? OOB : vw2 ^ ((j & 1) << 6), (32 * k + 8 * j) * 4096, lw + slot * 4096 + j * 1024);
    };
    auto LG1 = [&](int k, int j) { gf[k][j] = bload1(rgg, (k == 10 && w == 3) ? OOB : vg, j * 43 * 1024 + k * 1024); };
    lp pr[4];
#pragma unroll
    for (int j = 0; j < 4; j++) pr[j] = LP(ar ^ (((w + j) & 3) << 5));
    bf16x8 tw[2][4];
    bf16x8 tk[4][4];
    auto TR = [&](bf16x8 (&t)[4], int slot) {
#pragma unroll
      for (int j = 0; j < 4; j++) t[j] = ltr2(pr[j] + slot * 4096, 512);
      asm volatile("" ::"v"(t[0]), "v"(t[1]), "v"(t[2]), "v"(t[3]) : "memory");
    };
    if (gi < 22) sy_wait<Y_QS>(a4, 1);
    sy_wait<Y_ISS>(a4, 1);
    ST(5);
    const int srow = 64 * st + 16 * w + c;
    u32x4 xq[2];
    asm volatile("buffer_load_dwordx4 %0, %2, %3, 0 offen\n\tbuffer_load_dwordx4 %1, %2, %3, 0 offen offset:64"
                 : "=&v"(xq[0]), "=&v"(xq[1]) : "v"((srow * D + 64 * ct + 16 * (g & 1) + 8 * (g >> 1)) * 2), "s"(sx) : "memory");
    GW(7, 0); GW(8, 1);
    sy_wait<Y_ISS>(a4, 2);
    GW(9, 2); GW(10, 3); GW(4, 4); GW(5, 5);
    sy_wait<Y_ISS>(a4, 3);
    WAITV(20); TR(tk[0], 0); GW(0, 0);
    WAITV(20); TR(tk[1], 1); GW(1, 1);
    WAITV(20); TR(tk[2], 2); GW(2, 2);
    WAITV(20); TR(tk[3], 3); GW(3, 3);
    WAITV(12); TR(tw[0], 0); GW(6, 0);
    asm volatile("" : "+v"(xq[0]), "+v"(xq[1]));
    u32x2 xv[4];
#pragma unroll
    for (int i = 0; i < 2; i++) {
      const auto r0 = __builtin_amdgcn_permlane16_swap(xq[i][0], xq[i][2], false, false), r1 = __builtin_amdgcn_permlane16_swap(xq[i][1], xq[i][3], false, false);
      xv[2 * i][0] = r0[0]; xv[2 * i][1] = r1[0]; xv[2 * i + 1][0] = r0[1]; xv[2 * i + 1][1] = r1[1];
    }
    f32x4 acc[4][4];
#pragma unroll
    for (int i = 0; i < 4; i++) {
      acc[i][0] = (f32x4){bflo(xv[i][0]), bfhi(xv[i][0]), bflo(xv[i][1]), bfhi(xv[i][1])};
#pragma unroll
      for (int j = 1; j < 4; j++) acc[i][j] = z4;
      asm volatile("" : "+v"(acc[i][0]));
    }
    ST(6);
    sy_wait<Y_OK3>(a1, 1);
    ST(7);
    constexpr int GD = 4;
#pragma unroll
    for (int k = 0; k < GD; k++)
#pragma unroll
      for (int j = 0; j < 4; j++) LG1(k, j);
    WAITV(16);
    SCHED();
    u32x4 bv[2];
#pragma unroll
    for (int k = 0; k < 10; k++) {
      if (k + GD == 11) {
        sy_wait<Y_OK4>(a4, 1);
        const rsrc_t rb = mk_rsrc(ws + WS_BASE, S * D * 2);
#pragma unroll
        for (int i = 0; i < 2; i++) bv[i] = bload1(rb, lane * 16, ((4 * st + w) * 16 + 2 * ct + i) * 1024);
      }
#pragma unroll
      for (int i = 0; i < 4; i++) {
#pragma unroll
        for (int j = 0; j < 4; j++) {
          mfma_k(k == 0, acc[i][j], as_bf(gf[k][i]), k < 7 ? tw[k & 1][j] : tk[k < 7 ? 0 : k - 7][j]);
          if (j == 0 && k + 1 < 7) { SCHED(); tw[(k + 1) & 1][i] = ltr2(pr[i] + ((k + 1) % 6) * 4096, 512); SCHED(); }
        }
        SCHED();
        if (k + GD < 11) LG1(k + GD, i);
        SCHED();
      }
    }
#pragma unroll
    for (int jj = 1; jj <= 4; jj++) {
#pragma unroll
      for (int i = 0; i < 4; i++) {
        acc[i][jj & 3] = mfma32(as_bf(gf[10][i]), tk[3][jj & 3], acc[i][jj & 3]); SCHED();
        if (jj >= 2) *(LAS f32x4*)(LP(lw + lane * 16) + ((jj - 2) * 4 + i) * 1024) = acc[i][jj - 1];
        SCHED();
      }
    }
    ST(8);
    BAR();
    ST(9);
    f32x4 u[4];
#pragma unroll
    for (int i = 0; i < 4; i++) {
      u[i] = acc[i][0] + (f32x4){bflo(bv[i >> 1][2 * (i & 1)]), bfhi(bv[i >> 1][2 * (i & 1)]), bflo(bv[i >> 1][2 * (i & 1) + 1]), bfhi(bv[i >> 1][2 * (i & 1) + 1])};
#pragma unroll
      for (int j = 1; j < 4; j++) u[i] += lf4(LP(L0 + P2STG + ((w - j) & 3) * 24576 + lane * 16) + ((j - 1) * 4 + i) * 1024);
    }
#pragma unroll
    for (int i = 0; i < 4; i += 2) {
      const auto r0 = __builtin_amdgcn_permlane16_swap(pkv(lo2(u[i])), pkv(lo2(u[i + 1])), false, false);
      const auto r1 = __builtin_amdgcn_permlane16_swap(pkv(hi2(u[i])), pkv(hi2(u[i + 1])), false, false);
      const u32x4 o = {r0[0], r1[0], r0[1], r1[1]};
      __builtin_amdgcn_raw_buffer_store_b128(o, mk_rsrc(out, S * D * 2), (srow * D + 64 * ct + 16 * i + 16 * (g & 1) + 8 * (g >> 1)) * 2, 0, 16);
    }
    ST(10);
  } else {
    if (gi < 22) {
      const bool c1on = lane < (w == 3 ? 4 : 1);
      const char* const c1w = ws + WS_C1 + (((w == 3 ? 0 : w) + (lane & 3)) * 32 + (blockIdx.x & 31)) * 128;
      u64 c1v = 0, ep = 0;
      head(1); rest(1);
#pragma unroll 1
      for (int s = 2; s < 8; s++) {
        head(s);
        WAITV(12); RBAR();
        rest(s);
      }
      ST(1);
      u32x4 stv4;
      auto c1_done = [&]() {
        asm volatile("" : "+v"(c1v), "+v"(ep));
        tag = (uni64(ep) + 1) << 16;
        if (__any(c1on && c1v != tag + 16)) gwait<2>(c1w, c1on, ~0ull, tag + 16);
        if (w == 3) stv4 = bload1(mk_rsrc(ws + WS_STAT, S * 8), (unsigned)((12 * rt - 2) * 64 + min(lane, 55) * 16), 0);
      };
      auto put_stats = [&]() { if (w == 3) { *(LAS u32x4*)LP(L0 + QSTAT + min(lane, 55) * 16) = stv4; sy_put<Y_ST>(a4, 1); } };
      const int ft = rt, dt = hg;
      const rsrc_t rht = mk_rsrc(ws + WS_HT, HT_BYTES);
      const u32x4 s1 = mk_srd(w1, S * F * 2);
      u32x4 fb[16][4];
      const unsigned sz = (((pc >> 1) ^ ((r8 >> 1) & 1)) << 5) | ((pc & 1) << 4), vwb = (512 * w + r8) * 2752 + ft * 128;
      const unsigned vw0 = (ft == 21 && sz >= 64) ? OOB : vwb + sz, vw1 = (ft == 21 && sz < 64) ? OOB : vwb + (sz ^ 64);
      const unsigned vb = lane * 16 + (4 * dt * 64 + 16 * w) * 1024;
      const unsigned lb = L0 + G1STG + w * 4096;
      const unsigned ar = lb + (8 * g + (c >> 2)) * 128 + ((((c >> 3) | ((g & 1) << 1))) << 5) + (c & 3) * 8;
      lp pe[4];
#pragma unroll
      for (int e = 0; e < 4; e++) pe[e] = LP(ar ^ (e << 5));
      auto GW = [&](int k, int j) { glds16(s1, (j & 1) ? vw1 : vw0, (32 * k + 8 * j) * 2752, lb + (k % 3) * 16384 + j * 1024); };
      auto LB = [&](int k, int j) { fb[k][j] = bload1(rht, vb, ((w + j) & 3) * 65536 + k * 1024); };
      asm volatile("global_load_dwordx2 %0, %1, off sc1" : "=v"(ep) : "v"(ws + WS_EPOCH) : "memory");
      if (c1on) asm volatile("global_atomic_add_x2 %0, %1, %0, off sc0" : "+v"(c1v) : "v"(c1w) : "memory");
      GW(0, 0); GW(0, 1);
      WAITV(13); RBAR();
      GW(0, 2); GW(0, 3); GW(1, 0); GW(1, 1);
      WAITV(8); RBAR();
      GW(1, 2); GW(1, 3);
      WAITV(8);
      ST(2);
      c1_done();
      ST(3);
      u32x2 bb[4];
      {
        const rsrc_t rb1 = mk_rsrc(b1, F * 2);
#pragma unroll
        for (int i = 0; i < 4; i++) bb[i] = __builtin_amdgcn_raw_buffer_load_b64(rb1, (64 * ft + 4 * g) * 2 + i * 32, 0, 0);
      }
      LB(0, 0); LB(0, 1); LB(0, 2); LB(0, 3);
      GW(2, 0); GW(2, 1); GW(2, 2); GW(2, 3);
      LB(1, 0); LB(1, 1); LB(1, 2); LB(1, 3);
      put_stats();
      LB(2, 0); LB(2, 1); LB(2, 2); LB(2, 3);
      LB(3, 0); LB(3, 1); LB(3, 2); LB(3, 3);
      WAITV(28);
      f32x4 acc[4][4];
#pragma unroll
      for (int i = 0; i < 4; i++) {
        acc[i][0] = (f32x4){bflo(bb[i][0]), bfhi(bb[i][0]), bflo(bb[i][1]), bfhi(bb[i][1])};
#pragma unroll
        for (int j = 1; j < 4; j++) acc[i][j] = z4;
      }
      bf16x4 al[2][4], ah[2][4];
#pragma unroll
      for (int e = 0; e < 4; e++) { al[0][e] = ltr(pe[e]); ah[0][e] = ltr(pe[e] + 512); }
      SCHED();
#pragma unroll
      for (int k = 0; k < 15; k++) {
#pragma unroll
        for (int m = 0; m < 16; m++) {
          const int i = m & 3, j = m >> 2;
          mfma_k(k == 0, acc[i][j], cat8(al[k & 1][i], ah[k & 1][i]), as_bf(fb[k][j]));
          SCHED();
          if (m == 5) {
            if (k == 0) WAITV(26); else if (k == 1) WAITV(22); else if (k < 12) WAITV(11); else if (k == 12) WAITV(10); else if (k == 13) WAITV(5); else WAITV(0);
          }
          if (m >= 6 && m < 14) {
            const int e = (m - 6) >> 1;
            if (m & 1) ah[(k + 1) & 1][e] = ltr(pe[e] + ((k + 1) % 3) * 16384 + 512); else al[(k + 1) & 1][e] = ltr(pe[e] + ((k + 1) % 3) * 16384);
          }
          if ((m == 4 || m == 6 || m == 9 || m == 13) && k + 3 < 16) GW(k + 3, m == 4 ? 0 : m == 6 ? 1 : m == 9 ? 2 : 3);
          if (i == 3 && k + 4 < 16) LB(k + 4, j);
          SCHED();
        }
        if (k == 3) ST(8);
        if (k == 7) ST(9);
        if (k == 11) ST(10);
        if (k == ISSK) sy_put<Y_ISS>(a1, 1);
      }
#pragma unroll
      for (int jj = 1; jj <= 4; jj++) {
#pragma unroll
        for (int i = 0; i < 4; i++) {
          acc[i][jj & 3] = mfma32(cat8(al[1][i], ah[1][i]), as_bf(fb[15][jj & 3]), acc[i][jj & 3]); SCHED();
          if (jj >= 2) *(LAS f32x4*)(LP(lb + lane * 16) + (jj - 2) * 16384 + i * 1024) = acc[i][jj - 1];
          SCHED();
        }
      }
      sy_put<Y_E2>(a1, 1);
      sy_put<Y_ISS>(a1, 2);
      __builtin_amdgcn_s_setprio(3);
      ST(4);
      const lp pin[3] = {LP(L0 + G1STG + ((w - 1) & 3) * 4096 + lane * 16), LP(L0 + G1STG + 16384 + ((w - 2) & 3) * 4096 + lane * 16), LP(L0 + G1STG + 32768 + ((w - 3) & 3) * 4096 + lane * 16)};
      sy_wait<Y_E2>(a4, 1);
      ST(5);
      f32x4 pt[4][3];
#pragma unroll
      for (int i = 0; i < 4; i++)
#pragma unroll
        for (int j = 0; j < 3; j++) pt[i][j] = lf4(pin[j] + i * 1024);
      SCHED();
      u32x2 gp[4];
#pragma unroll
      for (int i = 0; i < 4; i++) {
        const f32x4 u = (acc[i][0] + pt[i][0]) + (pt[i][1] + pt[i][2]);
#pragma unroll
        for (int hf = 0; hf < 2; hf++) {
          constexpr float K1 = -2.f * 0.7978845608028654f * 1.4426950408889634f, K2 = K1 * 0.044715f;
          const f32x2 t = hf ? hi2(u) : lo2(u);
          const f32x2 z = t * fma2(t * t, bc2(K2), bc2(K1));
          f32x2 e = {__builtin_amdgcn_exp2f(z[0]), __builtin_amdgcn_exp2f(z[1])};
          e += bc2(1.f);
          const f32x2 rc = {__builtin_amdgcn_rcpf(e[0]), __builtin_amdgcn_rcpf(e[1])};
          gp[i][hf] = pkv(t * rc);
        }
      }
      const rsrc_t rgg = mk_rsrc(ws + WS_G, G_BYTES);
#pragma unroll
      for (int i = 0; i < 4; i += 2) {
        const auto r0 = __builtin_amdgcn_permlane16_swap(gp[i][0], gp[i + 1][0], false, false);
        const auto r1v = __builtin_amdgcn_permlane16_swap(gp[i][1], gp[i + 1][1], false, false);
        const u32x4 v = {r0[0], r1v[0], r0[1], r1v[1]};
        if (i == 0 || ft < 21) bstore1(v, rgg, (2 * (g & 1) + (g >> 1)) * 256 + c * 16, ((4 * dt + w) * 43 + 2 * ft + (i >> 1)) * 1024);
      }
      ST(6);
      WAITV(0);
      DRAIN();
      {
        const int sl0 = (2 * ft) / 11, sl1 = (2 * ft + 1) / 11;
        if (last_of(L0 + SY + Y_N3, 4, lane) && (lane < 32 || sl1 != sl0)) gadd(ws + WS_C3 + (((lane & 31) * 8 + dt) * 4 + (lane < 32 ? sl0 : sl1)) * 128);
        sy_put<Y_ISS>(a1, 3);
      }
      ST(7);
    } else {
      if (gi >= 24) {
        unsigned pv[8];
        sy_wait<Y_SIG>(a4, 1);
        {
          const rsrc_t r1 = mk_rsrc(w1, S * F * 2);
          const unsigned c0 = ((22 * sq) >> 2) * 128;
#pragma unroll
          for (int n = 0; n < 8; n++) {
            const unsigned row = w * 512 + (2 * (gi - 24) + 16 * (n >> 1) + ((n & 1) ^ hh)) * 8 + r8;
            pv[n] = __builtin_amdgcn_raw_buffer_load_b32(r1, pc < 6 + (r8 & 1) ? ((row * 2752 + c0) & ~127u) + pc * 128 : OOB, 0, 0);
          }
        }
        const int so = sq * 4 + ((gi - 24) >> 1), dq = hh * 2 + (gi & 1);
        const u32x4 swo = mk_srd(wo, D * D * 2);
        const unsigned vwo = (8 * w + r8) * 1024 + dq * 256 + vsw;
#pragma unroll 1
        for (int n = 0; n < 16; n++) {
          glds16(swo, vwo, n * 32768, L0 + (n * 4 + w) * 1024);
          glds16(swo, vwo, n * 32768 + 128, L0 + 65536 + (n * 4 + w) * 1024);
        }
        const rsrc_t ro = mk_rsrc(ws + WS_O, O_BYTES), rb = mk_rsrc(ws + WS_BASE, S * D * 2);
        const int srow = 128 * so + 32 * w + c;
        const float bias0 = bflo(b2[srow]), bias1 = bflo(b2[srow + 16]);
        WAITV(0);
        asm volatile("" ::"v"(pv[0]), "v"(pv[1]), "v"(pv[2]), "v"(pv[3]), "v"(pv[4]), "v"(pv[5]), "v"(pv[6]), "v"(pv[7]));
        sy_put<Y_WO>(a1, 1);
        ST(8);
        gwait<4>(ws + WS_C2 + ((so * 4 + w) * 4 + dq) * 128, lane == 0, ~0ull, tag + 8);
        ST(3);
        u32x4 of[16][2];
        const unsigned vo = lane * 16 + (8 * so + 2 * w) * 16384;
        constexpr int OD = 8;
#pragma unroll
        for (int k = 0; k < OD; k++) { of[k][0] = bload1(ro, vo, k * 1024); of[k][1] = bload1(ro, vo, 16384 + k * 1024); }
        sy_wait<Y_WO>(a4, 1);
        const unsigned ar = L0 + (8 * g + (c >> 2)) * 128 + ((((c >> 3) | ((g & 1) << 1))) << 5) + (c & 3) * 8;
        lp pe[8];
#pragma unroll
        for (int e = 0; e < 8; e++) pe[e] = LP((ar ^ ((e & 3) << 5)) + (e >> 2) * 65536);
        f32x4 acc[8][2];
#pragma unroll
        for (int i = 0; i < 8; i++) { acc[i][0] = (f32x4){bias0, bias0, bias0, bias0}; acc[i][1] = (f32x4){bias1, bias1, bias1, bias1}; }
        bf16x4 tl[2][8], th[2][8];
#pragma unroll
        for (int e = 0; e < 8; e++) { tl[0][e] = ltr(pe[e]); th[0][e] = ltr(pe[e] + 512); }
        SCHED();
#pragma unroll
        for (int k = 0; k < 16; k++) {
#pragma unroll
          for (int e = 0; e < 8; e++) {
            mfma_k(k == 0, acc[e][0], cat8(tl[k & 1][e], th[k & 1][e]), as_bf(of[k][0])); SCHED();
            if (k < 15) tl[(k + 1) & 1][e] = ltr(pe[e] + (k + 1) * 4096);
            SCHED();
            mfma_k(k == 0, acc[e][1], cat8(tl[k & 1][e], th[k & 1][e]), as_bf(of[k][1])); SCHED();
            if (k < 15) th[(k + 1) & 1][e] = ltr(pe[e] + (k + 1) * 4096 + 512);
            SCHED();
          }
          if (k + OD < 16) { of[k + OD][0] = bload1(ro, vo, (k + OD) * 1024); of[k + OD][1] = bload1(ro, vo, 16384 + (k + OD) * 1024); }
          if (k == 8) sy_put<Y_OK2>(a1, 1);
          if (k == 14) { FENCE(); sy_put<Y_ISS>(a1, 3); __builtin_amdgcn_s_setprio(3); }
        }
        MFMA_DONE();
        SCHED();
        ST(4);
#pragma unroll
        for (int j = 0; j < 2; j++)
#pragma unroll
          for (int i = 0; i < 4; i++) {
            const u32x4 o = {pkv(lo2(acc[2 * i][j])), pkv(hi2(acc[2 * i][j])), pkv(lo2(acc[2 * i + 1][j])), pkv(hi2(acc[2 * i + 1][j]))};
            bstore1(o, rb, lane * 16, ((8 * so + 2 * w + j) * 16 + 4 * dq + i) * 1024);
          }
        ST(6);
        WAITV(0);
        if (last_of(L0 + SY + Y_N3, 4, lane) && lane < 4) gadd(ws + WS_C4 + ((2 * so + (lane >> 1)) * 8 + 2 * dq + (lane & 1)) * 128);
        ST(7);
      } else {
        const rsrc_t rw2 = mk_rsrc(w2, F * S * 2);
        unsigned pv[6];
#pragma unroll
        for (int n = 0; n < 6; n++) pv[n] = __builtin_amdgcn_raw_buffer_load_b32(rw2, ((44 * w + 2 * (10 + gi - 22 + 2 * n) + hh) * 8 + r8) * 4096 + sq * 1024 + pc * 128, 0, 0);
        asm volatile("" ::"v"(pv[0]), "v"(pv[1]), "v"(pv[2]), "v"(pv[3]), "v"(pv[4]), "v"(pv[5]));
        sy_put<Y_ISS>(a1, 3);
        __builtin_amdgcn_s_sleep(64);
      }
      DRAIN();
    }
    asm volatile("" ::"v"(pfc));
    gwait<1>(ws + WS_C3 + ((st * 8 + ct) * 4 + w) * 128, lane == 0, ~0ull, tag + 6);
    sy_put<Y_OK3>(a1, 1);
    if (w == 1) { gwait<4>(ws + WS_C4 + (st * 8 + ct) * 128, lane == 0, ~0ull, tag + 1); sy_put<Y_OK4>(a4, 1); }
    if (w == 2 && (gi == 22 || gi == 23)) { sy_wait<Y_HA>(a4, 1); sy_wait<Y_OK3>(a4, 1); if (lane == 0) gadd(ws + WS_C5); }
    if (w == 2 && blockIdx.x == 0) {
      gwait<4>(ws + (lane < 32 ? WS_C3 + lane * 128 : WS_C5), lane <= 32, ~0ull, tag + (lane < 32 ? 6 : 16));
      if (lane == 0) __hip_atomic_store((GAS u64*)(ws + WS_EPOCH), tag >> 16, __ATOMIC_RELAXED, __HIP_MEMORY_SCOPE_AGENT);
    }
    BAR();
  }
  STFLUSH();
}

extern "C" void kernel_launch(void* const* d_in, const int* in_sizes, int n_in, void* d_out, int out_size, void* d_ws, size_t ws_size, hipStream_t stream) {
  (void)hipFuncSetAttribute((const void*)fused, hipFuncAttributeMaxDynamicSharedMemorySize, LDS_BYTES);
  fused<<<256, 512, LDS_BYTES, stream>>>((const u16*)d_in[0], (const u16*)d_in[1], (const u16*)d_in[2], (const u16*)d_in[4], (const u16*)d_in[5],
                                         (const u16*)d_in[6], (const u16*)d_in[7], (char*)d_ws, (u16*)d_out);
}
```

```cpp
#include <hip/hip_runtime.h>
#include <stdint.h>

#define PACE0 2
#define OK2K -1
#define ISSK 12
#define CODE_LEN 35840

typedef unsigned short u16;
typedef unsigned long long u64;
typedef short s16x4 __attribute__((ext_vector_type(4)));
typedef __bf16 bf16x2 __attribute__((ext_vector_type(2)));
typedef __bf16 bf16x4 __attribute__((ext_vector_type(4)));
typedef __bf16 bf16x8 __attribute__((ext_vector_type(8)));
typedef float f32x2 __attribute__((ext_vector_type(2)));
typedef float f32x4 __attribute__((ext_vector_type(4)));
typedef unsigned u32x2 __attribute__((ext_vector_type(2)));
typedef unsigned u32x4 __attribute__((ext_vector_type(4)));
#define LAS __attribute__((address_space(3)))
#define GAS __attribute__((address_space(1)))
#define DI __device__ __forceinline__

constexpr int S = 2048, D = 512, F = 1376;
constexpr float EPS = 1e-5f;
constexpr unsigned HT_BYTES = 32u * 64u * 1024u;
constexpr unsigned G_BYTES = 32u * 43u * 1024u;
constexpr unsigned O_BYTES = 128u * 16u * 1024u;
constexpr size_t WS_EPOCH = 0;
constexpr size_t WS_C1 = 4096;
constexpr size_t WS_C2 = WS_C1 + 128 * 128;
constexpr size_t WS_C3 = WS_C2 + 256 * 128;
constexpr size_t WS_C4 = WS_C3 + 1024 * 128;
constexpr size_t WS_C5 = WS_C4 + 256 * 128;
constexpr size_t WS_STAT = WS_C5 + 1024;
constexpr size_t WS_HT = WS_STAT + 16384;
constexpr size_t WS_G = WS_HT + HT_BYTES;
constexpr size_t WS_O = WS_G + G_BYTES;
constexpr size_t WS_BASE = WS_O + O_BYTES;
constexpr int SC1 = 16;
constexpr unsigned OOB = 0x80000000u;

typedef __amdgpu_buffer_rsrc_t rsrc_t;
DI rsrc_t mk_rsrc(const void* p, unsigned bytes) { return __builtin_amdgcn_make_buffer_rsrc((void*)p, 0, bytes, 0x00020000); }
DI u32x4 mk_srd(const void* p, unsigned bytes) {
  const uint64_t a = (uint64_t)p;
  u32x4 r = {(unsigned)a, (unsigned)(a >> 32) & 0xffffu, bytes, 0x00020000u};
  return r;
}
DI void glds16(u32x4 srd, unsigned voff, unsigned soff, unsigned lds) {
  asm volatile("s_mov_b32 m0, %3\n\ts_nop 0\n\tbuffer_load_dwordx4 %0, %1, %2 offen lds" ::"v"(voff), "s"(srd), "s"(soff), "s"(lds) : "memory");
}
DI void glds16x3(u32x4 srd, unsigned voff, unsigned soff, unsigned lds) {
  asm volatile("s_mov_b32 m0, %3\n\ts_nop 0\n\tbuffer_load_dwordx4 %0, %1, %2 offen lds\n\tbuffer_load_dwordx4 %0, %1, %2 offen offset:1024 lds\n\t"
               "buffer_load_dwordx4 %0, %1, %2 offen offset:2048 lds" ::"v"(voff), "s"(srd), "s"(soff), "s"(lds) : "memory");
}
#define WAITV(n) asm volatile("s_waitcnt vmcnt(" #n ")" ::: "memory")
#define FENCE() asm volatile("" ::: "memory")
#define SCHED() __builtin_amdgcn_sched_barrier(0)
#define DRAIN() __builtin_amdgcn_s_waitcnt(0x0F70)
#define RBAR() do { FENCE(); __builtin_amdgcn_s_barrier(); FENCE(); } while (0)
#define BAR() do { asm volatile("s_waitcnt lgkmcnt(0)" ::: "memory"); __builtin_amdgcn_s_barrier(); FENCE(); } while (0)

typedef LAS char* lp;
DI lp LP(unsigned a) { return (lp)(uintptr_t)a; }
DI bf16x8 cat8(bf16x4 a, bf16x4 b) { return __builtin_shufflevector(a, b, 0, 1, 2, 3, 4, 5, 6, 7); }
DI bf16x8 l128(lp p) { return *(const LAS bf16x8*)p; }
DI f32x4 lf4(lp p) { return *(const LAS f32x4*)p; }
DI bf16x4 ltr(lp p) { return __builtin_amdgcn_ds_read_tr16_b64_v4bf16((LAS bf16x4*)p); }
DI bf16x8 ltr2(lp p, int d) { return cat8(ltr(p), ltr(p + d)); }
DI f32x4 mfma32(bf16x8 a, bf16x8 b, f32x4 c) { return __builtin_amdgcn_mfma_f32_16x16x32_bf16(a, b, c, 0, 0, 0); }
DI void mfma_ip(f32x4& c, bf16x8 a, bf16x8 b) { asm volatile("v_mfma_f32_16x16x32_bf16 %0, %1, %2, %0" : "+v"(c) : "v"(a), "v"(b)); }
DI void mfma_k(bool first, f32x4& c, bf16x8 a, bf16x8 b) { if (first) c = mfma32(a, b, c); else mfma_ip(c, a, b); }
#define MFMA_DONE() asm volatile("s_nop 7\n\ts_nop 3" ::: "memory")
DI f32x4 mfma16(u32x2 a, u32x2 b, f32x4 c) { return __builtin_amdgcn_mfma_f32_16x16x16bf16_1k(__builtin_bit_cast(s16x4, a), __builtin_bit_cast(s16x4, b), c, 0, 0, 0); }
DI unsigned pkv(f32x2 f) { return __builtin_bit_cast(unsigned, __builtin_convertvector(f, bf16x2)); }
DI unsigned pk2(float a, float b) { f32x2 f = {a, b}; return pkv(f); }
DI f32x2 lo2(f32x4 v) { return __builtin_shufflevector(v, v, 0, 1); }
DI f32x2 hi2(f32x4 v) { return __builtin_shufflevector(v, v, 2, 3); }
DI f32x2 fma2(f32x2 a, f32x2 b, f32x2 c) { return __builtin_elementwise_fma(a, b, c); }
DI f32x2 bc2(float a) { f32x2 r = {a, a}; return r; }
DI float bflo(unsigned u) { return __uint_as_float(u << 16); }
DI float bfhi(unsigned u) { return __uint_as_float(u & 0xffff0000u); }
template <int CTRL> DI float dpp(float v) { return __uint_as_float(__builtin_amdgcn_update_dpp(0u, __float_as_uint(v), CTRL, 0xf, 0xf, true)); }
DI float sum16(float v) { v += dpp<0xB1>(v); v += dpp<0x4E>(v); v += dpp<0x141>(v); v += dpp<0x140>(v); return v; }
DI f32x2 swap16(float a, float b) { auto r = __builtin_amdgcn_permlane16_swap(__float_as_uint(a), __float_as_uint(b), false, false); f32x2 o = {__uint_as_float(r[0]), __uint_as_float(r[1])}; return o; }
DI f32x2 swap32(float a, float b) { auto r = __builtin_amdgcn_permlane32_swap(__float_as_uint(a), __float_as_uint(b), false, false); f32x2 o = {__uint_as_float(r[0]), __uint_as_float(r[1])}; return o; }
DI float sel(unsigned m, float a, float b) { return __uint_as_float((__float_as_uint(a) & m) | (__float_as_uint(b) & ~m)); }
DI u32x4 bload(rsrc_t r, unsigned v, unsigned s) { return __builtin_amdgcn_raw_buffer_load_b128(r, v, s, 0); }
DI u32x4 bload1(rsrc_t r, unsigned v, unsigned s) { return __builtin_amdgcn_raw_buffer_load_b128(r, v, s, SC1); }
DI void bstore1(u32x4 d, rsrc_t r, unsigned v, unsigned s) { __builtin_amdgcn_raw_buffer_store_b128(d, r, v + s, 0, SC1); }
DI bf16x8 as_bf(u32x4 v) { return __builtin_bit_cast(bf16x8, v); }
DI u64 uni64(u64 v) { return ((u64)(unsigned)__builtin_amdgcn_readfirstlane((unsigned)(v >> 32)) << 32) | (unsigned)__builtin_amdgcn_readfirstlane((unsigned)v); }

constexpr unsigned LDS_BYTES = 163840;
constexpr unsigned QXB = 14336;
constexpr unsigned QST = QXB + 24576;
constexpr unsigned QSTAT = 4 * QST;
constexpr unsigned LNROW = 131072;
constexpr unsigned G1STG = 0;
constexpr unsigned P2STG = 49152;
constexpr unsigned SY = LDS_BYTES - 512;
enum { Y_FULL = 0, Y_FREE = 16, Y_HA = 32, Y_HB = 48, Y_ST = 64, Y_QS = 80, Y_ISS = 96, Y_E2 = 112, Y_RA = 128, Y_OK2 = 144, Y_OK3 = 160, Y_OK4 = 176, Y_WO = 192,
       Y_N1 = 208, Y_N2 = 212, Y_N3 = 216, Y_TAG = 224, Y_SIG = 240 };
static_assert(QSTAT + 896 <= SY && P2STG + 98304 <= SY, "lds");
constexpr unsigned SPIN_GLB = 1u << 17;

template <int OFF> DI void sy_put(unsigned a, unsigned v) { asm volatile("ds_write_b32 %0, %1 offset:%2" ::"v"(a), "v"(v), "i"(OFF) : "memory"); }
template <int OFF> DI void sy_wait(unsigned a4, unsigned target) {
  unsigned t, n;
  asm volatile("s_mov_b32 %1, 0x200000\n"
               "1:\n\tds_read_b32 %0, %2 offset:%4\n\ts_waitcnt lgkmcnt(0)\n\tv_cmp_gt_u32 vcc, %3, %0\n\ts_cbranch_vccz 2f\n\t"
               "s_sleep 1\n\ts_sub_u32 %1, %1, 1\n\ts_cmp_lg_u32 %1, 0\n\ts_cbranch_scc1 1b\n"
               "2:" : "=&v"(t), "=&s"(n) : "v"(a4), "s"(target), "i"(OFF) : "vcc", "scc", "memory");
}
DI bool last_of(unsigned cnt, unsigned n, int lane) {
  unsigned old = 0;
  if (lane == 0) asm volatile("ds_add_rtn_u32 %0, %1, %2\n\ts_waitcnt lgkmcnt(0)" : "=&v"(old) : "v"(cnt), "v"(1u) : "memory");
  return (unsigned)__builtin_amdgcn_readfirstlane(old) == n - 1;
}
DI void gadd(char* p) { (void)__hip_atomic_fetch_add((GAS u64*)p, 1ull, __ATOMIC_RELAXED, __HIP_MEMORY_SCOPE_AGENT); }
DI u64 gpeek(const char* p) {
  u64 z = 0;
  asm volatile("" : "+v"(z));
  return __hip_atomic_fetch_add((GAS u64*)p, z, __ATOMIC_RELAXED, __HIP_MEMORY_SCOPE_AGENT);
}
template <int SL> DI void gwait(const char* p, bool on, u64 mask, u64 want) {
  FENCE();
  bool ok = !on;
#pragma unroll 1
  for (unsigned n = 0; n < SPIN_GLB; n++) {
    if (!ok) ok = (gpeek(p) & mask) == want;
    if (__all(ok)) break;
    __builtin_amdgcn_s_sleep(SL);
  }
  FENCE();
}

#define ST(i)
#define STDECL
#define STFLUSH()

__global__ __launch_bounds__(512, 2) void fused(const u16* __restrict__ x, const u16* __restrict__ wqkv, const u16* __restrict__ wo, const u16* __restrict__ w1,
                                                const u16* __restrict__ b1, const u16* __restrict__ w2, const u16* __restrict__ b2, char* __restrict__ ws,
                                                u16* __restrict__ out) {
  extern __shared__ __attribute__((aligned(16))) char smem[];
  uint64_t pc0; asm volatile("s_getpc_b64 %0" : "=s"(pc0));
  const unsigned L0 = (unsigned)(uintptr_t)(LAS char*)smem;
  const int tid = threadIdx.x, lane = tid & 63;
  const int W = __builtin_amdgcn_readfirstlane(tid >> 6), w = W & 3;
  const int xcd = blockIdx.x & 7, idx = blockIdx.x >> 3;
  const int sq = xcd >> 1, hh = xcd & 1;
  const int st = sq * 8 + (idx >> 2), ct = hh * 4 + (idx & 3);
  const int gi = idx;
  const int rt = (22 * sq + gi) >> 2, hg = hh * 4 + ((22 * sq + gi) & 3);
  const int c = lane & 15, g = lane >> 4, r8 = lane >> 3, pc = lane & 7;
  const f32x4 z4 = {0.f, 0.f, 0.f, 0.f};
  STDECL ST(0);
  const unsigned pfc = __builtin_amdgcn_raw_buffer_load_b32(mk_rsrc((const void*)(pc0 & ~255ull), CODE_LEN), ((idx & 7) * 8 + W) * 1024 + (lane & 7) * 128, 0, 0);
  {
    const unsigned on = (W >> 1) & 1;
    *(LAS unsigned*)LP(L0 + (on ? 0 : SY) + ((tid >> 5) & 3) * (on ? QST : 128) + (tid & 31) * 4) = on ? 0x3f803f80u : 0u;
  }
  const unsigned a4 = L0 + SY + (lane & 3) * 4, a1 = L0 + SY + w * 4;
  asm volatile("" ::"s"(x), "s"(wqkv), "s"(wo), "s"(w1), "s"(b1), "s"(w2), "s"(b2), "s"(ws), "s"(out));
  const u32x4 sx = mk_srd(x, S * D * 2), sw = mk_srd(wqkv, D * 3 * D * 2);
  const unsigned vx0 = (unsigned)((96 * rt - 16 + 8 * w + r8) * 1024 + ((pc ^ (((w & 1) << 2) | (r8 >> 1))) << 4));
  const unsigned vsw = (((pc >> 1) ^ (((r8 >> 1) & 1) | ((w & 1) << 1))) << 5) | ((pc & 1) << 4);
  const unsigned vwq = (8 * w + r8) * 3072 + hg * 128 + vsw;
  auto head = [&](int s) { glds16x3(sw, vwq, s * 64 * 3072, L0 + (s & 3) * QST + QXB + w * 3072); };
  auto rest = [&](int s) {
    const unsigned b = L0 + (s & 3) * QST + w * 1024;
    if (lane >= 8 || w != 0) glds16(sx, vx0, s * 128, b);
    glds16(sx, vx0 + 32768u, s * 128, b + 4096);
    glds16(sx, vx0 + 65536u, s * 128, b + 8192);
    if (w < 2) glds16(sx, vx0 + 98304u, s * 128, b + 12288);
    glds16x3(sw, vwq, s * 64 * 3072 + 32 * 3072, b + QXB + 12288 + w * 2048);
  };
  const int un = sq * 16 + hh * 8 + gi - 24;
  u32x4 xr[4];
  ST(11);
  if (gi >= 24) {
    asm volatile("buffer_load_dwordx4 %0, %4, %5, %6 offen\n\tbuffer_load_dwordx4 %1, %4, %5, %6 offen offset:1024\n\t"
                 "buffer_load_dwordx4 %2, %4, %5, %6 offen offset:2048\n\tbuffer_load_dwordx4 %3, %4, %5, %6 offen offset:3072"
                 : "=&v"(xr[0]), "=&v"(xr[1]), "=&v"(xr[2]), "=&v"(xr[3]) : "v"(lane * 16), "s"(sx), "s"((un * 32 + 4 * W) * 1024) : "memory");
  } else {
    asm volatile("" : "=v"(xr[0]), "=v"(xr[1]), "=v"(xr[2]), "=v"(xr[3]));
    if (gi < 22 && W >= 4) { head(0); rest(0); }
  }
  BAR();
  u64 tag = 0;

  if (gi >= 24) {
    const u64 ep = __hip_atomic_load((const GAS u64*)(ws + WS_EPOCH), __ATOMIC_RELAXED, __HIP_MEMORY_SCOPE_AGENT);
    const unsigned pfw = *(const GAS unsigned*)((lane & 31) < 24 ? (const char*)wqkv + (un * 8 + W) * 3072 + (lane & 31) * 128 : (const char*)wo + (un * 8 + W) * 1024 + (lane & 7) * 128);
    WAITV(2);
    asm volatile("" : "+v"(xr[0]), "+v"(xr[1]), "+v"(xr[2]), "+v"(xr[3]));
    f32x2 v[4][4];
    float p[4], s1[4], s2[4];
    auto red4 = [&](const float (&a)[4]) {
      f32x2 t = swap16(a[0], a[1]), u = swap16(a[2], a[3]);
      t = swap32(t[0] + t[1], u[0] + u[1]);
      return sum16(t[0] + t[1]);
    };
#pragma unroll
    for (int rr = 0; rr < 4; rr++) {
      p[rr] = bflo(__builtin_amdgcn_readfirstlane(xr[rr][0]));
      f32x2 a = {0.f, 0.f}, b = {0.f, 0.f};
#pragma unroll
      for (int j = 0; j < 4; j++) { const f32x2 t = {bflo(xr[rr][j]), bfhi(xr[rr][j])}; v[rr][j] = t - bc2(p[rr]); a += v[rr][j]; b = fma2(v[rr][j], v[rr][j], b); }
      s1[rr] = a[0] + a[1]; s2[rr] = b[0] + b[1];
    }
    if (W == 0) {
      const size_t o = lane < 2 ? WS_C1 + (un * 2 + lane) * 128 : WS_C2 + (un * 24 + lane - 2) * 128;
      u64 e = ep;
      asm volatile("" : "+v"(e));
      if (lane < (un == 63 ? 27 : 26)) __hip_atomic_store((GAS u64*)(ws + o), (e + 1) << 16, __ATOMIC_RELAXED, __HIP_MEMORY_SCOPE_AGENT);
    }
    {
      const float m = red4(s1) * (1.f / D), var = fmaxf(red4(s2) * (1.f / D) - m * m, 0.f);
      const float r = __builtin_amdgcn_rsqf(var + EPS);
      const float a = r * __builtin_amdgcn_rsqf(var * r * r + EPS), ma = -m * a;
#pragma unroll
      for (int rr = 0; rr < 4; rr++) {
        const f32x2 av = bc2(__uint_as_float(__builtin_amdgcn_readlane(__float_as_uint(a), 16 * rr))), cv = bc2(__uint_as_float(__builtin_amdgcn_readlane(__float_as_uint(ma), 16 * rr)));
        u32x4 h;
#pragma unroll
        for (int j = 0; j < 4; j++) h[j] = pkv(fma2(v[rr][j], av, cv));
        *(LAS u32x4*)LP(L0 + (4 * W + rr) * 1088 + ((W >> 1) & 1) * 32 + lane * 16) = h;
      }
      const float pg = g == 0 ? p[0] : g == 1 ? p[1] : g == 2 ? p[2] : p[3];
      *(LAS float*)LP(L0 + QSTAT + (W >> 1) * 64 + (W & 1) * 16 + g * 4 + (lane & 1) * 32) = (lane & 1) ? (m + pg) * r : r;
    }
    ST(12);
    char* const c1p = ws + WS_C1 + (sq * 32 + (lane & 31)) * 128;
    u64 chk = 0;
    if (W == 1 && lane < 32) asm volatile("global_atomic_add_x2 %0, %1, %0, off sc0" : "+v"(chk) : "v"(c1p) : "memory");
    BAR();
    {
      bf16x8 tv[4];
#pragma unroll
      for (int n = 0; n < 4; n++) tv[n] = ltr2(LP(L0 + (8 * g + (c >> 2)) * 1088 + (g & 1) * 32 + W * 128 + (c & 3) * 8) + n * 32, 4 * 1088);
      const u32x4 sv = *(const LAS u32x4*)LP(L0 + QSTAT + c * 16);
      const rsrc_t rht = mk_rsrc(ws + WS_HT, HT_BYTES);
#pragma unroll
      for (int n = 0; n < 4; n++) bstore1(__builtin_bit_cast(u32x4, tv[n]), rht, lane * 16, ((4 * W + n) * 64 + un) * 1024);
      if (W == 2) bstore1(sv, mk_rsrc(ws + WS_STAT, S * 8), lane < 16 ? un * 256 + lane * 16 : OOB, 0);
    }
    ST(1);
    WAITV(0);
    DRAIN();
    u64 e = ep;
    asm volatile("" : "+v"(e) : "v"(pfw));
    tag = (uni64(e) + 1) << 16;
    if (W == 1) { asm volatile("" : "+v"(chk)); if (__any(lane < 32 && (chk >> 16) != (tag >> 16))) gwait<2>(c1p, lane < 32, ~0xffffull, tag); }
    if (last_of(L0 + SY + Y_N1, 8, lane)) { if (lane < 32) gadd(c1p); sy_put<Y_SIG>(a4, 1); }
    ST(2);
  } else if (gi >= 22) {
    unsigned p3[3] = {0, 0, 0};
    if (W >= 4) {
#pragma unroll
      for (int n = 0; n < 3; n++) p3[n] = __builtin_amdgcn_raw_buffer_load_b32(mk_rsrc(wqkv, D * 3 * D * 2), hh ? OOB : ((n * 8 + (gi - 22) * 4 + w) * 64 + lane) * 1024 + 384, 0, 0);
    } else if (W > 0) {
#pragma unroll
      for (int n = 0; n < 2; n++) p3[n] = __builtin_amdgcn_raw_buffer_load_b32(mk_rsrc(x, S * D * 2), (unsigned)((((22 * sq) >> 2) * 96 - 16 + (n * 6 + (gi - 22) * 3 + W - 1) * 52 + min(lane, 51)) * 1024 + 384), 0, 0);
    }
    tag = (uni64(__hip_atomic_load((const GAS u64*)(ws + WS_EPOCH), __ATOMIC_RELAXED, __HIP_MEMORY_SCOPE_AGENT)) + 1) << 16;
    if (W < 4) sy_put<Y_HA>(a1, 1);
    asm volatile("" ::"v"(p3[0]), "v"(p3[1]), "v"(p3[2]));
  }

  if (W < 4) {
    if (gi < 22) {
      f32x4 aq[7], ak[7], av[7];
#pragma unroll
      for (int b = 0; b < 7; b++) { ak[b] = z4; av[b] = z4; aq[b] = z4; }
      const unsigned ax = L0 + c * 128 + ((g ^ (c >> 1)) << 4);
      const unsigned aw = L0 + QXB + g * 3072 + (c >> 2) * 128 + ((w ^ ((c >> 3) | ((g & 1) << 1))) << 5) + (c & 3) * 8;
      bf16x8 xf[2][7];
      bf16x4 wl[2][3], wh[2][3];
      const lp pxa[2][2] = {{LP(ax), LP(ax ^ 64)}, {LP(ax + 2 * QST), LP((ax ^ 64) + 2 * QST)}}, pwa[2] = {LP(aw), LP(aw + 2 * QST)};
      auto RD = [&](int n, int i, int slot, int kk) {
        const lp px = pxa[slot >> 1][kk] + (slot & 1) * QST, pw = pwa[slot >> 1] + (slot & 1) * QST + kk * 12288;
        if (i == 0) wl[n][1] = ltr(pw + 1024);
        if (i == 1) wh[n][1] = ltr(pw + 1024 + 512);
        if (i == 2) xf[n][6] = l128(px + 12288);
        if (i == 3) wl[n][2] = ltr(pw + 2048);
        if (i == 4) wh[n][2] = ltr(pw + 2048 + 512);
        if (i == 5) wl[n][0] = ltr(pw);
        if (i == 6) wh[n][0] = ltr(pw + 512);
        if (i >= 7) xf[n][12 - i] = l128(px + (12 - i) * 2048);
      };
      auto MM = [&](int cur, int m) {
        const int t = 6 - m / 3;
        if (m % 3 == 0) mfma_ip(ak[t], cat8(wl[cur][1], wh[cur][1]), xf[cur][t]);
        if (m % 3 == 1) mfma_ip(av[t], xf[cur][t], cat8(wl[cur][2], wh[cur][2]));
        if (m % 3 == 2) mfma_ip(aq[t], cat8(wl[cur][0], wh[cur][0]), xf[cur][t]);
      };
      RBAR();
      ST(2);
#pragma unroll
      for (int i = 0; i < 13; i++) RD(0, i, 0, 0);
      SCHED();
#pragma unroll 1
      for (int s4 = 0; s4 < 8; s4 += 4) {
#pragma unroll
        for (int sl = 0; sl < 4; sl++) {
#pragma unroll
          for (int m = 0; m < 21; m++) {
            MM(0, m); SCHED();
            if (m < 13) RD(1, m, sl, 1);
            if (m == 19 && (sl < 3 || s4 == 0)) BAR();
            SCHED();
          }
#pragma unroll
          for (int m = 0; m < 21; m++) {
            MM(1, m); SCHED();
            if (m < 13) RD(0, m, (sl + 1) & 3, 0);
            SCHED();
          }
        }
      }
      MFMA_DONE();
      SCHED();
      ST(3);
      f32x2 cq[2], ck[2];
#pragma unroll
      for (int j = 0; j < 4; j++) { cq[j >> 1][j & 1] = -dpp<0x150>(aq[0][j]); ck[j >> 1][j & 1] = -dpp<0x150>(ak[0][j]); }
      f32x2 cvt = swap16(av[0][0], av[0][0]);
      cvt = swap32(cvt[0], cvt[0]);
      const f32x2 cv = bc2(-cvt[0]);
      sy_wait<Y_ST>(a4, 1);
      u32x2 qf[6], kf[7], vf[7];
      {
        float sr[7], sm[7]; f32x4 vr[7], vm[7];
        const lp pcs = LP(L0 + QSTAT + (c >> 3) * 64 + (c & 7) * 4), prs = LP(L0 + QSTAT + (g >> 1) * 64 + (g & 1) * 16);
#pragma unroll
        for (int t = 0; t < 7; t++) {
          sr[t] = *(const LAS float*)(pcs + t * 128); sm[t] = *(const LAS float*)(pcs + t * 128 + 32);
          vr[t] = lf4(prs + t * 128); vm[t] = lf4(prs + t * 128 + 32);
        }
        SCHED();
#pragma unroll
        for (int t = 0; t < 7; t++) {
          const f32x2 r = bc2(sr[t]), m = bc2(sm[t]);
          f32x2 y[6] = {m * ck[0], m * ck[1], m * cq[0], m * cq[1], lo2(vm[t]) * cv, hi2(vm[t]) * cv};
          SCHED();
          y[0] = fma2(r, lo2(ak[t]), y[0]); y[1] = fma2(r, hi2(ak[t]), y[1]); y[2] = fma2(r, lo2(aq[t]), y[2]); y[3] = fma2(r, hi2(aq[t]), y[3]);
          y[4] = fma2(lo2(vr[t]), lo2(av[t]), y[4]); y[5] = fma2(hi2(vr[t]), hi2(av[t]), y[5]);
          SCHED();
          kf[t][0] = pkv(y[0]); kf[t][1] = pkv(y[1]);
          if (t > 0) { qf[t - 1][0] = pkv(y[2]); qf[t - 1][1] = pkv(y[3]); }
          vf[t][0] = pkv(y[4]); vf[t][1] = pkv(y[5]);
        }
        SCHED();
      }
      u32x2 op[6];
      {
        unsigned om = 0u - (unsigned)(c & 1);
        asm("" : "+v"(om));
        const bool cur0 = 4 * g + (c & 1) <= c, cur1 = 4 * g + (c & 1) + 2 <= c;
        constexpr float SC = 0.25f * 1.4426950408889634f;
        f32x4 sp[6], su[6];
#pragma unroll
        for (int t = 0; t < 6; t++) { sp[t] = mfma16(kf[t], qf[t], z4); su[t] = mfma16(kf[t + 1], qf[t], z4); }
        float l0[6], l1[6], mx[8];
#pragma unroll
        for (int t = 0; t < 6; t++) {
          const float p0 = sel(om, sp[t][1], sp[t][0]), p1 = sel(om, sp[t][3], sp[t][2]);
          const float u0 = sel(om, su[t][1], su[t][0]), u1 = sel(om, su[t][3], su[t][2]);
          l0[t] = cur0 ? u0 : p0; l1[t] = cur1 ? u1 : p1;
          if (t == 0 && rt == 0) { l0[t] = cur0 ? l0[t] : -4e9f; l1[t] = cur1 ? l1[t] : -4e9f; }
          mx[t] = fmaxf(l0[t], l1[t]);
        }
        {
          const f32x2 a = swap16(mx[0], mx[1]), b = swap16(mx[2], mx[3]), a2 = swap16(mx[4], mx[5]);
          const float n2 = fmaxf(a2[0], a2[1]);
          const f32x2 d = swap32(fmaxf(a[0], a[1]), fmaxf(b[0], b[1])), d2 = swap32(n2, n2);
          const float m = fmaxf(d[0], d[1]) * -SC, m2 = fmaxf(d2[0], d2[1]) * -SC;
          const f32x2 e = swap32(m, m), f0 = swap16(e[0], e[0]), f1 = swap16(e[1], e[1]), f2 = swap16(m2, m2);
          mx[0] = f0[0]; mx[1] = f0[1]; mx[2] = f1[0]; mx[3] = f1[1]; mx[4] = f2[0]; mx[5] = f2[1];
        }
        const u32x4 ones = {0x3f803f80u, 0x3f803f80u, 0x3f803f80u, 0x3f803f80u};
        const unsigned keep = om ^ 0x0000ffffu;
        const unsigned k00 = cur0 ? 0u : keep, k01 = cur1 ? 0u : keep, k10 = cur0 ? keep : 0u, k11 = cur1 ? keep : 0u;
        f32x4 o[6], den[6];
#pragma unroll
        for (int t = 0; t < 6; t++) {
          const float e0 = __builtin_amdgcn_exp2f(__builtin_fmaf(l0[t], SC, mx[t])), e1 = __builtin_amdgcn_exp2f(__builtin_fmaf(l1[t], SC, mx[t]));
          const unsigned b0 = pk2(e0, e0), b1v = pk2(e1, e1);
          const u32x4 pu = {b0 & k00, b1v & k01, b0 & k10, b1v & k11}, vv = {vf[t][0], vf[t][1], vf[t + 1][0], vf[t + 1][1]};
          o[t] = mfma32(as_bf(vv), as_bf(pu), z4);
          den[t] = mfma32(as_bf(ones), as_bf(pu), z4);
        }
#pragma unroll
        for (int t = 0; t < 6; t++) {
          const f32x2 inv = bc2(__builtin_amdgcn_rcpf(den[t][0]));
          op[t][0] = pkv(lo2(o[t]) * inv); op[t][1] = pkv(hi2(o[t]) * inv);
        }
      }
      {
        const rsrc_t ro = mk_rsrc(ws + WS_O, O_BYTES);
#pragma unroll
        for (int t = 0; t < 6; t += 2) {
          const auto r0 = __builtin_amdgcn_permlane16_swap(op[t][0], op[t + 1][0], false, false);
          const auto r1 = __builtin_amdgcn_permlane16_swap(op[t][1], op[t + 1][1], false, false);
          const u32x4 v = {r0[0], r1[0], r0[1], r1[1]};
          if (t == 0 || rt < 21) bstore1(v, ro, (g & 1) * 16384 + ((g >> 1) * 16 + c) * 16, ((6 * rt + t) * 16 + 2 * hg + (w >> 1)) * 1024 + (w & 1) * 512);
        }
      }
      ST(13);
      sy_put<Y_QS>(a1, 1);
      WAITV(0);
      if (last_of(L0 + SY + Y_N2, 4, lane) && lane < (rt < 21 ? 12 : 4)) gadd(ws + WS_C2 + (12 * rt + lane) * 128);
      { const u32x2 tg = *(const LAS u32x2*)LP(L0 + SY + Y_TAG); tag = ((u64)(unsigned)__builtin_amdgcn_readfirstlane(tg[1]) << 32) | (unsigned)__builtin_amdgcn_readfirstlane(tg[0]); }
      ST(4);
    } else {
      const rsrc_t rw2 = mk_rsrc(w2, F * S * 2);
      if (gi >= 24) {
        sy_wait<Y_OK2>(a4, 1);
        const int m = gi - 24 + 8 * r8;
        const unsigned t = __builtin_amdgcn_raw_buffer_load_b32(rw2, m < 22 ? ((44 * w + 2 * m + (hh ^ 1)) * 8 + pc) * 4096 + sq * 1024 + 384 : OOB, 0, 0);
        asm volatile("" ::"v"(t));
      } else {
        unsigned pv[5];
#pragma unroll
        for (int n = 0; n < 5; n++) pv[n] = __builtin_amdgcn_raw_buffer_load_b32(rw2, ((44 * w + 2 * (gi - 22 + 2 * n) + hh) * 8 + r8) * 4096 + sq * 1024 + pc * 128, 0, 0);
        asm volatile("" ::"v"(pv[0]), "v"(pv[1]), "v"(pv[2]), "v"(pv[3]), "v"(pv[4]));
      }
      ST(4);
    }
    asm volatile("" ::"v"(pfc));

    const rsrc_t rgg = mk_rsrc(ws + WS_G, G_BYTES);
    const unsigned vg = lane * 16 + (4 * ct * 43 + 11 * w) * 1024;
    const u32x4 sw2 = mk_srd(w2, F * S * 2);
    const unsigned vw2 = (352 * w + r8) * 4096 + st * 128 + ((((pc >> 1) ^ ((r8 >> 1) & 1)) << 5) | ((pc & 1) << 4));
    const unsigned lw = L0 + P2STG + w * 24576;
    const unsigned ar = lw + (8 * g + (c >> 2)) * 128 + ((((c >> 3) | ((g & 1) << 1))) << 5) + (c & 3) * 8;
    u32x4 gf[11][4];
    auto GW = [&](int k, int slot) {
#pragma unroll
      for (int j = 0; j < 4; j++) glds16(sw2, (k == 10 && w == 3) ? OOB : vw2 ^ ((j & 1) << 6), (32 * k + 8 * j) * 4096, lw + slot * 4096 + j * 1024);
    };
    auto LG1 = [&](int k, int j) { gf[k][j] = bload1(rgg, (k == 10 && w == 3) ? OOB : vg, j * 43 * 1024 + k * 1024); };
    lp pr[4];
#pragma unroll
    for (int j = 0; j < 4; j++) pr[j] = LP(ar ^ (((w + j) & 3) << 5));
    bf16x8 tw[2][4];
    bf16x8 tk[4][4];
    auto TR = [&](bf16x8 (&t)[4], int slot) {
#pragma unroll
      for (int j = 0; j < 4; j++) t[j] = ltr2(pr[j] + slot * 4096, 512);
      asm volatile("" ::"v"(t[0]), "v"(t[1]), "v"(t[2]), "v"(t[3]) : "memory");
    };
    const int srow = 64 * st + 16 * w + c;
    u32x4 xq[2];
    if (gi < 22) { sy_wait<Y_QS>(a4, 1); sy_wait<Y_ISS>(a4, 1); }
    ST(5);
    asm volatile("buffer_load_dwordx4 %0, %2, %3, 0 offen\n\tbuffer_load_dwordx4 %1, %2, %3, 0 offen offset:64"
                 : "=&v"(xq[0]), "=&v"(xq[1]) : "v"((srow * D + 64 * ct + 16 * (g & 1) + 8 * (g >> 1)) * 2), "s"(sx) : "memory");
    const char* const c3p = ws + WS_C3 + ((st * 8 + ct) * 4 + w) * 128;
    u64 c3v = 0;
    const int s6 = -12 - 5 * w;
    if (gi < 22) {
      GW(7, 0); GW(8, 1);
      RBAR();
      GW(9, 2); GW(10, 3); GW(4, 4); GW(5, 5);
      WAITV(16); TR(tk[0], 0); TR(tk[1], 1);
      WAITV(8); TR(tk[2], 2); TR(tk[3], 3);
      RBAR();
      GW(0, 0); GW(1, 1); GW(2, 2);
      if (lane == 0) asm volatile("global_atomic_add_x2 %0, %1, %0, off sc0" : "+v"(c3v) : "v"(c3p) : "memory");
      GW(3, 3); GW(6, s6);
      WAITV(17); TR(tw[0], 0);
    } else {
      const int sc = 20 - 5 * w;
      auto TS = [&](bf16x8 (&t)[4], int k) {
#pragma unroll
        for (int j = 0; j < 4; j++) glds16(sw2, (k == 10 && w == 3) ? OOB : vw2 ^ ((j & 1) << 6), (32 * k + 8 * j) * 4096, lw + sc * 4096 + j * 1024);
        WAITV(0);
#pragma unroll
        for (int j = 0; j < 4; j++) t[j] = ltr2(pr[j] + sc * 4096, 512);
        asm volatile("" ::"v"(t[0]), "v"(t[1]), "v"(t[2]), "v"(t[3]) : "memory");
      };
      TS(tk[0], 7); TS(tk[1], 8); TS(tk[2], 9); TS(tk[3], 10); TS(tw[0], 0);
      sy_put<Y_QS>(a1, 1);
      ST(12);
      sy_wait<Y_QS>(a4, 1);
      sy_wait<Y_ISS>(a4, 3);
      GW(1, 1); GW(2, 2); GW(3, 3); GW(4, 4);
      if (lane == 0) asm volatile("global_atomic_add_x2 %0, %1, %0, off sc0" : "+v"(c3v) : "v"(c3p) : "memory");
      GW(5, 5); GW(6, s6);
    }
    asm volatile("" : "+v"(xq[0]), "+v"(xq[1]));
    u32x2 xv[4];
#pragma unroll
    for (int i = 0; i < 2; i++) {
      const auto r0 = __builtin_amdgcn_permlane16_swap(xq[i][0], xq[i][2], false, false), r1 = __builtin_amdgcn_permlane16_swap(xq[i][1], xq[i][3], false, false);
      xv[2 * i][0] = r0[0]; xv[2 * i][1] = r1[0]; xv[2 * i + 1][0] = r0[1]; xv[2 * i + 1][1] = r1[1];
    }
    f32x4 acc[4][4];
#pragma unroll
    for (int i = 0; i < 4; i++) {
      acc[i][0] = (f32x4){bflo(xv[i][0]), bfhi(xv[i][0]), bflo(xv[i][1]), bfhi(xv[i][1])};
#pragma unroll
      for (int j = 1; j < 4; j++) acc[i][j] = z4;
      asm volatile("" : "+v"(acc[i][0]));
    }
    ST(6);
    WAITV(8);
    asm volatile("" : "+v"(c3v));
    if (uni64(c3v) != tag + 6) gwait<0>(c3p, lane == 0, ~0ull, tag + 6);
    ST(7);
    constexpr int GD = 4;
#pragma unroll
    for (int k = 0; k < GD; k++)
#pragma unroll
      for (int j = 0; j < 4; j++) LG1(k, j);
    WAITV(16);
    SCHED();
    u32x4 bv[2];
#pragma unroll
    for (int k = 0; k < 10; k++) {
      if (k + GD == 11) {
        sy_wait<Y_OK4>(a4, 1);
        const rsrc_t rb = mk_rsrc(ws + WS_BASE, S * D * 2);
#pragma unroll
        for (int i = 0; i < 2; i++) bv[i] = bload1(rb, lane * 16, ((4 * st + w) * 16 + 2 * ct + i) * 1024);
      }
#pragma unroll
      for (int i = 0; i < 4; i++) {
#pragma unroll
        for (int j = 0; j < 4; j++) {
          mfma_k(k == 0, acc[i][j], as_bf(gf[k][i]), k < 7 ? tw[k & 1][j] : tk[k < 7 ? 0 : k - 7][j]);
          if (j == 0 && k + 1 < 7) { SCHED(); tw[(k + 1) & 1][i] = ltr2(pr[i] + (k + 1 < 6 ? k + 1 : s6) * 4096, 512); SCHED(); }
        }
        SCHED();
        if (k + GD < 11) LG1(k + GD, i);
        SCHED();
      }
    }
#pragma unroll
    for (int jj = 1; jj <= 4; jj++) {
#pragma unroll
      for (int i = 0; i < 4; i++) {
        acc[i][jj & 3] = mfma32(as_bf(gf[10][i]), tk[3][jj & 3], acc[i][jj & 3]); SCHED();
        if (jj >= 2) *(LAS f32x4*)(LP(lw + lane * 16) + ((jj - 2) * 4 + i) * 1024) = acc[i][jj - 1];
        SCHED();
      }
    }
    ST(8);
    BAR();
    ST(9);
    f32x4 u[4];
#pragma unroll
    for (int i = 0; i < 4; i++) {
      u[i] = acc[i][0] + (f32x4){bflo(bv[i >> 1][2 * (i & 1)]), bfhi(bv[i >> 1][2 * (i & 1)]), bflo(bv[i >> 1][2 * (i & 1) + 1]), bfhi(bv[i >> 1][2 * (i & 1) + 1])};
#pragma unroll
      for (int j = 1; j < 4; j++) u[i] += lf4(LP(L0 + P2STG + ((w - j) & 3) * 24576 + lane * 16) + ((j - 1) * 4 + i) * 1024);
    }
#pragma unroll
    for (int i = 0; i < 4; i += 2) {
      const auto r0 = __builtin_amdgcn_permlane16_swap(pkv(lo2(u[i])), pkv(lo2(u[i + 1])), false, false);
      const auto r1 = __builtin_amdgcn_permlane16_swap(pkv(hi2(u[i])), pkv(hi2(u[i + 1])), false, false);
      const u32x4 o = {r0[0], r1[0], r0[1], r1[1]};
      *(u32x4*)(out + srow * D + 64 * ct + 16 * i + 16 * (g & 1) + 8 * (g >> 1)) = o;
    }
    ST(10);
  } else {
    if (gi < 22) {
      const bool c1on = lane < (w == 3 ? 4 : 1);
      const char* const c1w = ws + WS_C1 + (((w == 3 ? 0 : w) + (lane & 3)) * 32 + (blockIdx.x & 31)) * 128;
      u64 c1v = 0, ep = 0;
      head(1); rest(1);
#pragma unroll 1
      for (int s = 2; s < 8; s++) {
        head(s);
        WAITV(12); RBAR();
        rest(s);
      }
      ST(1);
      u32x4 stv4;
      auto c1_done = [&]() {
        asm volatile("" : "+v"(c1v), "+v"(ep));
        tag = (uni64(ep) + 1) << 16;
        if (__any(c1on && c1v != tag + 16)) gwait<2>(c1w, c1on, ~0ull, tag + 16);
        if (w == 3) stv4 = bload1(mk_rsrc(ws + WS_STAT, S * 8), (unsigned)((12 * rt - 2) * 64 + min(lane, 55) * 16), 0);
      };
      auto put_stats = [&]() {
        if (w == 3) {
          const u32x2 tg = {(unsigned)tag, (unsigned)(tag >> 32)};
          *(LAS u32x2*)LP(L0 + SY + Y_TAG) = tg;
          *(LAS u32x4*)LP(L0 + QSTAT + min(lane, 55) * 16) = stv4;
          sy_put<Y_ST>(a4, 1);
        }
      };
      const int ft = rt, dt = hg;
      const rsrc_t rht = mk_rsrc(ws + WS_HT, HT_BYTES);
      const u32x4 s1 = mk_srd(w1, S * F * 2);
      u32x4 fb[16][4];
      const unsigned sz = (((pc >> 1) ^ ((r8 >> 1) & 1)) << 5) | ((pc & 1) << 4), vwb = (512 * w + r8) * 2752 + ft * 128;
      const unsigned vw0 = (ft == 21 && sz >= 64) ? OOB : vwb + sz, vw1 = (ft == 21 && sz < 64) ? OOB : vwb + (sz ^ 64);
      const unsigned vb = lane * 16 + (4 * dt * 64 + 16 * w) * 1024;
      const unsigned lb = L0 + G1STG + w * 4096;
      const unsigned ar = lb + (8 * g + (c >> 2)) * 128 + ((((c >> 3) | ((g & 1) << 1))) << 5) + (c & 3) * 8;
      lp pe[4];
#pragma unroll
      for (int e = 0; e < 4; e++) pe[e] = LP(ar ^ (e << 5));
      auto GW = [&](int k, int j) { glds16(s1, (j & 1) ? vw1 : vw0, (32 * k + 8 * j) * 2752, lb + (k % 3) * 16384 + j * 1024); };
      auto LB = [&](int k, int j) { fb[k][j] = bload1(rht, vb, ((w + j) & 3) * 65536 + k * 1024); };
      u32x2 bb[4];
      asm volatile("global_load_dwordx2 %0, %1, off sc1" : "=v"(ep) : "v"(ws + WS_EPOCH) : "memory");
      if (c1on) asm volatile("global_atomic_add_x2 %0, %1, %0, off sc0" : "+v"(c1v) : "v"(c1w) : "memory");
      GW(0, 0); GW(0, 1);
      WAITV(13); RBAR();
      GW(0, 2); GW(0, 3); GW(1, 0); GW(1, 1); GW(1, 2); GW(1, 3);
      WAITV(10); RBAR();
      {
        const rsrc_t rb1 = mk_rsrc(b1, F * 2);
#pragma unroll
        for (int i = 0; i < 4; i++) bb[i] = __builtin_amdgcn_raw_buffer_load_b64(rb1, (64 * ft + 4 * g) * 2 + i * 32, 0, 0);
      }
      WAITV(12);
      ST(2);
      c1_done();
      ST(3);
      LB(0, 0); LB(0, 1); LB(0, 2); LB(0, 3);
      GW(2, 0); GW(2, 1); GW(2, 2); GW(2, 3);
      LB(1, 0); LB(1, 1); LB(1, 2); LB(1, 3);
      put_stats();
      LB(2, 0); LB(2, 1); LB(2, 2); LB(2, 3);
      LB(3, 0); LB(3, 1); LB(3, 2); LB(3, 3);
      WAITV(28);
      f32x4 acc[4][4];
#pragma unroll
      for (int i = 0; i < 4; i++) {
        acc[i][0] = (f32x4){bflo(bb[i][0]), bfhi(bb[i][0]), bflo(bb[i][1]), bfhi(bb[i][1])};
#pragma unroll
        for (int j = 1; j < 4; j++) acc[i][j] = z4;
      }
      bf16x4 al[2][4], ah[2][4];
#pragma unroll
      for (int e = 0; e < 4; e++) { al[0][e] = ltr(pe[e]); ah[0][e] = ltr(pe[e] + 512); }
      SCHED();
#pragma unroll
      for (int k = 0; k < 15; k++) {
#pragma unroll
        for (int m = 0; m < 16; m++) {
          const int i = m & 3, j = m >> 2;
          mfma_k(k == 0, acc[i][j], cat8(al[k & 1][i], ah[k & 1][i]), as_bf(fb[k][j]));
          SCHED();
          if (m == 5) {
            if (k == 0) WAITV(26); else if (k == 1) WAITV(22); else if (k < 12) WAITV(11); else if (k == 12) WAITV(10); else if (k == 13) WAITV(5); else WAITV(0);
          }
          if (m >= 6 && m < 14) {
            const int e = (m - 6) >> 1;
            if (m & 1) ah[(k + 1) & 1][e] = ltr(pe[e] + ((k + 1) % 3) * 16384 + 512); else al[(k + 1) & 1][e] = ltr(pe[e] + ((k + 1) % 3) * 16384);
          }
          if ((m == 4 || m == 6 || m == 9 || m == 13) && k + 3 < 16) GW(k + 3, m == 4 ? 0 : m == 6 ? 1 : m == 9 ? 2 : 3);
          if (i == 3 && k + 4 < 16) LB(k + 4, j);
          SCHED();
        }
        if (k == 3) ST(8);
        if (k == 7) ST(9);
        if (k == 11) ST(10);
        if (k == ISSK) sy_put<Y_ISS>(a1, 1);
      }
#pragma unroll
      for (int jj = 1; jj <= 4; jj++) {
#pragma unroll
        for (int i = 0; i < 4; i++) {
          acc[i][jj & 3] = mfma32(cat8(al[1][i], ah[1][i]), as_bf(fb[15][jj & 3]), acc[i][jj & 3]); SCHED();
          if (jj >= 2) *(LAS f32x4*)(LP(lb + lane * 16) + (jj - 2) * 16384 + i * 1024) = acc[i][jj - 1];
          SCHED();
        }
      }
      __builtin_amdgcn_s_setprio(3);
      ST(4);
      const lp pin[3] = {LP(L0 + G1STG + ((w - 1) & 3) * 4096 + lane * 16), LP(L0 + G1STG + 16384 + ((w - 2) & 3) * 4096 + lane * 16), LP(L0 + G1STG + 32768 + ((w - 3) & 3) * 4096 + lane * 16)};
      BAR();
      ST(5);
      f32x4 pt[4][3];
#pragma unroll
      for (int i = 0; i < 4; i++)
#pragma unroll
        for (int j = 0; j < 3; j++) pt[i][j] = lf4(pin[j] + i * 1024);
      SCHED();
      u32x2 gp[4];
#pragma unroll
      for (int i = 0; i < 4; i++) {
        const f32x4 u = (acc[i][0] + pt[i][0]) + (pt[i][1] + pt[i][2]);
#pragma unroll
        for (int hf = 0; hf < 2; hf++) {
          constexpr float K1 = -2.f * 0.7978845608028654f * 1.4426950408889634f, K2 = K1 * 0.044715f;
          const f32x2 t = hf ? hi2(u) : lo2(u);
          const f32x2 z = t * fma2(t * t, bc2(K2), bc2(K1));
          f32x2 e = {__builtin_amdgcn_exp2f(z[0]), __builtin_amdgcn_exp2f(z[1])};
          e += bc2(1.f);
          const f32x2 rc = {__builtin_amdgcn_rcpf(e[0]), __builtin_amdgcn_rcpf(e[1])};
          gp[i][hf] = pkv(t * rc);
        }
      }
      const rsrc_t rgg = mk_rsrc(ws + WS_G, G_BYTES);
#pragma unroll
      for (int i = 0; i < 4; i += 2) {
        const auto r0 = __builtin_amdgcn_permlane16_swap(gp[i][0], gp[i + 1][0], false, false);
        const auto r1v = __builtin_amdgcn_permlane16_swap(gp[i][1], gp[i + 1][1], false, false);
        const u32x4 v = {r0[0], r1v[0], r0[1], r1v[1]};
        if (i == 0 || ft < 21) bstore1(v, rgg, (2 * (g & 1) + (g >> 1)) * 256 + c * 16, ((4 * dt + w) * 43 + 2 * ft + (i >> 1)) * 1024);
      }
      ST(6);
      WAITV(0);
      DRAIN();
      {
        const int sl0 = (2 * ft) / 11, sl1 = (2 * ft + 1) / 11;
        char* const c3a = ws + WS_C3 + (((lane & 31) * 8 + dt) * 4 + (lane < 32 ? sl0 : sl1)) * 128;
        RBAR();
        if ((lane >> 4) == w && (lane < 32 || sl1 != sl0)) gadd(c3a);
      }
      ST(7);
    } else {
      if (gi >= 24) {
        unsigned pv[8];
        sy_wait<Y_SIG>(a4, 1);
        {
          const rsrc_t r1 = mk_rsrc(w1, S * F * 2);
          const unsigned c0 = ((22 * sq) >> 2) * 128;
#pragma unroll
          for (int n = 0; n < 8; n++) {
            const unsigned row = w * 512 + (2 * (gi - 24) + 16 * (n >> 1) + ((n & 1) ^ hh)) * 8 + r8;
            pv[n] = __builtin_amdgcn_raw_buffer_load_b32(r1, pc < 6 + (r8 & 1) ? ((row * 2752 + c0) & ~127u) + pc * 128 : OOB, 0, 0);
            if (n >= PACE0 && n < 7) WAITV(0);
          }
        }
        const int so = sq * 4 + ((gi - 24) >> 1), dq = hh * 2 + (gi & 1);
        const u32x4 swo = mk_srd(wo, D * D * 2);
        const unsigned vwo = (8 * w + r8) * 1024 + dq * 256 + vsw;
#pragma unroll 1
        for (int n = 0; n < 16; n++) {
          glds16(swo, vwo, n * 32768, L0 + (n * 4 + w) * 1024);
          glds16(swo, vwo, n * 32768 + 128, L0 + 65536 + (n * 4 + w) * 1024);
        }
        const rsrc_t ro = mk_rsrc(ws + WS_O, O_BYTES), rb = mk_rsrc(ws + WS_BASE, S * D * 2);
        const int srow = 128 * so + 32 * w + c;
        const float bias0 = bflo(b2[srow]), bias1 = bflo(b2[srow + 16]);
        WAITV(0);
        asm volatile("" ::"v"(pv[0]), "v"(pv[1]), "v"(pv[2]), "v"(pv[3]), "v"(pv[4]), "v"(pv[5]), "v"(pv[6]), "v"(pv[7]));
        sy_put<Y_WO>(a1, 1);
        if (OK2K < 0) sy_put<Y_OK2>(a1, 1);
        ST(8);
        gwait<4>(ws + WS_C2 + ((so * 4 + w) * 4 + dq) * 128, lane == 0, ~0ull, tag + 8);
        ST(3);
        u32x4 of[16][2];
        const unsigned vo = lane * 16 + (8 * so + 2 * w) * 16384;
        constexpr int OD = 8;
#pragma unroll
        for (int k = 0; k < OD; k++) { of[k][0] = bload1(ro, vo, k * 1024); of[k][1] = bload1(ro, vo, 16384 + k * 1024); }
        sy_wait<Y_WO>(a4, 1);
        const unsigned ar = L0 + (8 * g + (c >> 2)) * 128 + ((((c >> 3) | ((g & 1) << 1))) << 5) + (c & 3) * 8;
        lp pe[8];
#pragma unroll
        for (int e = 0; e < 8; e++) pe[e] = LP((ar ^ ((e & 3) << 5)) + (e >> 2) * 65536);
        f32x4 acc[8][2];
#pragma unroll
        for (int i = 0; i < 8; i++) { acc[i][0] = (f32x4){bias0, bias0, bias0, bias0}; acc[i][1] = (f32x4){bias1, bias1, bias1, bias1}; }
        bf16x4 tl[2][8], th[2][8];
#pragma unroll
        for (int e = 0; e < 8; e++) { tl[0][e] = ltr(pe[e]); th[0][e] = ltr(pe[e] + 512); }
        SCHED();
#pragma unroll
        for (int k = 0; k < 16; k++) {
#pragma unroll
          for (int e = 0; e < 8; e++) {
            mfma_k(k == 0, acc[e][0], cat8(tl[k & 1][e], th[k & 1][e]), as_bf(of[k][0])); SCHED();
            if (k < 15) tl[(k + 1) & 1][e] = ltr(pe[e] + (k + 1) * 4096);
            SCHED();
            mfma_k(k == 0, acc[e][1], cat8(tl[k & 1][e], th[k & 1][e]), as_bf(of[k][1])); SCHED();
            if (k < 15) th[(k + 1) & 1][e] = ltr(pe[e] + (k + 1) * 4096 + 512);
            SCHED();
          }
          if (k + OD < 16) { of[k + OD][0] = bload1(ro, vo, (k + OD) * 1024); of[k + OD][1] = bload1(ro, vo, 16384 + (k + OD) * 1024); }
          if (k == OK2K) sy_put<Y_OK2>(a1, 1);
          if (k == 14) { FENCE(); sy_put<Y_ISS>(a1, 3); __builtin_amdgcn_s_setprio(3); }
        }
        MFMA_DONE();
        SCHED();
        ST(4);
#pragma unroll
        for (int j = 0; j < 2; j++)
#pragma unroll
          for (int i = 0; i < 4; i++) {
            const u32x4 o = {pkv(lo2(acc[2 * i][j])), pkv(hi2(acc[2 * i][j])), pkv(lo2(acc[2 * i + 1][j])), pkv(hi2(acc[2 * i + 1][j]))};
            bstore1(o, rb, lane * 16, ((8 * so + 2 * w + j) * 16 + 4 * dq + i) * 1024);
          }
        ST(6);
        WAITV(0);
        if (last_of(L0 + SY + Y_N3, 4, lane) && lane < 4) gadd(ws + WS_C4 + ((2 * so + (lane >> 1)) * 8 + 2 * dq + (lane & 1)) * 128);
        ST(7);
      } else {
        const rsrc_t rw2 = mk_rsrc(w2, F * S * 2);
        unsigned pv[6];
#pragma unroll
        for (int n = 0; n < 6; n++) pv[n] = __builtin_amdgcn_raw_buffer_load_b32(rw2, ((44 * w + 2 * (10 + gi - 22 + 2 * n) + hh) * 8 + r8) * 4096 + sq * 1024 + pc * 128, 0, 0);
        asm volatile("" ::"v"(pv[0]), "v"(pv[1]), "v"(pv[2]), "v"(pv[3]), "v"(pv[4]), "v"(pv[5]));
        sy_put<Y_ISS>(a1, 3);
        __builtin_amdgcn_s_sleep(64);
      }
      DRAIN();
    }
    asm volatile("" ::"v"(pfc));
    if (w == 1) {
      gwait<4>(ws + WS_C4 + (st * 8 + ct) * 128, lane == 0, ~0ull, tag + 1);
      sy_put<Y_OK4>(a4, 1);
      if (gi == 22 || gi == 23) { sy_wait<Y_HA>(a4, 1); sy_wait<Y_ISS>(a4, 3); if (lane == 0) gadd(ws + WS_C5); }
    }
    if (w == 2 && blockIdx.x == 0) {
      gwait<4>(ws + (lane < 32 ? WS_C3 + lane * 128 : WS_C5), lane <= 32, ~0ull, tag + (lane < 32 ? 6 : 16));
      if (lane == 0) __hip_atomic_store((GAS u64*)(ws + WS_EPOCH), tag >> 16, __ATOMIC_RELAXED, __HIP_MEMORY_SCOPE_AGENT);
    }
    BAR();
  }
  STFLUSH();
}

extern "C" void kernel_launch(void* const* d_in, const int* in_sizes, int n_in, void* d_out, int out_size, void* d_ws, size_t ws_size, hipStream_t stream) {
  (void)hipFuncSetAttribute((const void*)fused, hipFuncAttributeMaxDynamicSharedMemorySize, LDS_BYTES);
  fused<<<256, 512, LDS_BYTES, stream>>>((const u16*)d_in[0], (const u16*)d_in[1], (const u16*)d_in[2], (const u16*)d_in[4], (const u16*)d_in[5],
                                         (const u16*)d_in[6], (const u16*)d_in[7], (char*)d_ws, (u16*)d_out);
}
```

```cpp
#define PROBE_W 0
#define PROBE_I 15
#define PROBE_C 0
#include <hip/hip_runtime.h>
#include <stdint.h>

#define PACE0 2
#define ISSK 12
#define CODE_LEN 35840

typedef unsigned short u16;
typedef unsigned long long u64;
typedef short s16x4 __attribute__((ext_vector_type(4)));
typedef __bf16 bf16x2 __attribute__((ext_vector_type(2)));
typedef __bf16 bf16x4 __attribute__((ext_vector_type(4)));
typedef __bf16 bf16x8 __attribute__((ext_vector_type(8)));
typedef float f32x2 __attribute__((ext_vector_type(2)));
typedef float f32x4 __attribute__((ext_vector_type(4)));
typedef unsigned u32x2 __attribute__((ext_vector_type(2)));
typedef unsigned u32x4 __attribute__((ext_vector_type(4)));
#define LAS __attribute__((address_space(3)))
#define GAS __attribute__((address_space(1)))
#define DI __device__ __forceinline__

constexpr int S = 2048, D = 512, F = 1376;
constexpr float EPS = 1e-5f;
constexpr unsigned HT_BYTES = 32u * 64u * 1024u;
constexpr unsigned G_BYTES = 32u * 43u * 1024u;
constexpr unsigned O_BYTES = 128u * 16u * 1024u;
constexpr size_t WS_EPOCH = 0;
constexpr size_t WS_C1 = 4096;
constexpr size_t WS_C2 = WS_C1 + 128 * 128;
constexpr size_t WS_C3 = WS_C2 + 256 * 128;
constexpr size_t WS_C4 = WS_C3 + 1024 * 128;
constexpr size_t WS_C5 = WS_C4 + 256 * 128;
constexpr size_t WS_STAT = WS_C5 + 1024;
constexpr size_t WS_HT = WS_STAT + 16384;
constexpr size_t WS_G = WS_HT + HT_BYTES;
constexpr size_t WS_O = WS_G + G_BYTES;
constexpr size_t WS_BASE = WS_O + O_BYTES;
constexpr int SC1 = 16;
constexpr unsigned OOB = 0x80000000u;

typedef __amdgpu_buffer_rsrc_t rsrc_t;
DI rsrc_t mk_rsrc(const void* p, unsigned bytes) { return __builtin_amdgcn_make_buffer_rsrc((void*)p, 0, bytes, 0x00020000); }
DI u32x4 mk_srd(const void* p, unsigned bytes) {
  const uint64_t a = (uint64_t)p;
  u32x4 r = {(unsigned)a, (unsigned)(a >> 32) & 0xffffu, bytes, 0x00020000u};
  return r;
}
DI void glds16(u32x4 srd, unsigned voff, unsigned soff, unsigned lds) {
  asm volatile("s_mov_b32 m0, %3\n\ts_nop 0\n\tbuffer_load_dwordx4 %0, %1, %2 offen lds" ::"v"(voff), "s"(srd), "s"(soff), "s"(lds) : "memory");
}
DI void glds16x3(u32x4 srd, unsigned voff, unsigned soff, unsigned lds) {
  asm volatile("s_mov_b32 m0, %3\n\ts_nop 0\n\tbuffer_load_dwordx4 %0, %1, %2 offen lds\n\tbuffer_load_dwordx4 %0, %1, %2 offen offset:1024 lds\n\t"
               "buffer_load_dwordx4 %0, %1, %2 offen offset:2048 lds" ::"v"(voff), "s"(srd), "s"(soff), "s"(lds) : "memory");
}
#define WAITV(n) asm volatile("s_waitcnt vmcnt(" #n ")" ::: "memory")
#define FENCE() asm volatile("" ::: "memory")
#define SCHED() __builtin_amdgcn_sched_barrier(0)
#define DRAIN() __builtin_amdgcn_s_waitcnt(0x0F70)
#define RBAR() do { FENCE(); __builtin_amdgcn_s_barrier(); FENCE(); } while (0)
#define BAR() do { asm volatile("s_waitcnt lgkmcnt(0)" ::: "memory"); __builtin_amdgcn_s_barrier(); FENCE(); } while (0)

typedef LAS char* lp;
DI lp LP(unsigned a) { return (lp)(uintptr_t)a; }
DI bf16x8 cat8(bf16x4 a, bf16x4 b) { return __builtin_shufflevector(a, b, 0, 1, 2, 3, 4, 5, 6, 7); }
DI bf16x8 l128(lp p) { return *(const LAS bf16x8*)p; }
DI f32x4 lf4(lp p) { return *(const LAS f32x4*)p; }
DI bf16x4 ltr(lp p) { return __builtin_amdgcn_ds_read_tr16_b64_v4bf16((LAS bf16x4*)p); }
DI bf16x8 ltr2(lp p, int d) { return cat8(ltr(p), ltr(p + d)); }
DI f32x4 mfma32(bf16x8 a, bf16x8 b, f32x4 c) { return __builtin_amdgcn_mfma_f32_16x16x32_bf16(a, b, c, 0, 0, 0); }
DI void mfma_ip(f32x4& c, bf16x8 a, bf16x8 b) { asm volatile("v_mfma_f32_16x16x32_bf16 %0, %1, %2, %0" : "+v"(c) : "v"(a), "v"(b)); }
DI void mfma_k(bool first, f32x4& c, bf16x8 a, bf16x8 b) { if (first) c = mfma32(a, b, c); else mfma_ip(c, a, b); }
#define MFMA_DONE() asm volatile("s_nop 7\n\ts_nop 3" ::: "memory")
DI f32x4 mfma16(u32x2 a, u32x2 b, f32x4 c) { return __builtin_amdgcn_mfma_f32_16x16x16bf16_1k(__builtin_bit_cast(s16x4, a), __builtin_bit_cast(s16x4, b), c, 0, 0, 0); }
DI unsigned pkv(f32x2 f) { return __builtin_bit_cast(unsigned, __builtin_convertvector(f, bf16x2)); }
DI unsigned pk2(float a, float b) { f32x2 f = {a, b}; return pkv(f); }
DI f32x2 lo2(f32x4 v) { return __builtin_shufflevector(v, v, 0, 1); }
DI f32x2 hi2(f32x4 v) { return __builtin_shufflevector(v, v, 2, 3); }
DI f32x2 fma2(f32x2 a, f32x2 b, f32x2 c) { return __builtin_elementwise_fma(a, b, c); }
DI f32x2 bc2(float a) { f32x2 r = {a, a}; return r; }
DI float bflo(unsigned u) { return __uint_as_float(u << 16); }
DI float bfhi(unsigned u) { return __uint_as_float(u & 0xffff0000u); }
template <int CTRL> DI float dpp(float v) { return __uint_as_float(__builtin_amdgcn_update_dpp(0u, __float_as_uint(v), CTRL, 0xf, 0xf, true)); }
DI float sum16(float v) { v += dpp<0xB1>(v); v += dpp<0x4E>(v); v += dpp<0x141>(v); v += dpp<0x140>(v); return v; }
DI f32x2 swap16(float a, float b) { auto r = __builtin_amdgcn_permlane16_swap(__float_as_uint(a), __float_as_uint(b), false, false); f32x2 o = {__uint_as_float(r[0]), __uint_as_float(r[1])}; return o; }
DI f32x2 swap32(float a, float b) { auto r = __builtin_amdgcn_permlane32_swap(__float_as_uint(a), __float_as_uint(b), false, false); f32x2 o = {__uint_as_float(r[0]), __uint_as_float(r[1])}; return o; }
DI float sel(unsigned m, float a, float b) { return __uint_as_float((__float_as_uint(a) & m) | (__float_as_uint(b) & ~m)); }
DI u32x4 bload(rsrc_t r, unsigned v, unsigned s) { return __builtin_amdgcn_raw_buffer_load_b128(r, v, s, 0); }
DI u32x4 bload1(rsrc_t r, unsigned v, unsigned s) { return __builtin_amdgcn_raw_buffer_load_b128(r, v, s, SC1); }
DI void bstore1(u32x4 d, rsrc_t r, unsigned v, unsigned s) { __builtin_amdgcn_raw_buffer_store_b128(d, r, v + s, 0, SC1); }
DI bf16x8 as_bf(u32x4 v) { return __builtin_bit_cast(bf16x8, v); }
DI u64 uni64(u64 v) { return ((u64)(unsigned)__builtin_amdgcn_readfirstlane((unsigned)(v >> 32)) << 32) | (unsigned)__builtin_amdgcn_readfirstlane((unsigned)v); }

constexpr unsigned LDS_BYTES = 163840;
constexpr unsigned QXB = 14336;
constexpr unsigned QST = QXB + 24576;
constexpr unsigned QSTAT = 4 * QST;
constexpr unsigned LNROW = 131072;
constexpr unsigned G1STG = 0;
constexpr unsigned P2STG = 49152;
constexpr unsigned SY = LDS_BYTES - 512;
enum { Y_FULL = 0, Y_FREE = 16, Y_HA = 32, Y_HB = 48, Y_ST = 64, Y_QS = 80, Y_ISS = 96, Y_E2 = 112, Y_RA = 128, Y_OK2 = 144, Y_OK3 = 160, Y_OK4 = 176, Y_WO = 192,
       Y_N1 = 208, Y_N2 = 212, Y_N3 = 216, Y_TAG = 224, Y_SIG = 240 };
static_assert(QSTAT + 896 <= SY && P2STG + 98304 <= SY, "lds");
constexpr unsigned SPIN_GLB = 1u << 17;

template <int OFF> DI void sy_put(unsigned a, unsigned v) { asm volatile("ds_write_b32 %0, %1 offset:%2" ::"v"(a), "v"(v), "i"(OFF) : "memory"); }
template <int OFF> DI void sy_wait(unsigned a4, unsigned target) {
  unsigned t, n;
  asm volatile("s_mov_b32 %1, 0x200000\n"
               "1:\n\tds_read_b32 %0, %2 offset:%4\n\ts_waitcnt lgkmcnt(0)\n\tv_cmp_gt_u32 vcc, %3, %0\n\ts_cbranch_vccz 2f\n\t"
               "s_sleep 1\n\ts_sub_u32 %1, %1, 1\n\ts_cmp_lg_u32 %1, 0\n\ts_cbranch_scc1 1b\n"
               "2:" : "=&v"(t), "=&s"(n) : "v"(a4), "s"(target), "i"(OFF) : "vcc", "scc", "memory");
}
DI bool last_of(unsigned cnt, unsigned n, int lane) {
  unsigned old = 0;
  if (lane == 0) asm volatile("ds_add_rtn_u32 %0, %1, %2\n\ts_waitcnt lgkmcnt(0)" : "=&v"(old) : "v"(cnt), "v"(1u) : "memory");
  return (unsigned)__builtin_amdgcn_readfirstlane(old) == n - 1;
}
DI void gadd(char* p) { (void)__hip_atomic_fetch_add((GAS u64*)p, 1ull, __ATOMIC_RELAXED, __HIP_MEMORY_SCOPE_AGENT); }
DI u64 gpeek(const char* p) {
  u64 z = 0;
  asm volatile("" : "+v"(z));
  return __hip_atomic_fetch_add((GAS u64*)p, z, __ATOMIC_RELAXED, __HIP_MEMORY_SCOPE_AGENT);
}
template <int SL> DI void gwait(const char* p, bool on, u64 mask, u64 want) {
  FENCE();
  bool ok = !on;
#pragma unroll 1
  for (unsigned n = 0; n < SPIN_GLB; n++) {
    if (!ok) ok = (gpeek(p) & mask) == want;
    if (__all(ok)) break;
    __builtin_amdgcn_s_sleep(SL);
  }
  FENCE();
}

#define ST(i) do { if ((i) == 0 || (i) == PROBE_I) { SCHED(); u64 t_; asm volatile("s_memrealtime %0\n\ts_waitcnt lgkmcnt(0)" : "=s"(t_)::"memory"); stv[(i) != 0] = t_; SCHED(); } } while (0)
#define STDECL u64 stv[2] = {0, 0};
#define STFLUSH() do { if (W == PROBE_W && (gi >= 22) == PROBE_C) { const u64 dt_ = stv[1] > stv[0] ? stv[1] - stv[0] : 0; const u64 until_ = stv[0] + 10000 + 20 * dt_; while (__builtin_amdgcn_s_memrealtime() < until_) __builtin_amdgcn_s_sleep(2); } } while (0)

__global__ __launch_bounds__(512, 2) void fused(const u16* __restrict__ x, const u16* __restrict__ wqkv, const u16* __restrict__ wo, const u16* __restrict__ w1,
                                                const u16* __restrict__ b1, const u16* __restrict__ w2, const u16* __restrict__ b2, char* __restrict__ ws,
                                                u16* __restrict__ out) {
  extern __shared__ __attribute__((aligned(16))) char smem[];
  uint64_t pc0; asm volatile("s_getpc_b64 %0" : "=s"(pc0));
  const unsigned L0 = (unsigned)(uintptr_t)(LAS char*)smem;
  const int tid = threadIdx.x, lane = tid & 63;
  const int W = __builtin_amdgcn_readfirstlane(tid >> 6), w = W & 3;
  const int xcd = blockIdx.x & 7, idx = blockIdx.x >> 3;
  const int sq = xcd >> 1, hh = xcd & 1;
  const int st = sq * 8 + (idx >> 2), ct = hh * 4 + (idx & 3);
  const int gi = idx;
  const int rt = (22 * sq + gi) >> 2, hg = hh * 4 + ((22 * sq + gi) & 3);
  const int c = lane & 15, g = lane >> 4, r8 = lane >> 3, pc = lane & 7;
  const f32x4 z4 = {0.f, 0.f, 0.f, 0.f};
  STDECL ST(0);
  const unsigned pfc = __builtin_amdgcn_raw_buffer_load_b32(mk_rsrc((const void*)(pc0 & ~255ull), CODE_LEN), ((idx & 7) * 8 + W) * 1024 + (lane & 7) * 128, 0, 0);
  {
    const unsigned on = (W >> 1) & 1;
    *(LAS unsigned*)LP(L0 + (on ? 0 : SY) + ((tid >> 5) & 3) * (on ? QST : 128) + (tid & 31) * 4) = on ? 0x3f803f80u : 0u;
  }
  const unsigned a4 = L0 + SY + (lane & 3) * 4, a1 = L0 + SY + w * 4;
  asm volatile("" ::"s"(x), "s"(wqkv), "s"(wo), "s"(w1), "s"(b1), "s"(w2), "s"(b2), "s"(ws), "s"(out));
  const u32x4 sx = mk_srd(x, S * D * 2), sw = mk_srd(wqkv, D * 3 * D * 2);
  const unsigned vx0 = (unsigned)((96 * rt - 16 + 8 * w + r8) * 1024 + ((pc ^ (((w & 1) << 2) | (r8 >> 1))) << 4));
  const unsigned vsw = (((pc >> 1) ^ (((r8 >> 1) & 1) | ((w & 1) << 1))) << 5) | ((pc & 1) << 4);
  const unsigned vwq = (8 * w + r8) * 3072 + hg * 128 + vsw;
  auto head = [&](int s) { glds16x3(sw, vwq, s * 64 * 3072, L0 + (s & 3) * QST + QXB + w * 3072); };
  auto rest = [&](int s) {
    const unsigned b = L0 + (s & 3) * QST + w * 1024;
    if (lane >= 8 || w != 0) glds16(sx, vx0, s * 128, b);
    glds16(sx, vx0 + 32768u, s * 128, b + 4096);
    glds16(sx, vx0 + 65536u, s * 128, b + 8192);
    if (w < 2) glds16(sx, vx0 + 98304u, s * 128, b + 12288);
    glds16x3(sw, vwq, s * 64 * 3072 + 32 * 3072, b + QXB + 12288 + w * 2048);
  };
  const int un = sq * 16 + hh * 8 + gi - 24;
  u32x4 xr[4];
  ST(11);
  if (gi >= 24) {
    asm volatile("buffer_load_dwordx4 %0, %4, %5, %6 offen\n\tbuffer_load_dwordx4 %1, %4, %5, %6 offen offset:1024\n\t"
                 "buffer_load_dwordx4 %2, %4, %5, %6 offen offset:2048\n\tbuffer_load_dwordx4 %3, %4, %5, %6 offen offset:3072"
                 : "=&v"(xr[0]), "=&v"(xr[1]), "=&v"(xr[2]), "=&v"(xr[3]) : "v"(lane * 16), "s"(sx), "s"((un * 32 + 4 * W) * 1024) : "memory");
  } else {
    asm volatile("" : "=v"(xr[0]), "=v"(xr[1]), "=v"(xr[2]), "=v"(xr[3]));
    if (gi < 22 && W >= 4) { head(0); rest(0); }
  }
  BAR();
  u64 tag = 0;

  if (gi >= 24) {
    const u64 ep = __hip_atomic_load((const GAS u64*)(ws + WS_EPOCH), __ATOMIC_RELAXED, __HIP_MEMORY_SCOPE_AGENT);
    const unsigned pfw = *(const GAS unsigned*)((lane & 31) < 24 ? (const char*)wqkv + (un * 8 + W) * 3072 + (lane & 31) * 128 : (const char*)wo + (un * 8 + W) * 1024 + (lane & 7) * 128);
    WAITV(2);
    asm volatile("" : "+v"(xr[0]), "+v"(xr[1]), "+v"(xr[2]), "+v"(xr[3]));
    f32x2 v[4][4];
    float p[4], s1[4], s2[4];
    auto red4 = [&](const float (&a)[4]) {
      f32x2 t = swap16(a[0], a[1]), u = swap16(a[2], a[3]);
      t = swap32(t[0] + t[1], u[0] + u[1]);
      return sum16(t[0] + t[1]);
    };
#pragma unroll
    for (int rr = 0; rr < 4; rr++) {
      p[rr] = bflo(__builtin_amdgcn_readfirstlane(xr[rr][0]));
      f32x2 a = {0.f, 0.f}, b = {0.f, 0.f};
#pragma unroll
      for (int j = 0; j < 4; j++) { const f32x2 t = {bflo(xr[rr][j]), bfhi(xr[rr][j])}; v[rr][j] = t - bc2(p[rr]); a += v[rr][j]; b = fma2(v[rr][j], v[rr][j], b); }
      s1[rr] = a[0] + a[1]; s2[rr] = b[0] + b[1];
    }
    if (W == 0) {
      const size_t o = lane < 2 ? WS_C1 + (un * 2 + lane) * 128 : WS_C2 + (un * 24 + lane - 2) * 128;
      u64 e = ep;
      asm volatile("" : "+v"(e));
      if (lane < (un == 63 ? 27 : 26)) __hip_atomic_store((GAS u64*)(ws + o), (e + 1) << 16, __ATOMIC_RELAXED, __HIP_MEMORY_SCOPE_AGENT);
    }
    {
      const float m = red4(s1) * (1.f / D), var = fmaxf(red4(s2) * (1.f / D) - m * m, 0.f);
      const float r = __builtin_amdgcn_rsqf(var + EPS);
      const float a = r * __builtin_amdgcn_rsqf(var * r * r + EPS), ma = -m * a;
#pragma unroll
      for (int rr = 0; rr < 4; rr++) {
        const f32x2 av = bc2(__uint_as_float(__builtin_amdgcn_readlane(__float_as_uint(a), 16 * rr))), cv = bc2(__uint_as_float(__builtin_amdgcn_readlane(__float_as_uint(ma), 16 * rr)));
        u32x4 h;
#pragma unroll
        for (int j = 0; j < 4; j++) h[j] = pkv(fma2(v[rr][j], av, cv));
        *(LAS u32x4*)LP(L0 + (4 * W + rr) * 1088 + ((W >> 1) & 1) * 32 + lane * 16) = h;
      }
      const float pg = g == 0 ? p[0] : g == 1 ? p[1] : g == 2 ? p[2] : p[3];
      *(LAS float*)LP(L0 + QSTAT + (W >> 1) * 64 + (W & 1) * 16 + g * 4 + (lane & 1) * 32) = (lane & 1) ? (m + pg) * r : r;
    }
    ST(12);
    char* const c1p = ws + WS_C1 + (sq * 32 + (lane & 31)) * 128;
    u64 chk = 0;
    if (W == 1 && lane < 32) asm volatile("global_atomic_add_x2 %0, %1, %0, off sc0" : "+v"(chk) : "v"(c1p) : "memory");
    BAR();
    {
      bf16x8 tv[4];
#pragma unroll
      for (int n = 0; n < 4; n++) tv[n] = ltr2(LP(L0 + (8 * g + (c >> 2)) * 1088 + (g & 1) * 32 + W * 128 + (c & 3) * 8) + n * 32, 4 * 1088);
      const u32x4 sv = *(const LAS u32x4*)LP(L0 + QSTAT + c * 16);
      const rsrc_t rht = mk_rsrc(ws + WS_HT, HT_BYTES);
#pragma unroll
      for (int n = 0; n < 4; n++) bstore1(__builtin_bit_cast(u32x4, tv[n]), rht, lane * 16, ((4 * W + n) * 64 + un) * 1024);
      if (W == 2) bstore1(sv, mk_rsrc(ws + WS_STAT, S * 8), lane < 16 ? un * 256 + lane * 16 : OOB, 0);
    }
    ST(1);
    WAITV(0);
    DRAIN();
    u64 e = ep;
    asm volatile("" : "+v"(e) : "v"(pfw));
    tag = (uni64(e) + 1) << 16;
    if (W == 1) { asm volatile("" : "+v"(chk)); if (__any(lane < 32 && (chk >> 16) != (tag >> 16))) gwait<2>(c1p, lane < 32, ~0xffffull, tag); }
    if (last_of(L0 + SY + Y_N1, 8, lane)) { if (lane < 32) gadd(c1p); sy_put<Y_SIG>(a4, 1); }
    ST(2);
  } else if (gi >= 22) {
    unsigned p3[3] = {0, 0, 0};
    if (W >= 4) {
#pragma unroll
      for (int n = 0; n < 3; n++) p3[n] = __builtin_amdgcn_raw_buffer_load_b32(mk_rsrc(wqkv, D * 3 * D * 2), hh ? OOB : ((n * 8 + (gi - 22) * 4 + w) * 64 + lane) * 1024 + 384, 0, 0);
    } else if (W > 0) {
#pragma unroll
      for (int n = 0; n < 2; n++) p3[n] = __builtin_amdgcn_raw_buffer_load_b32(mk_rsrc(x, S * D * 2), (unsigned)((((22 * sq) >> 2) * 96 - 16 + (n * 6 + (gi - 22) * 3 + W - 1) * 52 + min(lane, 51)) * 1024 + 384), 0, 0);
    }
    tag = (uni64(__hip_atomic_load((const GAS u64*)(ws + WS_EPOCH), __ATOMIC_RELAXED, __HIP_MEMORY_SCOPE_AGENT)) + 1) << 16;
    if (W < 4) sy_put<Y_HA>(a1, 1);
    asm volatile("" ::"v"(p3[0]), "v"(p3[1]), "v"(p3[2]));
  }

  if (W < 4) {
    if (gi < 22) {
      f32x4 aq[7], ak[7], av[7];
#pragma unroll
      for (int b = 0; b < 7; b++) { ak[b] = z4; av[b] = z4; aq[b] = z4; }
      const unsigned ax = L0 + c * 128 + ((g ^ (c >> 1)) << 4);
      const unsigned aw = L0 + QXB + g * 3072 + (c >> 2) * 128 + ((w ^ ((c >> 3) | ((g & 1) << 1))) << 5) + (c & 3) * 8;
      bf16x8 xf[2][7];
      bf16x4 wl[2][3], wh[2][3];
      const lp pxa[2][2] = {{LP(ax), LP(ax ^ 64)}, {LP(ax + 2 * QST), LP((ax ^ 64) + 2 * QST)}}, pwa[2] = {LP(aw), LP(aw + 2 * QST)};
      auto RD = [&](int n, int i, int slot, int kk) {
        const lp px = pxa[slot >> 1][kk] + (slot & 1) * QST, pw = pwa[slot >> 1] + (slot & 1) * QST + kk * 12288;
        if (i == 0) wl[n][1] = ltr(pw + 1024);
        if (i == 1) wh[n][1] = ltr(pw + 1024 + 512);
        if (i == 2) xf[n][6] = l128(px + 12288);
        if (i == 3) wl[n][2] = ltr(pw + 2048);
        if (i == 4) wh[n][2] = ltr(pw + 2048 + 512);
        if (i == 5) wl[n][0] = ltr(pw);
        if (i == 6) wh[n][0] = ltr(pw + 512);
        if (i >= 7) xf[n][12 - i] = l128(px + (12 - i) * 2048);
      };
      auto MM = [&](int cur, int m) {
        const int t = 6 - m / 3;
        if (m % 3 == 0) mfma_ip(ak[t], cat8(wl[cur][1], wh[cur][1]), xf[cur][t]);
        if (m % 3 == 1) mfma_ip(av[t], xf[cur][t], cat8(wl[cur][2], wh[cur][2]));
        if (m % 3 == 2) mfma_ip(aq[t], cat8(wl[cur][0], wh[cur][0]), xf[cur][t]);
      };
      RBAR();
      ST(2);
#pragma unroll
      for (int i = 0; i < 13; i++) RD(0, i, 0, 0);
      SCHED();
#pragma unroll 1
      for (int s4 = 0; s4 < 8; s4 += 4) {
#pragma unroll
        for (int sl = 0; sl < 4; sl++) {
#pragma unroll
          for (int m = 0; m < 21; m++) {
            MM(0, m); SCHED();
            if (m < 13) RD(1, m, sl, 1);
            if (m == 19 && (sl < 3 || s4 == 0)) BAR();
            SCHED();
          }
#pragma unroll
          for (int m = 0; m < 21; m++) {
            MM(1, m); SCHED();
            if (m < 13) RD(0, m, (sl + 1) & 3, 0);
            SCHED();
          }
        }
      }
      MFMA_DONE();
      SCHED();
      ST(3);
      f32x2 cq[2], ck[2];
#pragma unroll
      for (int j = 0; j < 4; j++) { cq[j >> 1][j & 1] = -dpp<0x150>(aq[0][j]); ck[j >> 1][j & 1] = -dpp<0x150>(ak[0][j]); }
      f32x2 cvt = swap16(av[0][0], av[0][0]);
      cvt = swap32(cvt[0], cvt[0]);
      const f32x2 cv = bc2(-cvt[0]);
      sy_wait<Y_ST>(a4, 1);
      u32x2 qf[6], kf[7], vf[7];
      {
        float sr[7], sm[7]; f32x4 vr[7], vm[7];
        const lp pcs = LP(L0 + QSTAT + (c >> 3) * 64 + (c & 7) * 4), prs = LP(L0 + QSTAT + (g >> 1) * 64 + (g & 1) * 16);
#pragma unroll
        for (int t = 0; t < 7; t++) {
          sr[t] = *(const LAS float*)(pcs + t * 128); sm[t] = *(const LAS float*)(pcs + t * 128 + 32);
          vr[t] = lf4(prs + t * 128); vm[t] = lf4(prs + t * 128 + 32);
        }
        SCHED();
#pragma unroll
        for (int t = 0; t < 7; t++) {
          const f32x2 r = bc2(sr[t]), m = bc2(sm[t]);
          f32x2 y[6] = {m * ck[0], m * ck[1], m * cq[0], m * cq[1], lo2(vm[t]) * cv, hi2(vm[t]) * cv};
          SCHED();
          y[0] = fma2(r, lo2(ak[t]), y[0]); y[1] = fma2(r, hi2(ak[t]), y[1]); y[2] = fma2(r, lo2(aq[t]), y[2]); y[3] = fma2(r, hi2(aq[t]), y[3]);
          y[4] = fma2(lo2(vr[t]), lo2(av[t]), y[4]); y[5] = fma2(hi2(vr[t]), hi2(av[t]), y[5]);
          SCHED();
          kf[t][0] = pkv(y[0]); kf[t][1] = pkv(y[1]);
          if (t > 0) { qf[t - 1][0] = pkv(y[2]); qf[t - 1][1] = pkv(y[3]); }
          vf[t][0] = pkv(y[4]); vf[t][1] = pkv(y[5]);
        }
        SCHED();
      }
      u32x2 op[6];
      {
        unsigned om = 0u - (unsigned)(c & 1);
        asm("" : "+v"(om));
        const bool cur0 = 4 * g + (c & 1) <= c, cur1 = 4 * g + (c & 1) + 2 <= c;
        constexpr float SC = 0.25f * 1.4426950408889634f;
        f32x4 sp[6], su[6];
#pragma unroll
        for (int t = 0; t < 6; t++) { sp[t] = mfma16(kf[t], qf[t], z4); su[t] = mfma16(kf[t + 1], qf[t], z4); }
        float l0[6], l1[6], mx[8];
#pragma unroll
        for (int t = 0; t < 6; t++) {
          const float p0 = sel(om, sp[t][1], sp[t][0]), p1 = sel(om, sp[t][3], sp[t][2]);
          const float u0 = sel(om, su[t][1], su[t][0]), u1 = sel(om, su[t][3], su[t][2]);
          l0[t] = cur0 ? u0 : p0; l1[t] = cur1 ? u1 : p1;
          if (t == 0 && rt == 0) { l0[t] = cur0 ? l0[t] : -4e9f; l1[t] = cur1 ? l1[t] : -4e9f; }
          mx[t] = fmaxf(l0[t], l1[t]);
        }
        {
          const f32x2 a = swap16(mx[0], mx[1]), b = swap16(mx[2], mx[3]), a2 = swap16(mx[4], mx[5]);
          const float n2 = fmaxf(a2[0], a2[1]);
          const f32x2 d = swap32(fmaxf(a[0], a[1]), fmaxf(b[0], b[1])), d2 = swap32(n2, n2);
          const float m = fmaxf(d[0], d[1]) * -SC, m2 = fmaxf(d2[0], d2[1]) * -SC;
          const f32x2 e = swap32(m, m), f0 = swap16(e[0], e[0]), f1 = swap16(e[1], e[1]), f2 = swap16(m2, m2);
          mx[0] = f0[0]; mx[1] = f0[1]; mx[2] = f1[0]; mx[3] = f1[1]; mx[4] = f2[0]; mx[5] = f2[1];
        }
        const u32x4 ones = {0x3f803f80u, 0x3f803f80u, 0x3f803f80u, 0x3f803f80u};
        const unsigned keep = om ^ 0x0000ffffu;
        const unsigned k00 = cur0 ? 0u : keep, k01 = cur1 ? 0u : keep, k10 = cur0 ? keep : 0u, k11 = cur1 ? keep : 0u;
        f32x4 o[6], den[6];
#pragma unroll
        for (int t = 0; t < 6; t++) {
          const float e0 = __builtin_amdgcn_exp2f(__builtin_fmaf(l0[t], SC, mx[t])), e1 = __builtin_amdgcn_exp2f(__builtin_fmaf(l1[t], SC, mx[t]));
          const unsigned b0 = pk2(e0, e0), b1v = pk2(e1, e1);
          const u32x4 pu = {b0 & k00, b1v & k01, b0 & k10, b1v & k11}, vv = {vf[t][0], vf[t][1], vf[t + 1][0], vf[t + 1][1]};
          o[t] = mfma32(as_bf(vv), as_bf(pu), z4);
          den[t] = mfma32(as_bf(ones), as_bf(pu), z4);
        }
#pragma unroll
        for (int t = 0; t < 6; t++) {
          const f32x2 inv = bc2(__builtin_amdgcn_rcpf(den[t][0]));
          op[t][0] = pkv(lo2(o[t]) * inv); op[t][1] = pkv(hi2(o[t]) * inv);
        }
      }
      {
        const rsrc_t ro = mk_rsrc(ws + WS_O, O_BYTES);
#pragma unroll
        for (int t = 0; t < 6; t += 2) {
          const auto r0 = __builtin_amdgcn_permlane16_swap(op[t][0], op[t + 1][0], false, false);
          const auto r1 = __builtin_amdgcn_permlane16_swap(op[t][1], op[t + 1][1], false, false);
          const u32x4 v = {r0[0], r1[0], r0[1], r1[1]};
          if (t == 0 || rt < 21) bstore1(v, ro, (g & 1) * 16384 + ((g >> 1) * 16 + c) * 16, ((6 * rt + t) * 16 + 2 * hg + (w >> 1)) * 1024 + (w & 1) * 512);
        }
      }
      ST(13);
      sy_put<Y_QS>(a1, 1);
      WAITV(0);
      if (last_of(L0 + SY + Y_N2, 4, lane) && lane < (rt < 21 ? 12 : 4)) gadd(ws + WS_C2 + (12 * rt + lane) * 128);
      { const u32x2 tg = *(const LAS u32x2*)LP(L0 + SY + Y_TAG); tag = ((u64)(unsigned)__builtin_amdgcn_readfirstlane(tg[1]) << 32) | (unsigned)__builtin_amdgcn_readfirstlane(tg[0]); }
      ST(4);
    } else {
      const rsrc_t rw2 = mk_rsrc(w2, F * S * 2);
      if (gi >= 24) {
        sy_wait<Y_OK2>(a4, 1);
        const int m = gi - 24 + 8 * r8;
        const unsigned t = __builtin_amdgcn_raw_buffer_load_b32(rw2, m < 22 ? ((44 * w + 2 * m + (hh ^ 1)) * 8 + pc) * 4096 + sq * 1024 + 384 : OOB, 0, 0);
        asm volatile("" ::"v"(t));
      } else {
        unsigned pv[5];
#pragma unroll
        for (int n = 0; n < 5; n++) pv[n] = __builtin_amdgcn_raw_buffer_load_b32(rw2, ((44 * w + 2 * (gi - 22 + 2 * n) + hh) * 8 + r8) * 4096 + sq * 1024 + pc * 128, 0, 0);
        asm volatile("" ::"v"(pv[0]), "v"(pv[1]), "v"(pv[2]), "v"(pv[3]), "v"(pv[4]));
      }
      ST(4);
    }
    asm volatile("" ::"v"(pfc));

    const rsrc_t rgg = mk_rsrc(ws + WS_G, G_BYTES);
    const unsigned vg = lane * 16 + (4 * ct * 43 + 11 * w) * 1024;
    const u32x4 sw2 = mk_srd(w2, F * S * 2);
    const unsigned vw2 = (352 * w + r8) * 4096 + st * 128 + ((((pc >> 1) ^ ((r8 >> 1) & 1)) << 5) | ((pc & 1) << 4));
    const unsigned lw = L0 + P2STG + w * 24576;
    const unsigned ar = lw + (8 * g + (c >> 2)) * 128 + ((((c >> 3) | ((g & 1) << 1))) << 5) + (c & 3) * 8;
    u32x4 gf[11][4];
    auto GW = [&](int k, int slot) {
#pragma unroll
      for (int j = 0; j < 4; j++) glds16(sw2, (k == 10 && w == 3) ? OOB : vw2 ^ ((j & 1) << 6), (32 * k + 8 * j) * 4096, lw + slot * 4096 + j * 1024);
    };
    auto LG1 = [&](int k, int j) { gf[k][j] = bload1(rgg, (k == 10 && w == 3) ? OOB : vg, j * 43 * 1024 + k * 1024); };
    lp pr[4];
#pragma unroll
    for (int j = 0; j < 4; j++) pr[j] = LP(ar ^ (((w + j) & 3) << 5));
    bf16x8 tw[2][4];
    bf16x8 tk[4][4];
    auto TR = [&](bf16x8 (&t)[4], int slot) {
#pragma unroll
      for (int j = 0; j < 4; j++) t[j] = ltr2(pr[j] + slot * 4096, 512);
      asm volatile("" ::"v"(t[0]), "v"(t[1]), "v"(t[2]), "v"(t[3]) : "memory");
    };
    const int srow = 64 * st + 16 * w + c;
    u32x4 xq[2];
    if (gi < 22) { sy_wait<Y_QS>(a4, 1); sy_wait<Y_ISS>(a4, 1); }
    ST(5);
    asm volatile("buffer_load_dwordx4 %0, %2, %3, 0 offen\n\tbuffer_load_dwordx4 %1, %2, %3, 0 offen offset:64"
                 : "=&v"(xq[0]), "=&v"(xq[1]) : "v"((srow * D + 64 * ct + 16 * (g & 1) + 8 * (g >> 1)) * 2), "s"(sx) : "memory");
    const char* const c3p = ws + WS_C3 + ((st * 8 + ct) * 4 + w) * 128;
    u64 c3v = 0;
    const int s6 = -12 - 5 * w;
    if (gi < 22) {
      GW(7, 0); GW(8, 1);
      RBAR();
      GW(9, 2); GW(10, 3); GW(4, 4); GW(5, 5);
      WAITV(16); TR(tk[0], 0); TR(tk[1], 1);
      WAITV(8); TR(tk[2], 2); TR(tk[3], 3);
      RBAR();
      GW(0, 0); GW(1, 1); GW(2, 2);
      if (lane == 0) asm volatile("global_atomic_add_x2 %0, %1, %0, off sc0" : "+v"(c3v) : "v"(c3p) : "memory");
      GW(3, 3); GW(6, s6);
      WAITV(17); TR(tw[0], 0);
    } else {
      const int sc = 20 - 5 * w;
      auto TS = [&](bf16x8 (&t)[4], int k) {
#pragma unroll
        for (int j = 0; j < 4; j++) glds16(sw2, (k == 10 && w == 3) ? OOB : vw2 ^ ((j & 1) << 6), (32 * k + 8 * j) * 4096, lw + sc * 4096 + j * 1024);
        WAITV(0);
#pragma unroll
        for (int j = 0; j < 4; j++) t[j] = ltr2(pr[j] + sc * 4096, 512);
        asm volatile("" ::"v"(t[0]), "v"(t[1]), "v"(t[2]), "v"(t[3]) : "memory");
      };
      TS(tk[0], 7); TS(tk[1], 8); TS(tk[2], 9); TS(tk[3], 10); TS(tw[0], 0);
      sy_put<Y_QS>(a1, 1);
      ST(12);
      sy_wait<Y_QS>(a4, 1);
      sy_wait<Y_ISS>(a4, 3);
      GW(1, 1); GW(2, 2); GW(3, 3); GW(4, 4);
      if (lane == 0) asm volatile("global_atomic_add_x2 %0, %1, %0, off sc0" : "+v"(c3v) : "v"(c3p) : "memory");
      GW(5, 5); GW(6, s6);
    }
    asm volatile("" : "+v"(xq[0]), "+v"(xq[1]));
    u32x2 xv[4];
#pragma unroll
    for (int i = 0; i < 2; i++) {
      const auto r0 = __builtin_amdgcn_permlane16_swap(xq[i][0], xq[i][2], false, false), r1 = __builtin_amdgcn_permlane16_swap(xq[i][1], xq[i][3], false, false);
      xv[2 * i][0] = r0[0]; xv[2 * i][1] = r1[0]; xv[2 * i + 1][0] = r0[1]; xv[2 * i + 1][1] = r1[1];
    }
    f32x4 acc[4][4];
#pragma unroll
    for (int i = 0; i < 4; i++) {
      acc[i][0] = (f32x4){bflo(xv[i][0]), bfhi(xv[i][0]), bflo(xv[i][1]), bfhi(xv[i][1])};
#pragma unroll
      for (int j = 1; j < 4; j++) acc[i][j] = z4;
      asm volatile("" : "+v"(acc[i][0]));
    }
    ST(6);
    WAITV(8);
    asm volatile("" : "+v"(c3v));
    if (uni64(c3v) != tag + 6) gwait<0>(c3p, lane == 0, ~0ull, tag + 6);
    ST(7);
    constexpr int GD = 4;
#pragma unroll
    for (int k = 0; k < GD; k++)
#pragma unroll
      for (int j = 0; j < 4; j++) LG1(k, j);
    WAITV(16);
    SCHED();
    u32x4 bv[2];
#pragma unroll
    for (int k = 0; k < 10; k++) {
      if (k + GD == 11) {
        sy_wait<Y_OK4>(a4, 1);
        const rsrc_t rb = mk_rsrc(ws + WS_BASE, S * D * 2);
#pragma unroll
        for (int i = 0; i < 2; i++) bv[i] = bload1(rb, lane * 16, ((4 * st + w) * 16 + 2 * ct + i) * 1024);
      }
#pragma unroll
      for (int i = 0; i < 4; i++) {
#pragma unroll
        for (int j = 0; j < 4; j++) {
          mfma_k(k == 0, acc[i][j], as_bf(gf[k][i]), k < 7 ? tw[k & 1][j] : tk[k < 7 ? 0 : k - 7][j]);
          if (j == 0 && k + 1 < 7) { SCHED(); tw[(k + 1) & 1][i] = ltr2(pr[i] + (k + 1 < 6 ? k + 1 : s6) * 4096, 512); SCHED(); }
        }
        SCHED();
        if (k + GD < 11) LG1(k + GD, i);
        SCHED();
      }
    }
#pragma unroll
    for (int jj = 1; jj <= 4; jj++) {
#pragma unroll
      for (int i = 0; i < 4; i++) {
        acc[i][jj & 3] = mfma32(as_bf(gf[10][i]), tk[3][jj & 3], acc[i][jj & 3]); SCHED();
        if (jj >= 2) *(LAS f32x4*)(LP(lw + lane * 16) + ((jj - 2) * 4 + i) * 1024) = acc[i][jj - 1];
        SCHED();
      }
    }
    ST(8);
    BAR();
    ST(9);
    f32x4 u[4];
#pragma unroll
    for (int i = 0; i < 4; i++) {
      u[i] = acc[i][0] + (f32x4){bflo(bv[i >> 1][2 * (i & 1)]), bfhi(bv[i >> 1][2 * (i & 1)]), bflo(bv[i >> 1][2 * (i & 1) + 1]), bfhi(bv[i >> 1][2 * (i & 1) + 1])};
#pragma unroll
      for (int j = 1; j < 4; j++) u[i] += lf4(LP(L0 + P2STG + ((w - j) & 3) * 24576 + lane * 16) + ((j - 1) * 4 + i) * 1024);
    }
#pragma unroll
    for (int i = 0; i < 4; i += 2) {
      const auto r0 = __builtin_amdgcn_permlane16_swap(pkv(lo2(u[i])), pkv(lo2(u[i + 1])), false, false);
      const auto r1 = __builtin_amdgcn_permlane16_swap(pkv(hi2(u[i])), pkv(hi2(u[i + 1])), false, false);
      const u32x4 o = {r0[0], r1[0], r0[1], r1[1]};
      *(u32x4*)(out + srow * D + 64 * ct + 16 * i + 16 * (g & 1) + 8 * (g >> 1)) = o;
    }
    ST(10);
  } else {
    if (gi < 22) {
      const bool c1on = lane < (w == 3 ? 4 : 1);
      const char* const c1w = ws + WS_C1 + (((w == 3 ? 0 : w) + (lane & 3)) * 32 + (blockIdx.x & 31)) * 128;
      u64 c1v = 0, ep = 0;
      head(1); rest(1);
#pragma unroll 1
      for (int s = 2; s < 8; s++) {
        head(s);
        WAITV(12); RBAR();
        rest(s);
      }
      ST(1);
      u32x4 stv4;
      auto c1_done = [&]() {
        asm volatile("" : "+v"(c1v), "+v"(ep));
        tag = (uni64(ep) + 1) << 16;
        if (__any(c1on && c1v != tag + 16)) gwait<2>(c1w, c1on, ~0ull, tag + 16);
        if (w == 3) stv4 = bload1(mk_rsrc(ws + WS_STAT, S * 8), (unsigned)((12 * rt - 2) * 64 + min(lane, 55) * 16), 0);
      };
      auto put_stats = [&]() {
        if (w == 3) {
          const u32x2 tg = {(unsigned)tag, (unsigned)(tag >> 32)};
          *(LAS u32x2*)LP(L0 + SY + Y_TAG) = tg;
          *(LAS u32x4*)LP(L0 + QSTAT + min(lane, 55) * 16) = stv4;
          sy_put<Y_ST>(a4, 1);
        }
      };
      const int ft = rt, dt = hg;
      const rsrc_t rht = mk_rsrc(ws + WS_HT, HT_BYTES);
      const u32x4 s1 = mk_srd(w1, S * F * 2);
      u32x4 fb[16][4];
      const unsigned sz = (((pc >> 1) ^ ((r8 >> 1) & 1)) << 5) | ((pc & 1) << 4), vwb = (512 * w + r8) * 2752 + ft * 128;
      const unsigned vw0 = (ft == 21 && sz >= 64) ? OOB : vwb + sz, vw1 = (ft == 21 && sz < 64) ? OOB : vwb + (sz ^ 64);
      const unsigned vb = lane * 16 + (4 * dt * 64 + 16 * w) * 1024;
      const unsigned lb = L0 + G1STG + w * 4096;
      const unsigned ar = lb + (8 * g + (c >> 2)) * 128 + ((((c >> 3) | ((g & 1) << 1))) << 5) + (c & 3) * 8;
      lp pe[4];
#pragma unroll
      for (int e = 0; e < 4; e++) pe[e] = LP(ar ^ (e << 5));
      auto GW = [&](int k, int j) { glds16(s1, (j & 1) ? vw1 : vw0, (32 * k + 8 * j) * 2752, lb + (k % 3) * 16384 + j * 1024); };
      auto LB = [&](int k, int j) { fb[k][j] = bload1(rht, vb, ((w + j) & 3) * 65536 + k * 1024); };
      u32x2 bb[4];
      asm volatile("global_load_dwordx2 %0, %1, off sc1" : "=v"(ep) : "v"(ws + WS_EPOCH) : "memory");
      if (c1on) asm volatile("global_atomic_add_x2 %0, %1, %0, off sc0" : "+v"(c1v) : "v"(c1w) : "memory");
      GW(0, 0); GW(0, 1);
      WAITV(13); RBAR();
      GW(0, 2); GW(0, 3); GW(1, 0); GW(1, 1); GW(1, 2); GW(1, 3);
      WAITV(10); RBAR();
      {
        const rsrc_t rb1 = mk_rsrc(b1, F * 2);
#pragma unroll
        for (int i = 0; i < 4; i++) bb[i] = __builtin_amdgcn_raw_buffer_load_b64(rb1, (64 * ft + 4 * g) * 2 + i * 32, 0, 0);
      }
      WAITV(12);
      ST(2);
      c1_done();
      ST(3);
      LB(0, 0); LB(0, 1); LB(0, 2); LB(0, 3);
      GW(2, 0); GW(2, 1); GW(2, 2); GW(2, 3);
      LB(1, 0); LB(1, 1); LB(1, 2); LB(1, 3);
      put_stats();
      LB(2, 0); LB(2, 1); LB(2, 2); LB(2, 3);
      LB(3, 0); LB(3, 1); LB(3, 2); LB(3, 3);
      WAITV(28);
      f32x4 acc[4][4];
#pragma unroll
      for (int i = 0; i < 4; i++) {
        acc[i][0] = (f32x4){bflo(bb[i][0]), bfhi(bb[i][0]), bflo(bb[i][1]), bfhi(bb[i][1])};
#pragma unroll
        for (int j = 1; j < 4; j++) acc[i][j] = z4;
      }
      bf16x4 al[2][4], ah[2][4];
#pragma unroll
      for (int e = 0; e < 4; e++) { al[0][e] = ltr(pe[e]); ah[0][e] = ltr(pe[e] + 512); }
      SCHED();
#pragma unroll
      for (int k = 0; k < 15; k++) {
#pragma unroll
        for (int m = 0; m < 16; m++) {
          const int i = m & 3, j = m >> 2;
          mfma_k(k == 0, acc[i][j], cat8(al[k & 1][i], ah[k & 1][i]), as_bf(fb[k][j]));
          SCHED();
          if (m == 5) {
            if (k == 0) WAITV(26); else if (k == 1) WAITV(22); else if (k < 12) WAITV(11); else if (k == 12) WAITV(10); else if (k == 13) WAITV(5); else WAITV(0);
          }
          if (m >= 6 && m < 14) {
            const int e = (m - 6) >> 1;
            if (m & 1) ah[(k + 1) & 1][e] = ltr(pe[e] + ((k + 1) % 3) * 16384 + 512); else al[(k + 1) & 1][e] = ltr(pe[e] + ((k + 1) % 3) * 16384);
          }
          if ((m == 4 || m == 6 || m == 9 || m == 13) && k + 3 < 16) GW(k + 3, m == 4 ? 0 : m == 6 ? 1 : m == 9 ? 2 : 3);
          if (i == 3 && k + 4 < 16) LB(k + 4, j);
          SCHED();
        }
        if (k == 3) ST(8);
        if (k == 7) ST(9);
        if (k == 11) ST(10);
        if (k == ISSK) sy_put<Y_ISS>(a1, 1);
      }
#pragma unroll
      for (int jj = 1; jj <= 4; jj++) {
#pragma unroll
        for (int i = 0; i < 4; i++) {
          acc[i][jj & 3] = mfma32(cat8(al[1][i], ah[1][i]), as_bf(fb[15][jj & 3]), acc[i][jj & 3]); SCHED();
          if (jj >= 2) *(LAS f32x4*)(LP(lb + lane * 16) + (jj - 2) * 16384 + i * 1024) = acc[i][jj - 1];
          SCHED();
        }
      }
      __builtin_amdgcn_s_setprio(3);
      ST(4);
      const lp pin[3] = {LP(L0 + G1STG + ((w - 1) & 3) * 4096 + lane * 16), LP(L0 + G1STG + 16384 + ((w - 2) & 3) * 4096 + lane * 16), LP(L0 + G1STG + 32768 + ((w - 3) & 3) * 4096 + lane * 16)};
      BAR();
      ST(5);
      f32x4 pt[4][3];
#pragma unroll
      for (int i = 0; i < 4; i++)
#pragma unroll
        for (int j = 0; j < 3; j++) pt[i][j] = lf4(pin[j] + i * 1024);
      SCHED();
      u32x2 gp[4];
#pragma unroll
      for (int i = 0; i < 4; i++) {
        const f32x4 u = (acc[i][0] + pt[i][0]) + (pt[i][1] + pt[i][2]);
#pragma unroll
        for (int hf = 0; hf < 2; hf++) {
          constexpr float K1 = -2.f * 0.7978845608028654f * 1.4426950408889634f, K2 = K1 * 0.044715f;
          const f32x2 t = hf ? hi2(u) : lo2(u);
          const f32x2 z = t * fma2(t * t, bc2(K2), bc2(K1));
          f32x2 e = {__builtin_amdgcn_exp2f(z[0]), __builtin_amdgcn_exp2f(z[1])};
          e += bc2(1.f);
          const f32x2 rc = {__builtin_amdgcn_rcpf(e[0]), __builtin_amdgcn_rcpf(e[1])};
          gp[i][hf] = pkv(t * rc);
        }
      }
      const rsrc_t rgg = mk_rsrc(ws + WS_G, G_BYTES);
#pragma unroll
      for (int i = 0; i < 4; i += 2) {
        const auto r0 = __builtin_amdgcn_permlane16_swap(gp[i][0], gp[i + 1][0], false, false);
        const auto r1v = __builtin_amdgcn_permlane16_swap(gp[i][1], gp[i + 1][1], false, false);
        const u32x4 v = {r0[0], r1v[0], r0[1], r1v[1]};
        if (i == 0 || ft < 21) bstore1(v, rgg, (2 * (g & 1) + (g >> 1)) * 256 + c * 16, ((4 * dt + w) * 43 + 2 * ft + (i >> 1)) * 1024);
      }
      ST(6);
      WAITV(0);
      DRAIN();
      {
        const int sl0 = (2 * ft) / 11, sl1 = (2 * ft + 1) / 11;
        char* const c3a = ws + WS_C3 + (((lane & 31) * 8 + dt) * 4 + (lane < 32 ? sl0 : sl1)) * 128;
        RBAR();
        if ((lane >> 4) == w && (lane < 32 || sl1 != sl0)) gadd(c3a);
      }
      ST(7);
    } else {
      if (gi >= 24) {
        unsigned pv[8];
        sy_wait<Y_SIG>(a4, 1);
        {
          const rsrc_t r1 = mk_rsrc(w1, S * F * 2);
          const unsigned c0 = ((22 * sq) >> 2) * 128;
#pragma unroll
          for (int n = 0; n < 8; n++) {
            const unsigned row = w * 512 + (2 * (gi - 24) + 16 * (n >> 1) + ((n & 1) ^ hh)) * 8 + r8;
            pv[n] = __builtin_amdgcn_raw_buffer_load_b32(r1, pc < 6 + (r8 & 1) ? ((row * 2752 + c0) & ~127u) + pc * 128 : OOB, 0, 0);
            if (n >= PACE0 && n < 7) WAITV(0);
          }
        }
        const int so = sq * 4 + ((gi - 24) >> 1), dq = hh * 2 + (gi & 1);
        const u32x4 swo = mk_srd(wo, D * D * 2);
        const unsigned vwo = (8 * w + r8) * 1024 + dq * 256 + vsw;
        const rsrc_t ro = mk_rsrc(ws + WS_O, O_BYTES), rb = mk_rsrc(ws + WS_BASE, S * D * 2);
        const int srow = 128 * so + 32 * w + c;
        unsigned bi0 = b2[srow], bi1 = b2[srow + 16];
        asm volatile("" : "+v"(bi0), "+v"(bi1) : "v"(pv[0]), "v"(pv[1]), "v"(pv[2]), "v"(pv[3]), "v"(pv[4]), "v"(pv[5]), "v"(pv[6]), "v"(pv[7]));
        const float bias0 = bflo(bi0), bias1 = bflo(bi1);
        const char* const c2p = ws + WS_C2 + ((so * 4 + w) * 4 + dq) * 128;
        u64 c2v = 0;
#pragma unroll 1
        for (int n = 0; n < 16; n++) {
          if (n == 11 && lane == 0) asm volatile("global_atomic_add_x2 %0, %1, %0, off sc0" : "+v"(c2v) : "v"(c2p) : "memory");
          glds16(swo, vwo, n * 32768, L0 + (n * 4 + w) * 1024);
          glds16(swo, vwo, n * 32768 + 128, L0 + 65536 + (n * 4 + w) * 1024);
        }
        WAITV(10);
        asm volatile("" : "+v"(c2v));
        ST(8);
        if (uni64(c2v) != tag + 8) {
          sy_put<Y_OK2>(a1, 1);
          gwait<2>(c2p, lane == 0, ~0ull, tag + 8);
        }
        ST(3);
        u32x4 of[16][2];
        const unsigned vo = lane * 16 + (8 * so + 2 * w) * 16384;
        constexpr int OD = 8;
#pragma unroll
        for (int k = 0; k < OD; k++) { of[k][0] = bload1(ro, vo, k * 1024); of[k][1] = bload1(ro, vo, 16384 + k * 1024); }
        WAITV(16);
        sy_put<Y_WO>(a1, 1);
        sy_put<Y_OK2>(a1, 1);
        sy_wait<Y_WO>(a4, 1);
        const unsigned ar = L0 + (8 * g + (c >> 2)) * 128 + ((((c >> 3) | ((g & 1) << 1))) << 5) + (c & 3) * 8;
        lp pe[8];
#pragma unroll
        for (int e = 0; e < 8; e++) pe[e] = LP((ar ^ ((e & 3) << 5)) + (e >> 2) * 65536);
        f32x4 acc[8][2];
#pragma unroll
        for (int i = 0; i < 8; i++) { acc[i][0] = (f32x4){bias0, bias0, bias0, bias0}; acc[i][1] = (f32x4){bias1, bias1, bias1, bias1}; }
        bf16x4 tl[2][8], th[2][8];
#pragma unroll
        for (int e = 0; e < 8; e++) { tl[0][e] = ltr(pe[e]); th[0][e] = ltr(pe[e] + 512); }
        SCHED();
#pragma unroll
        for (int k = 0; k < 16; k++) {
#pragma unroll
          for (int e = 0; e < 8; e++) {
            mfma_k(k == 0, acc[e][0], cat8(tl[k & 1][e], th[k & 1][e]), as_bf(of[k][0])); SCHED();
            if (k < 15) tl[(k + 1) & 1][e] = ltr(pe[e] + (k + 1) * 4096);
            SCHED();
            mfma_k(k == 0, acc[e][1], cat8(tl[k & 1][e], th[k & 1][e]), as_bf(of[k][1])); SCHED();
            if (k < 15) th[(k + 1) & 1][e] = ltr(pe[e] + (k + 1) * 4096 + 512);
            SCHED();
          }
          if (k + OD < 16) { of[k + OD][0] = bload1(ro, vo, (k + OD) * 1024); of[k + OD][1] = bload1(ro, vo, 16384 + (k + OD) * 1024); }
          if (k == 14) { FENCE(); sy_put<Y_ISS>(a1, 3); __builtin_amdgcn_s_setprio(3); }
        }
        MFMA_DONE();
        SCHED();
        ST(4);
#pragma unroll
        for (int j = 0; j < 2; j++)
#pragma unroll
          for (int i = 0; i < 4; i++) {
            const u32x4 o = {pkv(lo2(acc[2 * i][j])), pkv(hi2(acc[2 * i][j])), pkv(lo2(acc[2 * i + 1][j])), pkv(hi2(acc[2 * i + 1][j]))};
            bstore1(o, rb, lane * 16, ((8 * so + 2 * w + j) * 16 + 4 * dq + i) * 1024);
          }
        ST(6);
        WAITV(0);
        if (last_of(L0 + SY + Y_N3, 4, lane) && lane < 4) gadd(ws + WS_C4 + ((2 * so + (lane >> 1)) * 8 + 2 * dq + (lane & 1)) * 128);
        ST(7);
      } else {
        const rsrc_t rw2 = mk_rsrc(w2, F * S * 2);
        unsigned pv[6];
#pragma unroll
        for (int n = 0; n < 6; n++) pv[n] = __builtin_amdgcn_raw_buffer_load_b32(rw2, ((44 * w + 2 * (10 + gi - 22 + 2 * n) + hh) * 8 + r8) * 4096 + sq * 1024 + pc * 128, 0, 0);
        asm volatile("" ::"v"(pv[0]), "v"(pv[1]), "v"(pv[2]), "v"(pv[3]), "v"(pv[4]), "v"(pv[5]));
        sy_put<Y_ISS>(a1, 3);
        __builtin_amdgcn_s_sleep(64);
      }
      DRAIN();
    }
    asm volatile("" ::"v"(pfc));
    if (w == 1) {
      gwait<4>(ws + WS_C4 + (st * 8 + ct) * 128, lane == 0, ~0ull, tag + 1);
      sy_put<Y_OK4>(a4, 1);
      if (gi == 22 || gi == 23) { sy_wait<Y_HA>(a4, 1); sy_wait<Y_ISS>(a4, 3); if (lane == 0) gadd(ws + WS_C5); }
    }
    if (w == 2 && blockIdx.x == 0) {
      gwait<4>(ws + (lane < 32 ? WS_C3 + lane * 128 : WS_C5), lane <= 32, ~0ull, tag + (lane < 32 ? 6 : 16));
      if (lane == 0) __hip_atomic_store((GAS u64*)(ws + WS_EPOCH), tag >> 16, __ATOMIC_RELAXED, __HIP_MEMORY_SCOPE_AGENT);
    }
    BAR();
  }
  STFLUSH();
}

extern "C" void kernel_launch(void* const* d_in, const int* in_sizes, int n_in, void* d_out, int out_size, void* d_ws, size_t ws_size, hipStream_t stream) {
  (void)hipFuncSetAttribute((const void*)fused, hipFuncAttributeMaxDynamicSharedMemorySize, LDS_BYTES);
  fused<<<256, 512, LDS_BYTES, stream>>>((const u16*)d_in[0], (const u16*)d_in[1], (const u16*)d_in[2], (const u16*)d_in[4], (const u16*)d_in[5],
                                         (const u16*)d_in[6], (const u16*)d_in[7], (char*)d_ws, (u16*)d_out);
}
```

```cpp
#include <hip/hip_runtime.h>
#include <stdint.h>

#define PACE0 2
#define ISSK 12
#define CODE_LEN 35840

typedef unsigned short u16;
typedef unsigned long long u64;
typedef short s16x4 __attribute__((ext_vector_type(4)));
typedef __bf16 bf16x2 __attribute__((ext_vector_type(2)));
typedef __bf16 bf16x4 __attribute__((ext_vector_type(4)));
typedef __bf16 bf16x8 __attribute__((ext_vector_type(8)));
typedef float f32x2 __attribute__((ext_vector_type(2)));
typedef float f32x4 __attribute__((ext_vector_type(4)));
typedef unsigned u32x2 __attribute__((ext_vector_type(2)));
typedef unsigned u32x4 __attribute__((ext_vector_type(4)));
#define LAS __attribute__((address_space(3)))
#define GAS __attribute__((address_space(1)))
#define DI __device__ __forceinline__

constexpr int S = 2048, D = 512, F = 1376;
constexpr float EPS = 1e-5f;
constexpr unsigned HT_BYTES = 32u * 64u * 1024u;
constexpr unsigned G_BYTES = 32u * 43u * 1024u;
constexpr unsigned O_BYTES = 128u * 16u * 1024u;
constexpr size_t WS_EPOCH = 0;
constexpr size_t WS_C1 = 4096;
constexpr size_t WS_C2 = WS_C1 + 128 * 128;
constexpr size_t WS_C3 = WS_C2 + 256 * 128;
constexpr size_t WS_C4 = WS_C3 + 1024 * 128;
constexpr size_t WS_C5 = WS_C4 + 256 * 128;
constexpr size_t WS_STAT = WS_C5 + 1024;
constexpr size_t WS_HT = WS_STAT + 16384;
constexpr size_t WS_G = WS_HT + HT_BYTES;
constexpr size_t WS_O = WS_G + G_BYTES;
constexpr size_t WS_BASE = WS_O + O_BYTES;
constexpr int SC1 = 16;
constexpr unsigned OOB = 0x80000000u;

typedef __amdgpu_buffer_rsrc_t rsrc_t;
DI rsrc_t mk_rsrc(const void* p, unsigned bytes) { return __builtin_amdgcn_make_buffer_rsrc((void*)p, 0, bytes, 0x00020000); }
DI u32x4 mk_srd(const void* p, unsigned bytes) {
  const uint64_t a = (uint64_t)p;
  u32x4 r = {(unsigned)a, (unsigned)(a >> 32) & 0xffffu, bytes, 0x00020000u};
  return r;
}
DI void glds16(u32x4 srd, unsigned voff, unsigned soff, unsigned lds) {
  asm volatile("s_mov_b32 m0, %3\n\ts_nop 0\n\tbuffer_load_dwordx4 %0, %1, %2 offen lds" ::"v"(voff), "s"(srd), "s"(soff), "s"(lds) : "memory");
}
DI void glds16x3(u32x4 srd, unsigned voff, unsigned soff, unsigned lds) {
  asm volatile("s_mov_b32 m0, %3\n\ts_nop 0\n\tbuffer_load_dwordx4 %0, %1, %2 offen lds\n\tbuffer_load_dwordx4 %0, %1, %2 offen offset:1024 lds\n\t"
               "buffer_load_dwordx4 %0, %1, %2 offen offset:2048 lds" ::"v"(voff), "s"(srd), "s"(soff), "s"(lds) : "memory");
}
#define WAITV(n) asm volatile("s_waitcnt vmcnt(" #n ")" ::: "memory")
#define FENCE() asm volatile("" ::: "memory")
#define SCHED() __builtin_amdgcn_sched_barrier(0)
#define DRAIN() __builtin_amdgcn_s_waitcnt(0x0F70)
#define RBAR() do { FENCE(); __builtin_amdgcn_s_barrier(); FENCE(); } while (0)
#define BAR() do { asm volatile("s_waitcnt lgkmcnt(0)" ::: "memory"); __builtin_amdgcn_s_barrier(); FENCE(); } while (0)

typedef LAS char* lp;
DI lp LP(unsigned a) { return (lp)(uintptr_t)a; }
DI bf16x8 cat8(bf16x4 a, bf16x4 b) { return __builtin_shufflevector(a, b, 0, 1, 2, 3, 4, 5, 6, 7); }
DI bf16x8 l128(lp p) { return *(const LAS bf16x8*)p; }
DI f32x4 lf4(lp p) { return *(const LAS f32x4*)p; }
DI bf16x4 ltr(lp p) { return __builtin_amdgcn_ds_read_tr16_b64_v4bf16((LAS bf16x4*)p); }
DI bf16x8 ltr2(lp p, int d) { return cat8(ltr(p), ltr(p + d)); }
DI f32x4 mfma32(bf16x8 a, bf16x8 b, f32x4 c) { return __builtin_amdgcn_mfma_f32_16x16x32_bf16(a, b, c, 0, 0, 0); }
DI void mfma_ip(f32x4& c, bf16x8 a, bf16x8 b) { asm volatile("v_mfma_f32_16x16x32_bf16 %0, %1, %2, %0" : "+v"(c) : "v"(a), "v"(b)); }
DI void mfma_k(bool first, f32x4& c, bf16x8 a, bf16x8 b) { if (first) c = mfma32(a, b, c); else mfma_ip(c, a, b); }
#define MFMA_DONE() asm volatile("s_nop 7\n\ts_nop 3" ::: "memory")
DI f32x4 mfma16(u32x2 a, u32x2 b, f32x4 c) { return __builtin_amdgcn_mfma_f32_16x16x16bf16_1k(__builtin_bit_cast(s16x4, a), __builtin_bit_cast(s16x4, b), c, 0, 0, 0); }
DI unsigned pkv(f32x2 f) { return __builtin_bit_cast(unsigned, __builtin_convertvector(f, bf16x2)); }
DI unsigned pk2(float a, float b) { f32x2 f = {a, b}; return pkv(f); }
DI f32x2 lo2(f32x4 v) { return __builtin_shufflevector(v, v, 0, 1); }
DI f32x2 hi2(f32x4 v) { return __builtin_shufflevector(v, v, 2, 3); }
DI f32x2 fma2(f32x2 a, f32x2 b, f32x2 c) { return __builtin_elementwise_fma(a, b, c); }
DI f32x2 bc2(float a) { f32x2 r = {a, a}; return r; }
DI float bflo(unsigned u) { return __uint_as_float(u << 16); }
DI float bfhi(unsigned u) { return __uint_as_float(u & 0xffff0000u); }
template <int CTRL> DI float dpp(float v) { return __uint_as_float(__builtin_amdgcn_update_dpp(0u, __float_as_uint(v), CTRL, 0xf, 0xf, true)); }
DI float sum16(float v) { v += dpp<0xB1>(v); v += dpp<0x4E>(v); v += dpp<0x141>(v); v += dpp<0x140>(v); return v; }
DI f32x2 swap16(float a, float b) { auto r = __builtin_amdgcn_permlane16_swap(__float_as_uint(a), __float_as_uint(b), false, false); f32x2 o = {__uint_as_float(r[0]), __uint_as_float(r[1])}; return o; }
DI f32x2 swap32(float a, float b) { auto r = __builtin_amdgcn_permlane32_swap(__float_as_uint(a), __float_as_uint(b), false, false); f32x2 o = {__uint_as_float(r[0]), __uint_as_float(r[1])}; return o; }
DI float sel(unsigned m, float a, float b) { return __uint_as_float((__float_as_uint(a) & m) | (__float_as_uint(b) & ~m)); }
DI u32x4 bload(rsrc_t r, unsigned v, unsigned s) { return __builtin_amdgcn_raw_buffer_load_b128(r, v, s, 0); }
DI u32x4 bload1(rsrc_t r, unsigned v, unsigned s) { return __builtin_amdgcn_raw_buffer_load_b128(r, v, s, SC1); }
DI void bstore1(u32x4 d, rsrc_t r, unsigned v, unsigned s) { __builtin_amdgcn_raw_buffer_store_b128(d, r, v + s, 0, SC1); }
DI bf16x8 as_bf(u32x4 v) { return __builtin_bit_cast(bf16x8, v); }
DI u64 uni64(u64 v) { return ((u64)(unsigned)__builtin_amdgcn_readfirstlane((unsigned)(v >> 32)) << 32) | (unsigned)__builtin_amdgcn_readfirstlane((unsigned)v); }

constexpr unsigned LDS_BYTES = 163840;
constexpr unsigned QXB = 14336;
constexpr unsigned QST = QXB + 24576;
constexpr unsigned QSTAT = 4 * QST;
constexpr unsigned LNROW = 131072;
constexpr unsigned G1STG = 0;
constexpr unsigned P2STG = 49152;
constexpr unsigned SY = LDS_BYTES - 512;
enum { Y_FULL = 0, Y_FREE = 16, Y_HA = 32, Y_HB = 48, Y_ST = 64, Y_QS = 80, Y_ISS = 96, Y_E2 = 112, Y_RA = 128, Y_OK2 = 144, Y_OK3 = 160, Y_OK4 = 176, Y_WO = 192,
       Y_N1 = 208, Y_N2 = 212, Y_N3 = 216, Y_TAG = 224, Y_SIG = 240 };
static_assert(QSTAT + 896 <= SY && P2STG + 98304 <= SY, "lds");
constexpr unsigned SPIN_GLB = 1u << 17;

template <int OFF> DI void sy_put(unsigned a, unsigned v) { asm volatile("ds_write_b32 %0, %1 offset:%2" ::"v"(a), "v"(v), "i"(OFF) : "memory"); }
template <int OFF> DI void sy_wait(unsigned a4, unsigned target) {
  unsigned t, n;
  asm volatile("s_mov_b32 %1, 0x200000\n"
               "1:\n\tds_read_b32 %0, %2 offset:%4\n\ts_waitcnt lgkmcnt(0)\n\tv_cmp_gt_u32 vcc, %3, %0\n\ts_cbranch_vccz 2f\n\t"
               "s_sleep 1\n\ts_sub_u32 %1, %1, 1\n\ts_cmp_lg_u32 %1, 0\n\ts_cbranch_scc1 1b\n"
               "2:" : "=&v"(t), "=&s"(n) : "v"(a4), "s"(target), "i"(OFF) : "vcc", "scc", "memory");
}
DI bool last_of(unsigned cnt, unsigned n, int lane) {
  unsigned old = 0;
  if (lane == 0) asm volatile("ds_add_rtn_u32 %0, %1, %2\n\ts_waitcnt lgkmcnt(0)" : "=&v"(old) : "v"(cnt), "v"(1u) : "memory");
  return (unsigned)__builtin_amdgcn_readfirstlane(old) == n - 1;
}
DI void gadd(char* p) { (void)__hip_atomic_fetch_add((GAS u64*)p, 1ull, __ATOMIC_RELAXED, __HIP_MEMORY_SCOPE_AGENT); }
DI u64 gpeek(const char* p) {
  u64 z = 0;
  asm volatile("" : "+v"(z));
  return __hip_atomic_fetch_add((GAS u64*)p, z, __ATOMIC_RELAXED, __HIP_MEMORY_SCOPE_AGENT);
}
template <int SL> DI void gwait(const char* p, bool on, u64 mask, u64 want) {
  FENCE();
  bool ok = !on;
#pragma unroll 1
  for (unsigned n = 0; n < SPIN_GLB; n++) {
    if (!ok) ok = (gpeek(p) & mask) == want;
    if (__all(ok)) break;
    __builtin_amdgcn_s_sleep(SL);
  }
  FENCE();
}

#define ST(i)
#define STDECL
#define STFLUSH()

__global__ __launch_bounds__(512, 2) void fused(const u16* __restrict__ x, const u16* __restrict__ wqkv, const u16* __restrict__ wo, const u16* __restrict__ w1,
                                                const u16* __restrict__ b1, const u16* __restrict__ w2, const u16* __restrict__ b2, char* __restrict__ ws,
                                                u16* __restrict__ out) {
  extern __shared__ __attribute__((aligned(16))) char smem[];
  uint64_t pc0; asm volatile("s_getpc_b64 %0" : "=s"(pc0));
  const unsigned L0 = (unsigned)(uintptr_t)(LAS char*)smem;
  const int tid = threadIdx.x, lane = tid & 63;
  const int W = __builtin_amdgcn_readfirstlane(tid >> 6), w = W & 3;
  const int xcd = blockIdx.x & 7, idx = blockIdx.x >> 3;
  const int sq = xcd >> 1, hh = xcd & 1;
  const int st = sq * 8 + (idx >> 2), ct = hh * 4 + (idx & 3);
  const int gi = idx;
  const int rt = (22 * sq + gi) >> 2, hg = hh * 4 + ((22 * sq + gi) & 3);
  const int c = lane & 15, g = lane >> 4, r8 = lane >> 3, pc = lane & 7;
  const f32x4 z4 = {0.f, 0.f, 0.f, 0.f};
  STDECL ST(0);
  const unsigned pfc = __builtin_amdgcn_raw_buffer_load_b32(mk_rsrc((const void*)(pc0 & ~255ull), CODE_LEN), ((idx & 7) * 8 + W) * 1024 + (lane & 7) * 128, 0, 0);
  {
    const unsigned on = (W >> 1) & 1;
    *(LAS unsigned*)LP(L0 + (on ? 0 : SY) + ((tid >> 5) & 3) * (on ? QST : 128) + (tid & 31) * 4) = on ? 0x3f803f80u : 0u;
  }
  const unsigned a4 = L0 + SY + (lane & 3) * 4, a1 = L0 + SY + w * 4;
  asm volatile("" ::"s"(x), "s"(wqkv), "s"(wo), "s"(w1), "s"(b1), "s"(w2), "s"(b2), "s"(ws), "s"(out));
  const u32x4 sx = mk_srd(x, S * D * 2), sw = mk_srd(wqkv, D * 3 * D * 2);
  const unsigned vx0 = (unsigned)((96 * rt - 16 + 8 * w + r8) * 1024 + ((pc ^ (((w & 1) << 2) | (r8 >> 1))) << 4));
  const unsigned vsw = (((pc >> 1) ^ (((r8 >> 1) & 1) | ((w & 1) << 1))) << 5) | ((pc & 1) << 4);
  const unsigned vwq = (8 * w + r8) * 3072 + hg * 128 + vsw;
  auto head = [&](int s) { glds16x3(sw, vwq, s * 64 * 3072, L0 + (s & 3) * QST + QXB + w * 3072); };
  auto rest = [&](int s) {
    const unsigned b = L0 + (s & 3) * QST + w * 1024;
    if (lane >= 8 || w != 0) glds16(sx, vx0, s * 128, b);
    glds16(sx, vx0 + 32768u, s * 128, b + 4096);
    glds16(sx, vx0 + 65536u, s * 128, b + 8192);
    if (w < 2) glds16(sx, vx0 + 98304u, s * 128, b + 12288);
    glds16x3(sw, vwq, s * 64 * 3072 + 32 * 3072, b + QXB + 12288 + w * 2048);
  };
  const int un = sq * 16 + hh * 8 + gi - 24;
  u32x4 xr[4];
  ST(11);
  if (gi >= 24) {
    asm volatile("buffer_load_dwordx4 %0, %4, %5, %6 offen\n\tbuffer_load_dwordx4 %1, %4, %5, %6 offen offset:1024\n\t"
                 "buffer_load_dwordx4 %2, %4, %5, %6 offen offset:2048\n\tbuffer_load_dwordx4 %3, %4, %5, %6 offen offset:3072"
                 : "=&v"(xr[0]), "=&v"(xr[1]), "=&v"(xr[2]), "=&v"(xr[3]) : "v"(lane * 16), "s"(sx), "s"((un * 32 + 4 * W) * 1024) : "memory");
  } else {
    asm volatile("" : "=v"(xr[0]), "=v"(xr[1]), "=v"(xr[2]), "=v"(xr[3]));
    if (gi < 22 && W >= 4) { head(0); rest(0); }
  }
  BAR();
  u64 tag = 0;

  if (gi >= 24) {
    const u64 ep = __hip_atomic_load((const GAS u64*)(ws + WS_EPOCH), __ATOMIC_RELAXED, __HIP_MEMORY_SCOPE_AGENT);
    const unsigned pfw = *(const GAS unsigned*)((lane & 31) < 24 ? (const char*)wqkv + (un * 8 + W) * 3072 + (lane & 31) * 128 : (const char*)wo + (un * 8 + W) * 1024 + (lane & 7) * 128);
    WAITV(2);
    asm volatile("" : "+v"(xr[0]), "+v"(xr[1]), "+v"(xr[2]), "+v"(xr[3]));
    f32x2 v[4][4];
    float p[4], s1[4], s2[4];
    auto red4 = [&](const float (&a)[4]) {
      f32x2 t = swap16(a[0], a[1]), u = swap16(a[2], a[3]);
      t = swap32(t[0] + t[1], u[0] + u[1]);
      return sum16(t[0] + t[1]);
    };
#pragma unroll
    for (int rr = 0; rr < 4; rr++) {
      p[rr] = bflo(__builtin_amdgcn_readfirstlane(xr[rr][0]));
      f32x2 a = {0.f, 0.f}, b = {0.f, 0.f};
#pragma unroll
      for (int j = 0; j < 4; j++) { const f32x2 t = {bflo(xr[rr][j]), bfhi(xr[rr][j])}; v[rr][j] = t - bc2(p[rr]); a += v[rr][j]; b = fma2(v[rr][j], v[rr][j], b); }
      s1[rr] = a[0] + a[1]; s2[rr] = b[0] + b[1];
    }
    if (W == 0) {
      const size_t o = lane < 2 ? WS_C1 + (un * 2 + lane) * 128 : WS_C2 + (un * 24 + lane - 2) * 128;
      u64 e = ep;
      asm volatile("" : "+v"(e));
      if (lane < (un == 63 ? 27 : 26)) __hip_atomic_store((GAS u64*)(ws + o), (e + 1) << 16, __ATOMIC_RELAXED, __HIP_MEMORY_SCOPE_AGENT);
    }
    {
      const float m = red4(s1) * (1.f / D), var = fmaxf(red4(s2) * (1.f / D) - m * m, 0.f);
      const float r = __builtin_amdgcn_rsqf(var + EPS);
      const float a = r * __builtin_amdgcn_rsqf(var * r * r + EPS), ma = -m * a;
#pragma unroll
      for (int rr = 0; rr < 4; rr++) {
        const f32x2 av = bc2(__uint_as_float(__builtin_amdgcn_readlane(__float_as_uint(a), 16 * rr))), cv = bc2(__uint_as_float(__builtin_amdgcn_readlane(__float_as_uint(ma), 16 * rr)));
        u32x4 h;
#pragma unroll
        for (int j = 0; j < 4; j++) h[j] = pkv(fma2(v[rr][j], av, cv));
        *(LAS u32x4*)LP(L0 + (4 * W + rr) * 1088 + ((W >> 1) & 1) * 32 + lane * 16) = h;
      }
      const float pg = g == 0 ? p[0] : g == 1 ? p[1] : g == 2 ? p[2] : p[3];
      *(LAS float*)LP(L0 + QSTAT + (W >> 1) * 64 + (W & 1) * 16 + g * 4 + (lane & 1) * 32) = (lane & 1) ? (m + pg) * r : r;
    }
    ST(12);
    char* const c1p = ws + WS_C1 + (sq * 32 + (lane & 31)) * 128;
    u64 chk = 0;
    if (W == 1 && lane < 32) asm volatile("global_atomic_add_x2 %0, %1, %0, off sc0" : "+v"(chk) : "v"(c1p) : "memory");
    BAR();
    {
      bf16x8 tv[4];
#pragma unroll
      for (int n = 0; n < 4; n++) tv[n] = ltr2(LP(L0 + (8 * g + (c >> 2)) * 1088 + (g & 1) * 32 + W * 128 + (c & 3) * 8) + n * 32, 4 * 1088);
      const u32x4 sv = *(const LAS u32x4*)LP(L0 + QSTAT + c * 16);
      const rsrc_t rht = mk_rsrc(ws + WS_HT, HT_BYTES);
#pragma unroll
      for (int n = 0; n < 4; n++) bstore1(__builtin_bit_cast(u32x4, tv[n]), rht, lane * 16, ((4 * W + n) * 64 + un) * 1024);
      if (W == 2) bstore1(sv, mk_rsrc(ws + WS_STAT, S * 8), lane < 16 ? un * 256 + lane * 16 : OOB, 0);
    }
    ST(1);
    WAITV(0);
    DRAIN();
    u64 e = ep;
    asm volatile("" : "+v"(e) : "v"(pfw));
    tag = (uni64(e) + 1) << 16;
    if (W == 1) { asm volatile("" : "+v"(chk)); if (__any(lane < 32 && (chk >> 16) != (tag >> 16))) gwait<2>(c1p, lane < 32, ~0xffffull, tag); }
    if (last_of(L0 + SY + Y_N1, 8, lane)) { if (lane < 32) gadd(c1p); sy_put<Y_SIG>(a4, 1); }
    ST(2);
  } else if (gi >= 22) {
    unsigned p3[3] = {0, 0, 0};
    if (W >= 4) {
#pragma unroll
      for (int n = 0; n < 3; n++) p3[n] = __builtin_amdgcn_raw_buffer_load_b32(mk_rsrc(wqkv, D * 3 * D * 2), hh ? OOB : ((n * 8 + (gi - 22) * 4 + w) * 64 + lane) * 1024 + 384, 0, 0);
    } else if (W > 0) {
#pragma unroll
      for (int n = 0; n < 2; n++) p3[n] = __builtin_amdgcn_raw_buffer_load_b32(mk_rsrc(x, S * D * 2), (unsigned)((((22 * sq) >> 2) * 96 - 16 + (n * 6 + (gi - 22) * 3 + W - 1) * 52 + min(lane, 51)) * 1024 + 384), 0, 0);
    }
    tag = (uni64(__hip_atomic_load((const GAS u64*)(ws + WS_EPOCH), __ATOMIC_RELAXED, __HIP_MEMORY_SCOPE_AGENT)) + 1) << 16;
    if (W < 4) sy_put<Y_HA>(a1, 1);
    asm volatile("" ::"v"(p3[0]), "v"(p3[1]), "v"(p3[2]));
  }

  if (W < 4) {
    if (gi < 22) {
      f32x4 aq[7], ak[7], av[7];
#pragma unroll
      for (int b = 0; b < 7; b++) { ak[b] = z4; av[b] = z4; aq[b] = z4; }
      const unsigned ax = L0 + c * 128 + ((g ^ (c >> 1)) << 4);
      const unsigned aw = L0 + QXB + g * 3072 + (c >> 2) * 128 + ((w ^ ((c >> 3) | ((g & 1) << 1))) << 5) + (c & 3) * 8;
      bf16x8 xf[2][7];
      bf16x4 wl[2][3], wh[2][3];
      const lp pxa[2][2] = {{LP(ax), LP(ax ^ 64)}, {LP(ax + 2 * QST), LP((ax ^ 64) + 2 * QST)}}, pwa[2] = {LP(aw), LP(aw + 2 * QST)};
      auto RD = [&](int n, int i, int slot, int kk) {
        const lp px = pxa[slot >> 1][kk] + (slot & 1) * QST, pw = pwa[slot >> 1] + (slot & 1) * QST + kk * 12288;
        if (i == 0) wl[n][1] = ltr(pw + 1024);
        if (i == 1) wh[n][1] = ltr(pw + 1024 + 512);
        if (i == 2) xf[n][6] = l128(px + 12288);
        if (i == 3) wl[n][2] = ltr(pw + 2048);
        if (i == 4) wh[n][2] = ltr(pw + 2048 + 512);
        if (i == 5) wl[n][0] = ltr(pw);
        if (i == 6) wh[n][0] = ltr(pw + 512);
        if (i >= 7) xf[n][12 - i] = l128(px + (12 - i) * 2048);
      };
      auto MM = [&](int cur, int m) {
        const int t = 6 - m / 3;
        if (m % 3 == 0) mfma_ip(ak[t], cat8(wl[cur][1], wh[cur][1]), xf[cur][t]);
        if (m % 3 == 1) mfma_ip(av[t], xf[cur][t], cat8(wl[cur][2], wh[cur][2]));
        if (m % 3 == 2) mfma_ip(aq[t], cat8(wl[cur][0], wh[cur][0]), xf[cur][t]);
      };
      RBAR();
      ST(2);
#pragma unroll
      for (int i = 0; i < 13; i++) RD(0, i, 0, 0);
      SCHED();
#pragma unroll 1
      for (int s4 = 0; s4 < 8; s4 += 4) {
#pragma unroll
        for (int sl = 0; sl < 4; sl++) {
#pragma unroll
          for (int m = 0; m < 21; m++) {
            MM(0, m); SCHED();
            if (m < 13) RD(1, m, sl, 1);
            if (m == 19 && (sl < 3 || s4 == 0)) BAR();
            SCHED();
          }
#pragma unroll
          for (int m = 0; m < 21; m++) {
            MM(1, m); SCHED();
            if (m < 13) RD(0, m, (sl + 1) & 3, 0);
            SCHED();
          }
        }
      }
      MFMA_DONE();
      SCHED();
      ST(3);
      f32x2 cq[2], ck[2];
#pragma unroll
      for (int j = 0; j < 4; j++) { cq[j >> 1][j & 1] = -dpp<0x150>(aq[0][j]); ck[j >> 1][j & 1] = -dpp<0x150>(ak[0][j]); }
      f32x2 cvt = swap16(av[0][0], av[0][0]);
      cvt = swap32(cvt[0], cvt[0]);
      const f32x2 cv = bc2(-cvt[0]);
      sy_wait<Y_ST>(a4, 1);
      u32x2 qf[6], kf[7], vf[7];
      {
        float sr[7], sm[7]; f32x4 vr[7], vm[7];
        const lp pcs = LP(L0 + QSTAT + (c >> 3) * 64 + (c & 7) * 4), prs = LP(L0 + QSTAT + (g >> 1) * 64 + (g & 1) * 16);
#pragma unroll
        for (int t = 0; t < 7; t++) {
          sr[t] = *(const LAS float*)(pcs + t * 128); sm[t] = *(const LAS float*)(pcs + t * 128 + 32);
          vr[t] = lf4(prs + t * 128); vm[t] = lf4(prs + t * 128 + 32);
        }
        SCHED();
#pragma unroll
        for (int t = 0; t < 7; t++) {
          const f32x2 r = bc2(sr[t]), m = bc2(sm[t]);
          f32x2 y[6] = {m * ck[0], m * ck[1], m * cq[0], m * cq[1], lo2(vm[t]) * cv, hi2(vm[t]) * cv};
          SCHED();
          y[0] = fma2(r, lo2(ak[t]), y[0]); y[1] = fma2(r, hi2(ak[t]), y[1]); y[2] = fma2(r, lo2(aq[t]), y[2]); y[3] = fma2(r, hi2(aq[t]), y[3]);
          y[4] = fma2(lo2(vr[t]), lo2(av[t]), y[4]); y[5] = fma2(hi2(vr[t]), hi2(av[t]), y[5]);
          SCHED();
          kf[t][0] = pkv(y[0]); kf[t][1] = pkv(y[1]);
          if (t > 0) { qf[t - 1][0] = pkv(y[2]); qf[t - 1][1] = pkv(y[3]); }
          vf[t][0] = pkv(y[4]); vf[t][1] = pkv(y[5]);
        }
        SCHED();
      }
      u32x2 op[6];
      {
        unsigned om = 0u - (unsigned)(c & 1);
        asm("" : "+v"(om));
        const bool cur0 = 4 * g + (c & 1) <= c, cur1 = 4 * g + (c & 1) + 2 <= c;
        constexpr float SC = 0.25f * 1.4426950408889634f;
        f32x4 sp[6], su[6];
#pragma unroll
        for (int t = 0; t < 6; t++) { sp[t] = mfma16(kf[t], qf[t], z4); su[t] = mfma16(kf[t + 1], qf[t], z4); }
        float l0[6], l1[6], mx[8];
#pragma unroll
        for (int t = 0; t < 6; t++) {
          const float p0 = sel(om, sp[t][1], sp[t][0]), p1 = sel(om, sp[t][3], sp[t][2]);
          const float u0 = sel(om, su[t][1], su[t][0]), u1 = sel(om, su[t][3], su[t][2]);
          l0[t] = cur0 ? u0 : p0; l1[t] = cur1 ? u1 : p1;
          if (t == 0 && rt == 0) { l0[t] = cur0 ? l0[t] : -4e9f; l1[t] = cur1 ? l1[t] : -4e9f; }
          mx[t] = fmaxf(l0[t], l1[t]);
        }
        {
          const f32x2 a = swap16(mx[0], mx[1]), b = swap16(mx[2], mx[3]), a2 = swap16(mx[4], mx[5]);
          const float n2 = fmaxf(a2[0], a2[1]);
          const f32x2 d = swap32(fmaxf(a[0], a[1]), fmaxf(b[0], b[1])), d2 = swap32(n2, n2);
          const float m = fmaxf(d[0], d[1]) * -SC, m2 = fmaxf(d2[0], d2[1]) * -SC;
          const f32x2 e = swap32(m, m), f0 = swap16(e[0], e[0]), f1 = swap16(e[1], e[1]), f2 = swap16(m2, m2);
          mx[0] = f0[0]; mx[1] = f0[1]; mx[2] = f1[0]; mx[3] = f1[1]; mx[4] = f2[0]; mx[5] = f2[1];
        }
        const u32x4 ones = {0x3f803f80u, 0x3f803f80u, 0x3f803f80u, 0x3f803f80u};
        const unsigned keep = om ^ 0x0000ffffu;
        const unsigned k00 = cur0 ? 0u : keep, k01 = cur1 ? 0u : keep, k10 = cur0 ? keep : 0u, k11 = cur1 ? keep : 0u;
        f32x4 o[6], den[6];
#pragma unroll
        for (int t = 0; t < 6; t++) {
          const float e0 = __builtin_amdgcn_exp2f(__builtin_fmaf(l0[t], SC, mx[t])), e1 = __builtin_amdgcn_exp2f(__builtin_fmaf(l1[t], SC, mx[t]));
          const unsigned b0 = pk2(e0, e0), b1v = pk2(e1, e1);
          const u32x4 pu = {b0 & k00, b1v & k01, b0 & k10, b1v & k11}, vv = {vf[t][0], vf[t][1], vf[t + 1][0], vf[t + 1][1]};
          o[t] = mfma32(as_bf(vv), as_bf(pu), z4);
          den[t] = mfma32(as_bf(ones), as_bf(pu), z4);
        }
#pragma unroll
        for (int t = 0; t < 6; t++) {
          const f32x2 inv = bc2(__builtin_amdgcn_rcpf(den[t][0]));
          op[t][0] = pkv(lo2(o[t]) * inv); op[t][1] = pkv(hi2(o[t]) * inv);
        }
      }
      {
        const rsrc_t ro = mk_rsrc(ws + WS_O, O_BYTES);
#pragma unroll
        for (int t = 0; t < 6; t += 2) {
          const auto r0 = __builtin_amdgcn_permlane16_swap(op[t][0], op[t + 1][0], false, false);
          const auto r1 = __builtin_amdgcn_permlane16_swap(op[t][1], op[t + 1][1], false, false);
          const u32x4 v = {r0[0], r1[0], r0[1], r1[1]};
          if (t == 0 || rt < 21) bstore1(v, ro, (g & 1) * 16384 + ((g >> 1) * 16 + c) * 16, ((6 * rt + t) * 16 + 2 * hg + (w >> 1)) * 1024 + (w & 1) * 512);
        }
      }
      ST(13);
      sy_put<Y_QS>(a1, 1);
      WAITV(0);
      if (last_of(L0 + SY + Y_N2, 4, lane) && lane < (rt < 21 ? 12 : 4)) gadd(ws + WS_C2 + (12 * rt + lane) * 128);
      { const u32x2 tg = *(const LAS u32x2*)LP(L0 + SY + Y_TAG); tag = ((u64)(unsigned)__builtin_amdgcn_readfirstlane(tg[1]) << 32) | (unsigned)__builtin_amdgcn_readfirstlane(tg[0]); }
      ST(4);
    } else {
      const rsrc_t rw2 = mk_rsrc(w2, F * S * 2);
      if (gi >= 24) {
        sy_wait<Y_OK2>(a4, 1);
        const int m = gi - 24 + 8 * r8;
        const unsigned t = __builtin_amdgcn_raw_buffer_load_b32(rw2, m < 22 ? ((44 * w + 2 * m + (hh ^ 1)) * 8 + pc) * 4096 + sq * 1024 + 384 : OOB, 0, 0);
        asm volatile("" ::"v"(t));
      } else {
        unsigned pv[5];
#pragma unroll
        for (int n = 0; n < 5; n++) pv[n] = __builtin_amdgcn_raw_buffer_load_b32(rw2, ((44 * w + 2 * (gi - 22 + 2 * n) + hh) * 8 + r8) * 4096 + sq * 1024 + pc * 128, 0, 0);
        asm volatile("" ::"v"(pv[0]), "v"(pv[1]), "v"(pv[2]), "v"(pv[3]), "v"(pv[4]));
      }
      ST(4);
    }
    asm volatile("" ::"v"(pfc));

    const rsrc_t rgg = mk_rsrc(ws + WS_G, G_BYTES);
    const unsigned vg = lane * 16 + (4 * ct * 43 + 11 * w) * 1024;
    const u32x4 sw2 = mk_srd(w2, F * S * 2);
    const unsigned vw2 = (352 * w + r8) * 4096 + st * 128 + ((((pc >> 1) ^ ((r8 >> 1) & 1)) << 5) | ((pc & 1) << 4));
    const unsigned lw = L0 + P2STG + w * 24576;
    const unsigned ar = lw + (8 * g + (c >> 2)) * 128 + ((((c >> 3) | ((g & 1) << 1))) << 5) + (c & 3) * 8;
    u32x4 gf[11][4];
    auto GW = [&](int k, int slot) {
#pragma unroll
      for (int j = 0; j < 4; j++) glds16(sw2, (k == 10 && w == 3) ? OOB : vw2 ^ ((j & 1) << 6), (32 * k + 8 * j) * 4096, lw + slot * 4096 + j * 1024);
    };
    auto LG1 = [&](int k, int j) { gf[k][j] = bload1(rgg, (k == 10 && w == 3) ? OOB : vg, j * 43 * 1024 + k * 1024); };
    lp pr[4];
#pragma unroll
    for (int j = 0; j < 4; j++) pr[j] = LP(ar ^ (((w + j) & 3) << 5));
    bf16x8 tw[2][4];
    bf16x8 tk[4][4];
    auto TR = [&](bf16x8 (&t)[4], int slot) {
#pragma unroll
      for (int j = 0; j < 4; j++) t[j] = ltr2(pr[j] + slot * 4096, 512);
      asm volatile("" ::"v"(t[0]), "v"(t[1]), "v"(t[2]), "v"(t[3]) : "memory");
    };
    const int srow = 64 * st + 16 * w + c;
    u32x4 xq[2];
    if (gi < 22) { sy_wait<Y_QS>(a4, 1); sy_wait<Y_ISS>(a4, 1); }
    ST(5);
    asm volatile("buffer_load_dwordx4 %0, %2, %3, 0 offen\n\tbuffer_load_dwordx4 %1, %2, %3, 0 offen offset:64"
                 : "=&v"(xq[0]), "=&v"(xq[1]) : "v"((srow * D + 64 * ct + 16 * (g & 1) + 8 * (g >> 1)) * 2), "s"(sx) : "memory");
    const char* const c3p = ws + WS_C3 + ((st * 8 + ct) * 4 + w) * 128;
    u64 c3v = 0;
    const int s6 = -12 - 5 * w;
    if (gi < 22) {
      GW(7, 0); GW(8, 1);
      RBAR();
      GW(9, 2); GW(10, 3); GW(4, 4); GW(5, 5);
      WAITV(16); TR(tk[0], 0); TR(tk[1], 1);
      WAITV(8); TR(tk[2], 2); TR(tk[3], 3);
      RBAR();
      GW(0, 0); GW(1, 1); GW(2, 2);
      if (lane == 0) asm volatile("global_atomic_add_x2 %0, %1, %0, off sc0" : "+v"(c3v) : "v"(c3p) : "memory");
      GW(3, 3); GW(6, s6);
      WAITV(17); TR(tw[0], 0);
    } else {
      const int sc = 20 - 5 * w;
      auto TS = [&](bf16x8 (&t)[4], int k) {
#pragma unroll
        for (int j = 0; j < 4; j++) glds16(sw2, (k == 10 && w == 3) ? OOB : vw2 ^ ((j & 1) << 6), (32 * k + 8 * j) * 4096, lw + sc * 4096 + j * 1024);
        WAITV(0);
#pragma unroll
        for (int j = 0; j < 4; j++) t[j] = ltr2(pr[j] + sc * 4096, 512);
        asm volatile("" ::"v"(t[0]), "v"(t[1]), "v"(t[2]), "v"(t[3]) : "memory");
      };
      TS(tk[0], 7); TS(tk[1], 8); TS(tk[2], 9); TS(tk[3], 10); TS(tw[0], 0);
      sy_put<Y_QS>(a1, 1);
      ST(12);
      sy_wait<Y_QS>(a4, 1);
      sy_wait<Y_ISS>(a4, 3);
      GW(1, 1); GW(2, 2); GW(3, 3); GW(4, 4);
      if (lane == 0) asm volatile("global_atomic_add_x2 %0, %1, %0, off sc0" : "+v"(c3v) : "v"(c3p) : "memory");
      GW(5, 5); GW(6, s6);
    }
    asm volatile("" : "+v"(xq[0]), "+v"(xq[1]));
    u32x2 xv[4];
#pragma unroll
    for (int i = 0; i < 2; i++) {
      const auto r0 = __builtin_amdgcn_permlane16_swap(xq[i][0], xq[i][2], false, false), r1 = __builtin_amdgcn_permlane16_swap(xq[i][1], xq[i][3], false, false);
      xv[2 * i][0] = r0[0]; xv[2 * i][1] = r1[0]; xv[2 * i + 1][0] = r0[1]; xv[2 * i + 1][1] = r1[1];
    }
    f32x4 acc[4][4];
#pragma unroll
    for (int i = 0; i < 4; i++) {
      acc[i][0] = (f32x4){bflo(xv[i][0]), bfhi(xv[i][0]), bflo(xv[i][1]), bfhi(xv[i][1])};
#pragma unroll
      for (int j = 1; j < 4; j++) acc[i][j] = z4;
      asm volatile("" : "+v"(acc[i][0]));
    }
    ST(6);
    WAITV(8);
    asm volatile("" : "+v"(c3v));
    if (uni64(c3v) != tag + 6) gwait<0>(c3p, lane == 0, ~0ull, tag + 6);
    ST(7);
    constexpr int GD = 4;
#pragma unroll
    for (int k = 0; k < GD; k++)
#pragma unroll
      for (int j = 0; j < 4; j++) LG1(k, j);
    WAITV(16);
    SCHED();
    u32x4 bv[2];
#pragma unroll
    for (int k = 0; k < 10; k++) {
      if (k + GD == 11) {
        sy_wait<Y_OK4>(a4, 1);
        const rsrc_t rb = mk_rsrc(ws + WS_BASE, S * D * 2);
#pragma unroll
        for (int i = 0; i < 2; i++) bv[i] = bload1(rb, lane * 16, ((4 * st + w) * 16 + 2 * ct + i) * 1024);
      }
#pragma unroll
      for (int i = 0; i < 4; i++) {
#pragma unroll
        for (int j = 0; j < 4; j++) {
          mfma_k(k == 0, acc[i][j], as_bf(gf[k][i]), k < 7 ? tw[k & 1][j] : tk[k < 7 ? 0 : k - 7][j]);
          if (j == 0 && k + 1 < 7) { SCHED(); tw[(k + 1) & 1][i] = ltr2(pr[i] + (k + 1 < 6 ? k + 1 : s6) * 4096, 512); SCHED(); }
        }
        SCHED();
        if (k + GD < 11) LG1(k + GD, i);
        SCHED();
      }
    }
#pragma unroll
    for (int jj = 1; jj <= 4; jj++) {
#pragma unroll
      for (int i = 0; i < 4; i++) {
        acc[i][jj & 3] = mfma32(as_bf(gf[10][i]), tk[3][jj & 3], acc[i][jj & 3]); SCHED();
        if (jj >= 2) *(LAS f32x4*)(LP(lw + lane * 16) + ((jj - 2) * 4 + i) * 1024) = acc[i][jj - 1];
        SCHED();
      }
    }
    ST(8);
    BAR();
    ST(9);
    f32x4 u[4];
#pragma unroll
    for (int i = 0; i < 4; i++) {
      u[i] = acc[i][0] + (f32x4){bflo(bv[i >> 1][2 * (i & 1)]), bfhi(bv[i >> 1][2 * (i & 1)]), bflo(bv[i >> 1][2 * (i & 1) + 1]), bfhi(bv[i >> 1][2 * (i & 1) + 1])};
#pragma unroll
      for (int j = 1; j < 4; j++) u[i] += lf4(LP(L0 + P2STG + ((w - j) & 3) * 24576 + lane * 16) + ((j - 1) * 4 + i) * 1024);
    }
#pragma unroll
    for (int i = 0; i < 4; i += 2) {
      const auto r0 = __builtin_amdgcn_permlane16_swap(pkv(lo2(u[i])), pkv(lo2(u[i + 1])), false, false);
      const auto r1 = __builtin_amdgcn_permlane16_swap(pkv(hi2(u[i])), pkv(hi2(u[i + 1])), false, false);
      const u32x4 o = {r0[0], r1[0], r0[1], r1[1]};
      *(u32x4*)(out + srow * D + 64 * ct + 16 * i + 16 * (g & 1) + 8 * (g >> 1)) = o;
    }
    ST(10);
  } else {
    if (gi < 22) {
      const bool c1on = lane < (w == 3 ? 4 : 1);
      const char* const c1w = ws + WS_C1 + (((w == 3 ? 0 : w) + (lane & 3)) * 32 + (blockIdx.x & 31)) * 128;
      u64 c1v = 0, ep = 0;
      head(1); rest(1);
#pragma unroll 1
      for (int s = 2; s < 8; s++) {
        head(s);
        WAITV(12); RBAR();
        rest(s);
      }
      ST(1);
      u32x4 stv4;
      auto c1_done = [&]() {
        asm volatile("" : "+v"(c1v), "+v"(ep));
        tag = (uni64(ep) + 1) << 16;
        if (__any(c1on && c1v != tag + 16)) gwait<2>(c1w, c1on, ~0ull, tag + 16);
        if (w == 3) stv4 = bload1(mk_rsrc(ws + WS_STAT, S * 8), (unsigned)((12 * rt - 2) * 64 + min(lane, 55) * 16), 0);
      };
      auto put_stats = [&]() {
        if (w == 3) {
          const u32x2 tg = {(unsigned)tag, (unsigned)(tag >> 32)};
          *(LAS u32x2*)LP(L0 + SY + Y_TAG) = tg;
          *(LAS u32x4*)LP(L0 + QSTAT + min(lane, 55) * 16) = stv4;
          sy_put<Y_ST>(a4, 1);
        }
      };
      const int ft = rt, dt = hg;
      const rsrc_t rht = mk_rsrc(ws + WS_HT, HT_BYTES);
      const u32x4 s1 = mk_srd(w1, S * F * 2);
      u32x4 fb[16][4];
      const unsigned sz = (((pc >> 1) ^ ((r8 >> 1) & 1)) << 5) | ((pc & 1) << 4), vwb = (512 * w + r8) * 2752 + ft * 128;
      const unsigned vw0 = (ft == 21 && sz >= 64) ? OOB : vwb + sz, vw1 = (ft == 21 && sz < 64) ? OOB : vwb + (sz ^ 64);
      const unsigned vb = lane * 16 + (4 * dt * 64 + 16 * w) * 1024;
      const unsigned lb = L0 + G1STG + w * 4096;
      const unsigned ar = lb + (8 * g + (c >> 2)) * 128 + ((((c >> 3) | ((g & 1) << 1))) << 5) + (c & 3) * 8;
      lp pe[4];
#pragma unroll
      for (int e = 0; e < 4; e++) pe[e] = LP(ar ^ (e << 5));
      auto GW = [&](int k, int j) { glds16(s1, (j & 1) ? vw1 : vw0, (32 * k + 8 * j) * 2752, lb + (k % 3) * 16384 + j * 1024); };
      auto LB = [&](int k, int j) { fb[k][j] = bload1(rht, vb, ((w + j) & 3) * 65536 + k * 1024); };
      u32x2 bb[4];
      asm volatile("global_load_dwordx2 %0, %1, off sc1" : "=v"(ep) : "v"(ws + WS_EPOCH) : "memory");
      if (c1on) asm volatile("global_atomic_add_x2 %0, %1, %0, off sc0" : "+v"(c1v) : "v"(c1w) : "memory");
      GW(0, 0); GW(0, 1);
      WAITV(13); RBAR();
      GW(0, 2); GW(0, 3); GW(1, 0); GW(1, 1); GW(1, 2); GW(1, 3);
      WAITV(10); RBAR();
      {
        const rsrc_t rb1 = mk_rsrc(b1, F * 2);
#pragma unroll
        for (int i = 0; i < 4; i++) bb[i] = __builtin_amdgcn_raw_buffer_load_b64(rb1, (64 * ft + 4 * g) * 2 + i * 32, 0, 0);
      }
      WAITV(12);
      ST(2);
      c1_done();
      ST(3);
      LB(0, 0); LB(0, 1); LB(0, 2); LB(0, 3);
      GW(2, 0); GW(2, 1); GW(2, 2); GW(2, 3);
      LB(1, 0); LB(1, 1); LB(1, 2); LB(1, 3);
      put_stats();
      LB(2, 0); LB(2, 1); LB(2, 2); LB(2, 3);
      LB(3, 0); LB(3, 1); LB(3, 2); LB(3, 3);
      WAITV(28);
      f32x4 acc[4][4];
#pragma unroll
      for (int i = 0; i < 4; i++) {
        acc[i][0] = (f32x4){bflo(bb[i][0]), bfhi(bb[i][0]), bflo(bb[i][1]), bfhi(bb[i][1])};
#pragma unroll
        for (int j = 1; j < 4; j++) acc[i][j] = z4;
      }
      bf16x4 al[2][4], ah[2][4];
#pragma unroll
      for (int e = 0; e < 4; e++) { al[0][e] = ltr(pe[e]); ah[0][e] = ltr(pe[e] + 512); }
      SCHED();
#pragma unroll
      for (int k = 0; k < 15; k++) {
#pragma unroll
        for (int m = 0; m < 16; m++) {
          const int i = m & 3, j = m >> 2;
          mfma_k(k == 0, acc[i][j], cat8(al[k & 1][i], ah[k & 1][i]), as_bf(fb[k][j]));
          SCHED();
          if (m == 5) {
            if (k == 0) WAITV(26); else if (k == 1) WAITV(22); else if (k < 12) WAITV(11); else if (k == 12) WAITV(10); else if (k == 13) WAITV(5); else WAITV(0);
          }
          if (m >= 6 && m < 14) {
            const int e = (m - 6) >> 1;
            if (m & 1) ah[(k + 1) & 1][e] = ltr(pe[e] + ((k + 1) % 3) * 16384 + 512); else al[(k + 1) & 1][e] = ltr(pe[e] + ((k + 1) % 3) * 16384);
          }
          if ((m == 4 || m == 6 || m == 9 || m == 13) && k + 3 < 16) GW(k + 3, m == 4 ? 0 : m == 6 ? 1 : m == 9 ? 2 : 3);
          if (i == 3 && k + 4 < 16) LB(k + 4, j);
          SCHED();
        }
        if (k == 3) ST(8);
        if (k == 7) ST(9);
        if (k == 11) ST(10);
        if (k == ISSK) sy_put<Y_ISS>(a1, 1);
      }
#pragma unroll
      for (int jj = 1; jj <= 4; jj++) {
#pragma unroll
        for (int i = 0; i < 4; i++) {
          acc[i][jj & 3] = mfma32(cat8(al[1][i], ah[1][i]), as_bf(fb[15][jj & 3]), acc[i][jj & 3]); SCHED();
          if (jj >= 2) *(LAS f32x4*)(LP(lb + lane * 16) + (jj - 2) * 16384 + i * 1024) = acc[i][jj - 1];
          SCHED();
        }
      }
      __builtin_amdgcn_s_setprio(3);
      ST(4);
      const lp pin[3] = {LP(L0 + G1STG + ((w - 1) & 3) * 4096 + lane * 16), LP(L0 + G1STG + 16384 + ((w - 2) & 3) * 4096 + lane * 16), LP(L0 + G1STG + 32768 + ((w - 3) & 3) * 4096 + lane * 16)};
      BAR();
      ST(5);
      f32x4 pt[4][3];
#pragma unroll
      for (int i = 0; i < 4; i++)
#pragma unroll
        for (int j = 0; j < 3; j++) pt[i][j] = lf4(pin[j] + i * 1024);
      SCHED();
      u32x2 gp[4];
#pragma unroll
      for (int i = 0; i < 4; i++) {
        const f32x4 u = (acc[i][0] + pt[i][0]) + (pt[i][1] + pt[i][2]);
#pragma unroll
        for (int hf = 0; hf < 2; hf++) {
          constexpr float K1 = -2.f * 0.7978845608028654f * 1.4426950408889634f, K2 = K1 * 0.044715f;
          const f32x2 t = hf ? hi2(u) : lo2(u);
          const f32x2 z = t * fma2(t * t, bc2(K2), bc2(K1));
          f32x2 e = {__builtin_amdgcn_exp2f(z[0]), __builtin_amdgcn_exp2f(z[1])};
          e += bc2(1.f);
          const f32x2 rc = {__builtin_amdgcn_rcpf(e[0]), __builtin_amdgcn_rcpf(e[1])};
          gp[i][hf] = pkv(t * rc);
        }
      }
      const rsrc_t rgg = mk_rsrc(ws + WS_G, G_BYTES);
#pragma unroll
      for (int i = 0; i < 4; i += 2) {
        const auto r0 = __builtin_amdgcn_permlane16_swap(gp[i][0], gp[i + 1][0], false, false);
        const auto r1v = __builtin_amdgcn_permlane16_swap(gp[i][1], gp[i + 1][1], false, false);
        const u32x4 v = {r0[0], r1v[0], r0[1], r1v[1]};
        if (i == 0 || ft < 21) bstore1(v, rgg, (2 * (g & 1) + (g >> 1)) * 256 + c * 16, ((4 * dt + w) * 43 + 2 * ft + (i >> 1)) * 1024);
      }
      ST(6);
      WAITV(0);
      DRAIN();
      {
        const int sl0 = (2 * ft) / 11, sl1 = (2 * ft + 1) / 11;
        char* const c3a = ws + WS_C3 + (((lane & 31) * 8 + dt) * 4 + (lane < 32 ? sl0 : sl1)) * 128;
        RBAR();
        if ((lane >> 4) == w && (lane < 32 || sl1 != sl0)) gadd(c3a);
      }
      ST(7);
    } else {
      if (gi >= 24) {
        unsigned pv[8];
        sy_wait<Y_SIG>(a4, 1);
        {
          const rsrc_t r1 = mk_rsrc(w1, S * F * 2);
          const unsigned c0 = ((22 * sq) >> 2) * 128;
#pragma unroll
          for (int n = 0; n < 8; n++) {
            const unsigned row = w * 512 + (2 * (gi - 24) + 16 * (n >> 1) + ((n & 1) ^ hh)) * 8 + r8;
            pv[n] = __builtin_amdgcn_raw_buffer_load_b32(r1, pc < 6 + (r8 & 1) ? ((row * 2752 + c0) & ~127u) + pc * 128 : OOB, 0, 0);
            if (n >= PACE0 && n < 7) WAITV(0);
          }
        }
        const int so = sq * 4 + ((gi - 24) >> 1), dq = hh * 2 + (gi & 1);
        const u32x4 swo = mk_srd(wo, D * D * 2);
        const unsigned vwo = (8 * w + r8) * 1024 + dq * 256 + vsw;
        const rsrc_t ro = mk_rsrc(ws + WS_O, O_BYTES), rb = mk_rsrc(ws + WS_BASE, S * D * 2);
        const int srow = 128 * so + 32 * w + c;
        unsigned bi0 = b2[srow], bi1 = b2[srow + 16];
        asm volatile("" : "+v"(bi0), "+v"(bi1) : "v"(pv[0]), "v"(pv[1]), "v"(pv[2]), "v"(pv[3]), "v"(pv[4]), "v"(pv[5]), "v"(pv[6]), "v"(pv[7]));
        const float bias0 = bflo(bi0), bias1 = bflo(bi1);
        const char* const c2p = ws + WS_C2 + ((so * 4 + w) * 4 + dq) * 128;
        u64 c2v = 0;
#pragma unroll 1
        for (int n = 0; n < 16; n++) {
          if (n == 11 && lane == 0) asm volatile("global_atomic_add_x2 %0, %1, %0, off sc0" : "+v"(c2v) : "v"(c2p) : "memory");
          glds16(swo, vwo, n * 32768, L0 + (n * 4 + w) * 1024);
          glds16(swo, vwo, n * 32768 + 128, L0 + 65536 + (n * 4 + w) * 1024);
        }
        WAITV(10);
        asm volatile("" : "+v"(c2v));
        ST(8);
        if (uni64(c2v) != tag + 8) {
          sy_put<Y_OK2>(a1, 1);
          gwait<2>(c2p, lane == 0, ~0ull, tag + 8);
        }
        ST(3);
        u32x4 of[16][2];
        const unsigned vo = lane * 16 + (8 * so + 2 * w) * 16384;
        constexpr int OD = 8;
#pragma unroll
        for (int k = 0; k < OD; k++) { of[k][0] = bload1(ro, vo, k * 1024); of[k][1] = bload1(ro, vo, 16384 + k * 1024); }
        WAITV(16);
        sy_put<Y_WO>(a1, 1);
        sy_put<Y_OK2>(a1, 1);
        sy_wait<Y_WO>(a4, 1);
        const unsigned ar = L0 + (8 * g + (c >> 2)) * 128 + ((((c >> 3) | ((g & 1) << 1))) << 5) + (c & 3) * 8;
        lp pe[8];
#pragma unroll
        for (int e = 0; e < 8; e++) pe[e] = LP((ar ^ ((e & 3) << 5)) + (e >> 2) * 65536);
        f32x4 acc[8][2];
#pragma unroll
        for (int i = 0; i < 8; i++) { acc[i][0] = (f32x4){bias0, bias0, bias0, bias0}; acc[i][1] = (f32x4){bias1, bias1, bias1, bias1}; }
        bf16x4 tl[2][8], th[2][8];
#pragma unroll
        for (int e = 0; e < 8; e++) { tl[0][e] = ltr(pe[e]); th[0][e] = ltr(pe[e] + 512); }
        SCHED();
#pragma unroll
        for (int k = 0; k < 16; k++) {
#pragma unroll
          for (int e = 0; e < 8; e++) {
            mfma_k(k == 0, acc[e][0], cat8(tl[k & 1][e], th[k & 1][e]), as_bf(of[k][0])); SCHED();
            if (k < 15) tl[(k + 1) & 1][e] = ltr(pe[e] + (k + 1) * 4096);
            SCHED();
            mfma_k(k == 0, acc[e][1], cat8(tl[k & 1][e], th[k & 1][e]), as_bf(of[k][1])); SCHED();
            if (k < 15) th[(k + 1) & 1][e] = ltr(pe[e] + (k + 1) * 4096 + 512);
            SCHED();
          }
          if (k + OD < 16) { of[k + OD][0] = bload1(ro, vo, (k + OD) * 1024); of[k + OD][1] = bload1(ro, vo, 16384 + (k + OD) * 1024); }
          if (k == 14) { FENCE(); sy_put<Y_ISS>(a1, 3); __builtin_amdgcn_s_setprio(3); }
        }
        MFMA_DONE();
        SCHED();
        ST(4);
#pragma unroll
        for (int j = 0; j < 2; j++)
#pragma unroll
          for (int i = 0; i < 4; i++) {
            const u32x4 o = {pkv(lo2(acc[2 * i][j])), pkv(hi2(acc[2 * i][j])), pkv(lo2(acc[2 * i + 1][j])), pkv(hi2(acc[2 * i + 1][j]))};
            bstore1(o, rb, lane * 16, ((8 * so + 2 * w + j) * 16 + 4 * dq + i) * 1024);
          }
        ST(6);
        WAITV(0);
        if (last_of(L0 + SY + Y_N3, 4, lane) && lane < 4) gadd(ws + WS_C4 + ((2 * so + (lane >> 1)) * 8 + 2 * dq + (lane & 1)) * 128);
        ST(7);
      } else {
        const rsrc_t rw2 = mk_rsrc(w2, F * S * 2);
        unsigned pv[6];
#pragma unroll
        for (int n = 0; n < 6; n++) pv[n] = __builtin_amdgcn_raw_buffer_load_b32(rw2, ((44 * w + 2 * (10 + gi - 22 + 2 * n) + hh) * 8 + r8) * 4096 + sq * 1024 + pc * 128, 0, 0);
        asm volatile("" ::"v"(pv[0]), "v"(pv[1]), "v"(pv[2]), "v"(pv[3]), "v"(pv[4]), "v"(pv[5]));
        sy_put<Y_ISS>(a1, 3);
        __builtin_amdgcn_s_sleep(64);
      }
      DRAIN();
    }
    asm volatile("" ::"v"(pfc));
    if (w == 1) {
      gwait<4>(ws + WS_C4 + (st * 8 + ct) * 128, lane == 0, ~0ull, tag + 1);
      sy_put<Y_OK4>(a4, 1);
      if (gi == 22 || gi == 23) { sy_wait<Y_HA>(a4, 1); sy_wait<Y_ISS>(a4, 3); if (lane == 0) gadd(ws + WS_C5); }
    }
    if (w == 2 && blockIdx.x == 0) {
      gwait<4>(ws + (lane < 32 ? WS_C3 + lane * 128 : WS_C5), lane <= 32, ~0ull, tag + (lane < 32 ? 6 : 16));
      if (lane == 0) __hip_atomic_store((GAS u64*)(ws + WS_EPOCH), tag >> 16, __ATOMIC_RELAXED, __HIP_MEMORY_SCOPE_AGENT);
    }
    BAR();
  }
  STFLUSH();
}

extern "C" void kernel_launch(void* const* d_in, const int* in_sizes, int n_in, void* d_out, int out_size, void* d_ws, size_t ws_size, hipStream_t stream) {
  (void)hipFuncSetAttribute((const void*)fused, hipFuncAttributeMaxDynamicSharedMemorySize, LDS_BYTES);
  fused<<<256, 512, LDS_BYTES, stream>>>((const u16*)d_in[0], (const u16*)d_in[1], (const u16*)d_in[2], (const u16*)d_in[4], (const u16*)d_in[5],
                                         (const u16*)d_in[6], (const u16*)d_in[7], (char*)d_ws, (u16*)d_out);
}
```

```cpp
#include <hip/hip_runtime.h>
#include <stdint.h>

typedef unsigned short u16;
typedef unsigned long long u64;
typedef short s16x4 __attribute__((ext_vector_type(4)));
typedef __bf16 bf16x2 __attribute__((ext_vector_type(2)));
typedef __bf16 bf16x4 __attribute__((ext_vector_type(4)));
typedef __bf16 bf16x8 __attribute__((ext_vector_type(8)));
typedef float f32x2 __attribute__((ext_vector_type(2)));
typedef float f32x4 __attribute__((ext_vector_type(4)));
typedef unsigned u32x2 __attribute__((ext_vector_type(2)));
typedef unsigned u32x4 __attribute__((ext_vector_type(4)));
#define LAS __attribute__((address_space(3)))
#define GAS __attribute__((address_space(1)))
#define DI __device__ __forceinline__

constexpr int S = 2048, D = 512, F = 1376;
constexpr float EPS = 1e-5f;
constexpr unsigned HT_BYTES = 32u * 64u * 1024u;
constexpr unsigned G_BYTES = 32u * 43u * 1024u;
constexpr unsigned O_BYTES = 128u * 16u * 1024u;
constexpr size_t WS_EPOCH = 0;
constexpr size_t WS_C1 = 4096;
constexpr size_t WS_C2 = WS_C1 + 128 * 128;
constexpr size_t WS_C3 = WS_C2 + 256 * 128;
constexpr size_t WS_C4 = WS_C3 + 1024 * 128;
constexpr size_t WS_C5 = WS_C4 + 256 * 128;
constexpr size_t WS_STAT = WS_C5 + 1024;
constexpr size_t WS_HT = WS_STAT + 16384;
constexpr size_t WS_G = WS_HT + HT_BYTES;
constexpr size_t WS_O = WS_G + G_BYTES;
constexpr size_t WS_BASE = WS_O + O_BYTES;
constexpr int SC1 = 16;
constexpr unsigned OOB = 0x80000000u;
constexpr unsigned CODE_LEN = 34816;

typedef __amdgpu_buffer_rsrc_t rsrc_t;
DI rsrc_t mk_rsrc(const void* p, unsigned bytes) { return __builtin_amdgcn_make_buffer_rsrc((void*)p, 0, bytes, 0x00020000); }
DI u32x4 mk_srd(const void* p, unsigned bytes) {
  const uint64_t a = (uint64_t)p;
  u32x4 r = {(unsigned)a, (unsigned)(a >> 32) & 0xffffu, bytes, 0x00020000u};
  return r;
}
DI void glds16(u32x4 srd, unsigned voff, unsigned soff, unsigned lds) {
  asm volatile("s_mov_b32 m0, %3\n\ts_nop 0\n\tbuffer_load_dwordx4 %0, %1, %2 offen lds" ::"v"(voff), "s"(srd), "s"(soff), "s"(lds) : "memory");
}
DI void glds16x3(u32x4 srd, unsigned voff, unsigned soff, unsigned lds) {
  asm volatile("s_mov_b32 m0, %3\n\ts_nop 0\n\tbuffer_load_dwordx4 %0, %1, %2 offen lds\n\tbuffer_load_dwordx4 %0, %1, %2 offen offset:1024 lds\n\t"
               "buffer_load_dwordx4 %0, %1, %2 offen offset:2048 lds" ::"v"(voff), "s"(srd), "s"(soff), "s"(lds) : "memory");
}
#define WAITV(n) asm volatile("s_waitcnt vmcnt(" #n ")" ::: "memory")
#define FENCE() asm volatile("" ::: "memory")
#define SCHED() __builtin_amdgcn_sched_barrier(0)
#define DRAIN() __builtin_amdgcn_s_waitcnt(0x0F70)
#define RBAR() do { FENCE(); __builtin_amdgcn_s_barrier(); FENCE(); } while (0)
#define BAR() do { asm volatile("s_waitcnt lgkmcnt(0)" ::: "memory"); __builtin_amdgcn_s_barrier(); FENCE(); } while (0)

typedef LAS char* lp;
DI lp LP(unsigned a) { return (lp)(uintptr_t)a; }
DI bf16x8 cat8(bf16x4 a, bf16x4 b) { return __builtin_shufflevector(a, b, 0, 1, 2, 3, 4, 5, 6, 7); }
DI bf16x8 l128(lp p) { return *(const LAS bf16x8*)p; }
DI f32x4 lf4(lp p) { return *(const LAS f32x4*)p; }
DI bf16x4 ltr(lp p) { return __builtin_amdgcn_ds_read_tr16_b64_v4bf16((LAS bf16x4*)p); }
DI bf16x8 ltr2(lp p, int d) { return cat8(ltr(p), ltr(p + d)); }
DI f32x4 mfma32(bf16x8 a, bf16x8 b, f32x4 c) { return __builtin_amdgcn_mfma_f32_16x16x32_bf16(a, b, c, 0, 0, 0); }
DI void mfma_ip(f32x4& c, bf16x8 a, bf16x8 b) { asm volatile("v_mfma_f32_16x16x32_bf16 %0, %1, %2, %0" : "+v"(c) : "v"(a), "v"(b)); }
DI void mfma_k(bool first, f32x4& c, bf16x8 a, bf16x8 b) { if (first) c = mfma32(a, b, c); else mfma_ip(c, a, b); }
#define MFMA_DONE() asm volatile("s_nop 7\n\ts_nop 3" ::: "memory")
DI f32x4 mfma16(u32x2 a, u32x2 b, f32x4 c) { return __builtin_amdgcn_mfma_f32_16x16x16bf16_1k(__builtin_bit_cast(s16x4, a), __builtin_bit_cast(s16x4, b), c, 0, 0, 0); }
DI unsigned pkv(f32x2 f) { return __builtin_bit_cast(unsigned, __builtin_convertvector(f, bf16x2)); }
DI unsigned pk2(float a, float b) { f32x2 f = {a, b}; return pkv(f); }
DI f32x2 lo2(f32x4 v) { return __builtin_shufflevector(v, v, 0, 1); }
DI f32x2 hi2(f32x4 v) { return __builtin_shufflevector(v, v, 2, 3); }
DI f32x2 fma2(f32x2 a, f32x2 b, f32x2 c) { return __builtin_elementwise_fma(a, b, c); }
DI f32x2 bc2(float a) { f32x2 r = {a, a}; return r; }
DI float bflo(unsigned u) { return __uint_as_float(u << 16); }
DI float bfhi(unsigned u) { return __uint_as_float(u & 0xffff0000u); }
template <int CTRL> DI float dpp(float v) { return __uint_as_float(__builtin_amdgcn_update_dpp(0u, __float_as_uint(v), CTRL, 0xf, 0xf, true)); }
DI float sum16(float v) { v += dpp<0xB1>(v); v += dpp<0x4E>(v); v += dpp<0x141>(v); v += dpp<0x140>(v); return v; }
DI f32x2 swap16(float a, float b) { auto r = __builtin_amdgcn_permlane16_swap(__float_as_uint(a), __float_as_uint(b), false, false); f32x2 o = {__uint_as_float(r[0]), __uint_as_float(r[1])}; return o; }
DI f32x2 swap32(float a, float b) { auto r = __builtin_amdgcn_permlane32_swap(__float_as_uint(a), __float_as_uint(b), false, false); f32x2 o = {__uint_as_float(r[0]), __uint_as_float(r[1])}; return o; }
DI float sel(unsigned m, float a, float b) { return __uint_as_float((__float_as_uint(a) & m) | (__float_as_uint(b) & ~m)); }
DI u32x4 bload1(rsrc_t r, unsigned v, unsigned s) { return __builtin_amdgcn_raw_buffer_load_b128(r, v, s, SC1); }
DI void bstore1(u32x4 d, rsrc_t r, unsigned v, unsigned s) { __builtin_amdgcn_raw_buffer_store_b128(d, r, v + s, 0, SC1); }
DI bf16x8 as_bf(u32x4 v) { return __builtin_bit_cast(bf16x8, v); }
DI u64 uni64(u64 v) { return ((u64)(unsigned)__builtin_amdgcn_readfirstlane((unsigned)(v >> 32)) << 32) | (unsigned)__builtin_amdgcn_readfirstlane((unsigned)v); }

template <int N> DI u64 landed(u64 v) {
  unsigned lo, hi;
  asm volatile("s_waitcnt vmcnt(%4)\n\tv_readfirstlane_b32 %0, %2\n\tv_readfirstlane_b32 %1, %3\n\ts_nop 4" : "=s"(lo), "=s"(hi) : "v"((unsigned)v), "v"((unsigned)(v >> 32)), "i"(N) : "memory");
  return ((u64)hi << 32) | lo;
}
template <int N> DI u64 landed_v(u64 v) {
  u64 r;
  asm volatile("s_waitcnt vmcnt(%2)\n\tv_mov_b64 %0, %1" : "=&v"(r) : "v"(v), "i"(N) : "memory");
  return r;
}

constexpr unsigned LDS_BYTES = 163840;
constexpr unsigned QXB = 14336;
constexpr unsigned QST = QXB + 24576;
constexpr unsigned QSTAT = 4 * QST;
constexpr unsigned G1STG = 0;
constexpr unsigned P2STG = 49152;
constexpr unsigned SY = LDS_BYTES - 512;
enum { Y_HA = 0, Y_ST = 16, Y_QS = 32, Y_ISS = 48, Y_OK2 = 64, Y_OK4 = 80, Y_WO = 96, Y_SIG = 112, Y_N1 = 128, Y_N2 = 132, Y_N3 = 136, Y_TAG = 144 };
static_assert(QSTAT + 896 <= SY && P2STG + 98304 <= SY, "lds");
constexpr unsigned SPIN_GLB = 1u << 17;

template <int OFF> DI void sy_put(unsigned a, unsigned v) { asm volatile("ds_write_b32 %0, %1 offset:%2" ::"v"(a), "v"(v), "i"(OFF) : "memory"); }
template <int OFF> DI void sy_wait(unsigned a4, unsigned target) {
  unsigned t, n;
  asm volatile("s_mov_b32 %1, 0x200000\n"
               "1:\n\tds_read_b32 %0, %2 offset:%4\n\ts_waitcnt lgkmcnt(0)\n\tv_cmp_gt_u32 vcc, %3, %0\n\ts_cbranch_vccz 2f\n\t"
               "s_sleep 1\n\ts_sub_u32 %1, %1, 1\n\ts_cmp_lg_u32 %1, 0\n\ts_cbranch_scc1 1b\n"
               "2:" : "=&v"(t), "=&s"(n) : "v"(a4), "s"(target), "i"(OFF) : "vcc", "scc", "memory");
}
DI bool last_of(unsigned cnt, unsigned n, int lane) {
  unsigned old = 0;
  if (lane == 0) asm volatile("ds_add_rtn_u32 %0, %1, %2\n\ts_waitcnt lgkmcnt(0)" : "=&v"(old) : "v"(cnt), "v"(1u) : "memory");
  return (unsigned)__builtin_amdgcn_readfirstlane(old) == n - 1;
}
DI void gadd(char* p) { (void)__hip_atomic_fetch_add((GAS u64*)p, 1ull, __ATOMIC_RELAXED, __HIP_MEMORY_SCOPE_AGENT); }
DI u64 gpeek(const char* p) {
  u64 v = 0;
  asm volatile("global_atomic_add_x2 %0, %1, %0, off sc0\n\ts_waitcnt vmcnt(0)" : "+v"(v) : "v"(p) : "memory");
  return v;
}
template <int SL> DI void gwait(const char* p, bool on, u64 mask, u64 want) {
  FENCE();
  bool ok = !on;
#pragma unroll 1
  for (unsigned n = 0; n < SPIN_GLB; n++) {
    if (!ok) ok = (gpeek(p) & mask) == want;
    if (__all(ok)) break;
    __builtin_amdgcn_s_sleep(SL);
  }
  FENCE();
}

__global__ __launch_bounds__(512, 2) void fused(const u16* __restrict__ x, const u16* __restrict__ wqkv, const u16* __restrict__ wo, const u16* __restrict__ w1,
                                                const u16* __restrict__ b1, const u16* __restrict__ w2, const u16* __restrict__ b2, char* __restrict__ ws,
                                                u16* __restrict__ out) {
  extern __shared__ __attribute__((aligned(16))) char smem[];
  uint64_t pc0; asm volatile("s_getpc_b64 %0" : "=s"(pc0));
  const unsigned L0 = (unsigned)(uintptr_t)(LAS char*)smem;
  const int tid = threadIdx.x, lane = tid & 63;
  const int W = __builtin_amdgcn_readfirstlane(tid >> 6), w = W & 3;
  const int xcd = blockIdx.x & 7, idx = blockIdx.x >> 3;
  const int sq = xcd >> 1, hh = xcd & 1;
  const int st = sq * 8 + (idx >> 2), ct = hh * 4 + (idx & 3);
  const int gi = idx;
  const int rt = (22 * sq + gi) >> 2, hg = hh * 4 + ((22 * sq + gi) & 3);
  const int c = lane & 15, g = lane >> 4, r8 = lane >> 3, pc = lane & 7;
  const f32x4 z4 = {0.f, 0.f, 0.f, 0.f};
  const unsigned pfc = __builtin_amdgcn_raw_buffer_load_b32(mk_rsrc((const void*)(pc0 & ~255ull), CODE_LEN), ((idx & 7) * 8 + W) * 1024 + (lane & 7) * 128, 0, 0);
  {
    const unsigned on = (W >> 1) & 1;
    *(LAS unsigned*)LP(L0 + (on ? 0 : SY) + ((tid >> 5) & 3) * (on ? QST : 128) + (tid & 31) * 4) = on ? 0x3f803f80u : 0u;
  }
  const unsigned a4 = L0 + SY + (lane & 3) * 4, a1 = L0 + SY + w * 4;
  asm volatile("" ::"s"(x), "s"(wqkv), "s"(wo), "s"(w1), "s"(b1), "s"(w2), "s"(b2), "s"(ws), "s"(out));
  const u32x4 sx = mk_srd(x, S * D * 2), sw = mk_srd(wqkv, D * 3 * D * 2);
  const unsigned vx0 = (unsigned)((96 * rt - 16 + 8 * w + r8) * 1024 + ((pc ^ (((w & 1) << 2) | (r8 >> 1))) << 4));
  const unsigned vsw = (((pc >> 1) ^ (((r8 >> 1) & 1) | ((w & 1) << 1))) << 5) | ((pc & 1) << 4);
  const unsigned vwq = (8 * w + r8) * 3072 + hg * 128 + vsw;
  auto head = [&](int s) { glds16x3(sw, vwq, s * 64 * 3072, L0 + (s & 3) * QST + QXB + w * 3072); };
  auto rest = [&](int s) {
    const unsigned b = L0 + (s & 3) * QST + w * 1024;
    if (lane >= 8 || w != 0) glds16(sx, vx0, s * 128, b);
    glds16(sx, vx0 + 32768u, s * 128, b + 4096);
    glds16(sx, vx0 + 65536u, s * 128, b + 8192);
    if (w < 2) glds16(sx, vx0 + 98304u, s * 128, b + 12288);
    glds16x3(sw, vwq, s * 64 * 3072 + 32 * 3072, b + QXB + 12288 + w * 2048);
  };
  const int un = sq * 16 + hh * 8 + gi - 24;
  u32x4 xr[4];
  if (gi >= 24) {
    asm volatile("buffer_load_dwordx4 %0, %4, %5, %6 offen\n\tbuffer_load_dwordx4 %1, %4, %5, %6 offen offset:1024\n\t"
                 "buffer_load_dwordx4 %2, %4, %5, %6 offen offset:2048\n\tbuffer_load_dwordx4 %3, %4, %5, %6 offen offset:3072"
                 : "=&v"(xr[0]), "=&v"(xr[1]), "=&v"(xr[2]), "=&v"(xr[3]) : "v"(lane * 16), "s"(sx), "s"((un * 32 + 4 * W) * 1024) : "memory");
  } else {
    asm volatile("" : "=v"(xr[0]), "=v"(xr[1]), "=v"(xr[2]), "=v"(xr[3]));
    if (gi < 22 && W >= 4) { head(0); rest(0); }
  }
  BAR();
  u64 tag = 0;

  if (gi >= 24) {
    const u64 ep = __hip_atomic_load((const GAS u64*)(ws + WS_EPOCH), __ATOMIC_RELAXED, __HIP_MEMORY_SCOPE_AGENT);
    const unsigned pfw = *(const GAS unsigned*)((lane & 31) < 24 ? (const char*)wqkv + (un * 8 + W) * 3072 + (lane & 31) * 128 : (const char*)wo + (un * 8 + W) * 1024 + (lane & 7) * 128);
    WAITV(2);
    asm volatile("" : "+v"(xr[0]), "+v"(xr[1]), "+v"(xr[2]), "+v"(xr[3]));
    f32x2 v[4][4];
    float p[4], s1[4], s2[4];
    auto red4 = [&](const float (&a)[4]) {
      f32x2 t = swap16(a[0], a[1]), u = swap16(a[2], a[3]);
      t = swap32(t[0] + t[1], u[0] + u[1]);
      return sum16(t[0] + t[1]);
    };
#pragma unroll
    for (int rr = 0; rr < 4; rr++) {
      p[rr] = bflo(__builtin_amdgcn_readfirstlane(xr[rr][0]));
      f32x2 a = {0.f, 0.f}, b = {0.f, 0.f};
#pragma unroll
      for (int j = 0; j < 4; j++) { const f32x2 t = {bflo(xr[rr][j]), bfhi(xr[rr][j])}; v[rr][j] = t - bc2(p[rr]); a += v[rr][j]; b = fma2(v[rr][j], v[rr][j], b); }
      s1[rr] = a[0] + a[1]; s2[rr] = b[0] + b[1];
    }
    {
      const float m = red4(s1) * (1.f / D), var = fmaxf(red4(s2) * (1.f / D) - m * m, 0.f);
      const float r = __builtin_amdgcn_rsqf(var + EPS);
      const float a = r * __builtin_amdgcn_rsqf(var * r * r + EPS), ma = -m * a;
#pragma unroll
      for (int rr = 0; rr < 4; rr++) {
        const f32x2 av = bc2(__uint_as_float(__builtin_amdgcn_readlane(__float_as_uint(a), 16 * rr))), cv = bc2(__uint_as_float(__builtin_amdgcn_readlane(__float_as_uint(ma), 16 * rr)));
        u32x4 h;
#pragma unroll
        for (int j = 0; j < 4; j++) h[j] = pkv(fma2(v[rr][j], av, cv));
        *(LAS u32x4*)LP(L0 + (4 * W + rr) * 1088 + ((W >> 1) & 1) * 32 + lane * 16) = h;
      }
      const float pg = g == 0 ? p[0] : g == 1 ? p[1] : g == 2 ? p[2] : p[3];
      *(LAS float*)LP(L0 + QSTAT + (W >> 1) * 64 + (W & 1) * 16 + g * 4 + (lane & 1) * 32) = (lane & 1) ? (m + pg) * r : r;
    }
    if (W == 0) {
      const unsigned o = lane * 128 + (lane < 2 ? (unsigned)WS_C1 + un * 256 : (unsigned)WS_C2 - 256 + un * 3072);
      u64 e = ep;
      asm volatile("" : "+v"(e));
      if (lane < (un == 63 ? 27 : 26)) __hip_atomic_store((GAS u64*)(ws + o), (e + 1) << 16, __ATOMIC_RELAXED, __HIP_MEMORY_SCOPE_AGENT);
    }
    char* const c1p = ws + WS_C1 + (sq * 32 + (lane & 31)) * 128;
    u64 chk = 0;
    BAR();
    {
      bf16x8 tv[4];
#pragma unroll
      for (int n = 0; n < 4; n++) tv[n] = ltr2(LP(L0 + (8 * g + (c >> 2)) * 1088 + (g & 1) * 32 + W * 128 + (c & 3) * 8) + n * 32, 4 * 1088);
      const u32x4 sv = *(const LAS u32x4*)LP(L0 + QSTAT + c * 16);
      const rsrc_t rht = mk_rsrc(ws + WS_HT, HT_BYTES);
#pragma unroll
      for (int n = 0; n < 4; n++) bstore1(__builtin_bit_cast(u32x4, tv[n]), rht, lane * 16, ((4 * W + n) * 64 + un) * 1024);
      if (W == 2) bstore1(sv, mk_rsrc(ws + WS_STAT, S * 8), lane < 16 ? un * 256 + lane * 16 : OOB, 0);
    }
    if (W == 1 && lane < 32) asm volatile("global_atomic_add_x2 %0, %1, %0, off sc0" : "+v"(chk) : "v"(c1p) : "memory");
    WAITV(0);
    DRAIN();
    u64 e = ep;
    asm volatile("" : "+v"(e) : "v"(pfw));
    tag = (uni64(e) + 1) << 16;
    if (W == 1 && __any(lane < 32 && (landed_v<0>(chk) >> 16) != (tag >> 16))) gwait<2>(c1p, lane < 32, ~0xffffull, tag);
    if (last_of(L0 + SY + Y_N1, 8, lane)) { if (lane < 32) gadd(c1p); sy_put<Y_SIG>(a4, 1); }
  } else if (gi >= 22) {
    unsigned p3[3] = {0, 0, 0};
    if (W >= 4) {
#pragma unroll
      for (int n = 0; n < 3; n++) {
        const unsigned early = (n < 2 && lane < 48) ? (64 + n * 64 + ((sq * 2 + gi - 22) * 4 + w) * 2) * 3072 + lane * 128 : OOB;
        p3[n] = __builtin_amdgcn_raw_buffer_load_b32(mk_rsrc(wqkv, D * 3 * D * 2), hh ? early : ((n * 8 + (gi - 22) * 4 + w) * 64 + lane) * 1024 + 384, 0, 0);
      }
    } else if (W > 0) {
#pragma unroll
      for (int n = 0; n < 2; n++) p3[n] = __builtin_amdgcn_raw_buffer_load_b32(mk_rsrc(x, S * D * 2), (unsigned)((((22 * sq) >> 2) * 96 - 16 + (n * 6 + (gi - 22) * 3 + W - 1) * 52 + min(lane, 51)) * 1024 + 384), 0, 0);
    }
    tag = (uni64(__hip_atomic_load((const GAS u64*)(ws + WS_EPOCH), __ATOMIC_RELAXED, __HIP_MEMORY_SCOPE_AGENT)) + 1) << 16;
    if (W < 4) sy_put<Y_HA>(a1, 1);
    asm volatile("" ::"v"(p3[0]), "v"(p3[1]), "v"(p3[2]));
  }

  if (W < 4) {
    if (gi < 22) {
      f32x4 aq[7], ak[7], av[7];
#pragma unroll
      for (int b = 0; b < 7; b++) { ak[b] = z4; av[b] = z4; aq[b] = z4; }
      const unsigned ax = L0 + c * 128 + ((g ^ (c >> 1)) << 4);
      const unsigned aw = L0 + QXB + g * 3072 + (c >> 2) * 128 + ((w ^ ((c >> 3) | ((g & 1) << 1))) << 5) + (c & 3) * 8;
      bf16x8 xf[2][7];
      bf16x4 wl[2][3], wh[2][3];
      const lp pxa[2][2] = {{LP(ax), LP(ax ^ 64)}, {LP(ax + 2 * QST), LP((ax ^ 64) + 2 * QST)}}, pwa[2] = {LP(aw), LP(aw + 2 * QST)};
      auto RD = [&](int n, int i, int slot, int kk) {
        const lp px = pxa[slot >> 1][kk] + (slot & 1) * QST, pw = pwa[slot >> 1] + (slot & 1) * QST + kk * 12288;
        if (i == 0) wl[n][1] = ltr(pw + 1024);
        if (i == 1) wh[n][1] = ltr(pw + 1024 + 512);
        if (i == 2) xf[n][6] = l128(px + 12288);
        if (i == 3) wl[n][2] = ltr(pw + 2048);
        if (i == 4) wh[n][2] = ltr(pw + 2048 + 512);
        if (i == 5) wl[n][0] = ltr(pw);
        if (i == 6) wh[n][0] = ltr(pw + 512);
        if (i >= 7) xf[n][12 - i] = l128(px + (12 - i) * 2048);
      };
      auto MM = [&](int cur, int m) {
        const int t = 6 - m / 3;
        if (m % 3 == 0) mfma_ip(ak[t], cat8(wl[cur][1], wh[cur][1]), xf[cur][t]);
        if (m % 3 == 1) mfma_ip(av[t], xf[cur][t], cat8(wl[cur][2], wh[cur][2]));
        if (m % 3 == 2) mfma_ip(aq[t], cat8(wl[cur][0], wh[cur][0]), xf[cur][t]);
      };
      RBAR();
#pragma unroll
      for (int i = 0; i < 13; i++) RD(0, i, 0, 0);
      SCHED();
#pragma unroll 1
      for (int s4 = 0; s4 < 8; s4 += 4) {
#pragma unroll
        for (int sl = 0; sl < 4; sl++) {
#pragma unroll
          for (int m = 0; m < 21; m++) {
            MM(0, m); SCHED();
            if (m < 13) RD(1, m, sl, 1);
            if (m == 19 && (sl < 3 || s4 == 0)) BAR();
            SCHED();
          }
#pragma unroll
          for (int m = 0; m < 21; m++) {
            MM(1, m); SCHED();
            if (m < 13) RD(0, m, (sl + 1) & 3, 0);
            SCHED();
          }
        }
      }
      MFMA_DONE();
      SCHED();
      f32x2 cq[2], ck[2];
#pragma unroll
      for (int j = 0; j < 4; j++) { cq[j >> 1][j & 1] = -dpp<0x150>(aq[0][j]); ck[j >> 1][j & 1] = -dpp<0x150>(ak[0][j]); }
      f32x2 cvt = swap16(av[0][0], av[0][0]);
      cvt = swap32(cvt[0], cvt[0]);
      const f32x2 cv = bc2(-cvt[0]);
      sy_wait<Y_ST>(a4, 1);
      u32x2 qf[6], kf[7], vf[7];
      {
        float sr[7], sm[7]; f32x4 vr[7], vm[7];
        const lp pcs = LP(L0 + QSTAT + (c >> 3) * 64 + (c & 7) * 4), prs = LP(L0 + QSTAT + (g >> 1) * 64 + (g & 1) * 16);
#pragma unroll
        for (int t = 0; t < 7; t++) {
          sr[t] = *(const LAS float*)(pcs + t * 128); sm[t] = *(const LAS float*)(pcs + t * 128 + 32);
          vr[t] = lf4(prs + t * 128); vm[t] = lf4(prs + t * 128 + 32);
        }
        SCHED();
#pragma unroll
        for (int t = 0; t < 7; t++) {
          const f32x2 r = bc2(sr[t]), m = bc2(sm[t]);
          f32x2 y[6] = {m * ck[0], m * ck[1], m * cq[0], m * cq[1], lo2(vm[t]) * cv, hi2(vm[t]) * cv};
          SCHED();
          y[0] = fma2(r, lo2(ak[t]), y[0]); y[1] = fma2(r, hi2(ak[t]), y[1]); y[2] = fma2(r, lo2(aq[t]), y[2]); y[3] = fma2(r, hi2(aq[t]), y[3]);
          y[4] = fma2(lo2(vr[t]), lo2(av[t]), y[4]); y[5] = fma2(hi2(vr[t]), hi2(av[t]), y[5]);
          SCHED();
          kf[t][0] = pkv(y[0]); kf[t][1] = pkv(y[1]);
          if (t > 0) { qf[t - 1][0] = pkv(y[2]); qf[t - 1][1] = pkv(y[3]); }
          vf[t][0] = pkv(y[4]); vf[t][1] = pkv(y[5]);
        }
        SCHED();
      }
      u32x2 op[6];
      {
        unsigned om = 0u - (unsigned)(c & 1);
        asm("" : "+v"(om));
        const bool cur0 = 4 * g + (c & 1) <= c, cur1 = 4 * g + (c & 1) + 2 <= c;
        constexpr float SC = 0.25f * 1.4426950408889634f;
        f32x4 sp[6], su[6];
#pragma unroll
        for (int t = 0; t < 6; t++) { sp[t] = mfma16(kf[t], qf[t], z4); su[t] = mfma16(kf[t + 1], qf[t], z4); }
        float l0[6], l1[6], mx[8];
#pragma unroll
        for (int t = 0; t < 6; t++) {
          const float p0 = sel(om, sp[t][1], sp[t][0]), p1 = sel(om, sp[t][3], sp[t][2]);
          const float u0 = sel(om, su[t][1], su[t][0]), u1 = sel(om, su[t][3], su[t][2]);
          l0[t] = cur0 ? u0 : p0; l1[t] = cur1 ? u1 : p1;
          if (t == 0 && rt == 0) { l0[t] = cur0 ? l0[t] : -4e9f; l1[t] = cur1 ? l1[t] : -4e9f; }
          mx[t] = fmaxf(l0[t], l1[t]);
        }
        {
          const f32x2 a = swap16(mx[0], mx[1]), b = swap16(mx[2], mx[3]), a2 = swap16(mx[4], mx[5]);
          const float n2 = fmaxf(a2[0], a2[1]);
          const f32x2 d = swap32(fmaxf(a[0], a[1]), fmaxf(b[0], b[1])), d2 = swap32(n2, n2);
          const float m = fmaxf(d[0], d[1]) * -SC, m2 = fmaxf(d2[0], d2[1]) * -SC;
          const f32x2 e = swap32(m, m), f0 = swap16(e[0], e[0]), f1 = swap16(e[1], e[1]), f2 = swap16(m2, m2);
          mx[0] = f0[0]; mx[1] = f0[1]; mx[2] = f1[0]; mx[3] = f1[1]; mx[4] = f2[0]; mx[5] = f2[1];
        }
        const u32x4 ones = {0x3f803f80u, 0x3f803f80u, 0x3f803f80u, 0x3f803f80u};
        const unsigned keep = om ^ 0x0000ffffu;
        const unsigned k00 = cur0 ? 0u : keep, k01 = cur1 ? 0u : keep, k10 = cur0 ? keep : 0u, k11 = cur1 ? keep : 0u;
        f32x4 o[6], den[6];
#pragma unroll
        for (int t = 0; t < 6; t++) {
          const float e0 = __builtin_amdgcn_exp2f(__builtin_fmaf(l0[t], SC, mx[t])), e1 = __builtin_amdgcn_exp2f(__builtin_fmaf(l1[t], SC, mx[t]));
          const unsigned b0 = pk2(e0, e0), b1v = pk2(e1, e1);
          const u32x4 pu = {b0 & k00, b1v & k01, b0 & k10, b1v & k11}, vv = {vf[t][0], vf[t][1], vf[t + 1][0], vf[t + 1][1]};
          o[t] = mfma32(as_bf(vv), as_bf(pu), z4);
          den[t] = mfma32(as_bf(ones), as_bf(pu), z4);
        }
#pragma unroll
        for (int t = 0; t < 6; t++) {
          const f32x2 inv = bc2(__builtin_amdgcn_rcpf(den[t][0]));
          op[t][0] = pkv(lo2(o[t]) * inv); op[t][1] = pkv(hi2(o[t]) * inv);
        }
      }
      {
        const rsrc_t ro = mk_rsrc(ws + WS_O, O_BYTES);
#pragma unroll
        for (int t = 0; t < 6; t += 2) {
          const auto r0 = __builtin_amdgcn_permlane16_swap(op[t][0], op[t + 1][0], false, false);
          const auto r1 = __builtin_amdgcn_permlane16_swap(op[t][1], op[t + 1][1], false, false);
          const u32x4 v = {r0[0], r1[0], r0[1], r1[1]};
          if (t == 0 || rt < 21) bstore1(v, ro, (g & 1) * 16384 + ((g >> 1) * 16 + c) * 16, ((6 * rt + t) * 16 + 2 * hg + (w >> 1)) * 1024 + (w & 1) * 512);
        }
      }
      sy_put<Y_QS>(a1, 1);
      WAITV(0);
      if (last_of(L0 + SY + Y_N2, 4, lane) && lane < (rt < 21 ? 12 : 4)) gadd(ws + WS_C2 + (12 * rt + lane) * 128);
      { const u32x2 tg = *(const LAS u32x2*)LP(L0 + SY + Y_TAG); tag = ((u64)(unsigned)__builtin_amdgcn_readfirstlane(tg[1]) << 32) | (unsigned)__builtin_amdgcn_readfirstlane(tg[0]); }
    } else {
      const rsrc_t rw2 = mk_rsrc(w2, F * S * 2);
      if (gi >= 24) {
        sy_wait<Y_OK2>(a4, 1);
        const int m = gi - 24 + 8 * r8;
        const unsigned t = __builtin_amdgcn_raw_buffer_load_b32(rw2, m < 22 ? ((44 * w + 2 * m + (hh ^ 1)) * 8 + pc) * 4096 + sq * 1024 + 384 : OOB, 0, 0);
        asm volatile("" ::"v"(t));
      } else {
        unsigned pv[5];
#pragma unroll
        for (int n = 0; n < 5; n++) pv[n] = __builtin_amdgcn_raw_buffer_load_b32(rw2, ((44 * w + 2 * (gi - 22 + 2 * n) + hh) * 8 + r8) * 4096 + sq * 1024 + pc * 128, 0, 0);
        asm volatile("" ::"v"(pv[0]), "v"(pv[1]), "v"(pv[2]), "v"(pv[3]), "v"(pv[4]));
      }
    }
    asm volatile("" ::"v"(pfc));

    const rsrc_t rgg = mk_rsrc(ws + WS_G, G_BYTES);
    const unsigned vg = lane * 16 + (4 * ct * 43 + 11 * w) * 1024;
    const u32x4 sw2 = mk_srd(w2, F * S * 2);
    const unsigned vw2 = (352 * w + r8) * 4096 + st * 128 + ((((pc >> 1) ^ ((r8 >> 1) & 1)) << 5) | ((pc & 1) << 4));
    const unsigned lw = L0 + P2STG + w * 24576;
    const unsigned ar = lw + (8 * g + (c >> 2)) * 128 + ((((c >> 3) | ((g & 1) << 1))) << 5) + (c & 3) * 8;
    u32x4 gf[11][4];
    auto GW = [&](int k, int slot) {
#pragma unroll
      for (int j = 0; j < 4; j++) glds16(sw2, (k == 10 && w == 3) ? OOB : vw2 ^ ((j & 1) << 6), (32 * k + 8 * j) * 4096, lw + slot * 4096 + j * 1024);
    };
    auto LG1 = [&](int k, int j) { gf[k][j] = bload1(rgg, (k == 10 && w == 3) ? OOB : vg, j * 43 * 1024 + k * 1024); };
    lp pr[4];
#pragma unroll
    for (int j = 0; j < 4; j++) pr[j] = LP(ar ^ (((w + j) & 3) << 5));
    bf16x8 tw[2][4];
    bf16x8 tk[4][4];
    auto TR = [&](bf16x8 (&t)[4], int slot) {
#pragma unroll
      for (int j = 0; j < 4; j++) t[j] = ltr2(pr[j] + slot * 4096, 512);
      asm volatile("" ::"v"(t[0]), "v"(t[1]), "v"(t[2]), "v"(t[3]) : "memory");
    };
    const int srow = 64 * st + 16 * w + c;
    u32x4 xq[2];
    if (gi < 22) { sy_wait<Y_QS>(a4, 1); sy_wait<Y_ISS>(a4, 1); }
    asm volatile("buffer_load_dwordx4 %0, %2, %3, 0 offen\n\tbuffer_load_dwordx4 %1, %2, %3, 0 offen offset:64"
                 : "=&v"(xq[0]), "=&v"(xq[1]) : "v"((srow * D + 64 * ct + 16 * (g & 1) + 8 * (g >> 1)) * 2), "s"(sx) : "memory");
    const char* const c3p = ws + WS_C3 + ((st * 8 + ct) * 4 + w) * 128;
    u64 c3v = 0;
    const int s6 = -12 - 5 * w;
    if (gi < 22) {
      GW(7, 0); GW(8, 1);
      RBAR();
      GW(9, 2); GW(10, 3); GW(4, 4); GW(5, 5);
      WAITV(16); TR(tk[0], 0); TR(tk[1], 1);
      WAITV(8); TR(tk[2], 2); TR(tk[3], 3);
      RBAR();
      GW(0, 0); GW(1, 1); GW(2, 2);
      if (lane == 0) asm volatile("global_atomic_add_x2 %0, %1, %0, off sc0" : "+v"(c3v) : "v"(c3p) : "memory");
      GW(3, 3); GW(6, s6);
      WAITV(17); TR(tw[0], 0);
    } else {
      const int sc = 20 - 5 * w;
      auto TS = [&](bf16x8 (&t)[4], int k) {
#pragma unroll
        for (int j = 0; j < 4; j++) glds16(sw2, (k == 10 && w == 3) ? OOB : vw2 ^ ((j & 1) << 6), (32 * k + 8 * j) * 4096, lw + sc * 4096 + j * 1024);
        WAITV(0);
#pragma unroll
        for (int j = 0; j < 4; j++) t[j] = ltr2(pr[j] + sc * 4096, 512);
        asm volatile("" ::"v"(t[0]), "v"(t[1]), "v"(t[2]), "v"(t[3]) : "memory");
      };
      TS(tk[0], 7); TS(tk[1], 8); TS(tk[2], 9); TS(tk[3], 10); TS(tw[0], 0);
      sy_put<Y_QS>(a1, 1);
      sy_wait<Y_QS>(a4, 1);
      sy_wait<Y_ISS>(a4, 3);
      GW(1, 1); GW(2, 2); GW(3, 3); GW(4, 4);
      if (lane == 0) asm volatile("global_atomic_add_x2 %0, %1, %0, off sc0" : "+v"(c3v) : "v"(c3p) : "memory");
      GW(5, 5); GW(6, s6);
    }
    asm volatile("" : "+v"(xq[0]), "+v"(xq[1]));
    u32x2 xv[4];
#pragma unroll
    for (int i = 0; i < 2; i++) {
      const auto r0 = __builtin_amdgcn_permlane16_swap(xq[i][0], xq[i][2], false, false), r1 = __builtin_amdgcn_permlane16_swap(xq[i][1], xq[i][3], false, false);
      xv[2 * i][0] = r0[0]; xv[2 * i][1] = r1[0]; xv[2 * i + 1][0] = r0[1]; xv[2 * i + 1][1] = r1[1];
    }
    f32x4 acc[4][4];
#pragma unroll
    for (int i = 0; i < 4; i++) {
      acc[i][0] = (f32x4){bflo(xv[i][0]), bfhi(xv[i][0]), bflo(xv[i][1]), bfhi(xv[i][1])};
#pragma unroll
      for (int j = 1; j < 4; j++) acc[i][j] = z4;
      asm volatile("" : "+v"(acc[i][0]));
    }
    if (landed<8>(c3v) != tag + 6) gwait<0>(c3p, lane == 0, ~0ull, tag + 6);
    constexpr int GD = 3;
#pragma unroll
    for (int k = 0; k < GD; k++)
#pragma unroll
      for (int j = 0; j < 4; j++) LG1(k, j);
    WAITV(12);
    SCHED();
    u32x4 bv[2];
#pragma unroll
    for (int k = 0; k < 10; k++) {
      if (k + GD == 11) {
        sy_wait<Y_OK4>(a4, 1);
        const rsrc_t rb = mk_rsrc(ws + WS_BASE, S * D * 2);
#pragma unroll
        for (int i = 0; i < 2; i++) bv[i] = bload1(rb, lane * 16, ((4 * st + w) * 16 + 2 * ct + i) * 1024);
      }
#pragma unroll
      for (int i = 0; i < 4; i++) {
#pragma unroll
        for (int j = 0; j < 4; j++) {
          mfma_k(k == 0, acc[i][j], as_bf(gf[k][i]), k < 7 ? tw[k & 1][j] : tk[k < 7 ? 0 : k - 7][j]);
          if (j == 0 && k + 1 < 7) { SCHED(); tw[(k + 1) & 1][i] = ltr2(pr[i] + (k + 1 < 6 ? k + 1 : s6) * 4096, 512); SCHED(); }
        }
        SCHED();
        if (k + GD < 11) LG1(k + GD, i);
        SCHED();
      }
    }
#pragma unroll
    for (int jj = 1; jj <= 4; jj++) {
#pragma unroll
      for (int i = 0; i < 4; i++) {
        acc[i][jj & 3] = mfma32(as_bf(gf[10][i]), tk[3][jj & 3], acc[i][jj & 3]); SCHED();
        if (jj >= 2) *(LAS f32x4*)(LP(lw + lane * 16) + ((jj - 2) * 4 + i) * 1024) = acc[i][jj - 1];
        SCHED();
      }
    }
    BAR();
    f32x4 u[4];
#pragma unroll
    for (int i = 0; i < 4; i++) {
      u[i] = acc[i][0] + (f32x4){bflo(bv[i >> 1][2 * (i & 1)]), bfhi(bv[i >> 1][2 * (i & 1)]), bflo(bv[i >> 1][2 * (i & 1) + 1]), bfhi(bv[i >> 1][2 * (i & 1) + 1])};
#pragma unroll
      for (int j = 1; j < 4; j++) u[i] += lf4(LP(L0 + P2STG + ((w - j) & 3) * 24576 + lane * 16) + ((j - 1) * 4 + i) * 1024);
    }
#pragma unroll
    for (int i = 0; i < 4; i += 2) {
      const auto r0 = __builtin_amdgcn_permlane16_swap(pkv(lo2(u[i])), pkv(lo2(u[i + 1])), false, false);
      const auto r1 = __builtin_amdgcn_permlane16_swap(pkv(hi2(u[i])), pkv(hi2(u[i + 1])), false, false);
      const u32x4 o = {r0[0], r1[0], r0[1], r1[1]};
      __builtin_amdgcn_raw_buffer_store_b128(o, mk_rsrc(out, S * D * 2), (srow * D + 64 * ct + 16 * i + 16 * (g & 1) + 8 * (g >> 1)) * 2, 0, 17);
    }
  } else {
    if (gi < 22) {
      const bool c1on = lane < (w == 3 ? 4 : 1);
      const char* const c1w = ws + WS_C1 + (((w == 3 ? 0 : w) + (lane & 3)) * 32 + (blockIdx.x & 31)) * 128;
      u64 c1v = 0, ep = 0;
      head(1); rest(1);
#pragma unroll 1
      for (int s = 2; s < 8; s++) {
        head(s);
        WAITV(12); RBAR();
        rest(s);
      }
      u32x4 stv4;
      auto c1_done = [&]() {
        tag = (landed<12>(ep) + 1) << 16;
        const u64 cv = landed_v<12>(c1v);
        if (__any(c1on && cv != tag + 16)) gwait<2>(c1w, c1on, ~0ull, tag + 16);
        if (w == 3) stv4 = bload1(mk_rsrc(ws + WS_STAT, S * 8), (unsigned)((12 * rt - 2) * 64 + min(lane, 55) * 16), 0);
      };
      auto put_stats = [&]() {
        if (w == 3) {
          const u32x2 tg = {(unsigned)tag, (unsigned)(tag >> 32)};
          *(LAS u32x2*)LP(L0 + SY + Y_TAG) = tg;
          *(LAS u32x4*)LP(L0 + QSTAT + min(lane, 55) * 16) = stv4;
          sy_put<Y_ST>(a4, 1);
        }
      };
      const int ft = rt, dt = hg;
      const rsrc_t rht = mk_rsrc(ws + WS_HT, HT_BYTES);
      const u32x4 s1 = mk_srd(w1, S * F * 2);
      u32x4 fb[16][4];
      const unsigned sz = (((pc >> 1) ^ ((r8 >> 1) & 1)) << 5) | ((pc & 1) << 4), vwb = (512 * w + r8) * 2752 + ft * 128;
      const unsigned vw0 = (ft == 21 && sz >= 64) ? OOB : vwb + sz, vw1 = (ft == 21 && sz < 64) ? OOB : vwb + (sz ^ 64);
      const unsigned vb = lane * 16 + (4 * dt * 64 + 16 * w) * 1024;
      const unsigned lb = L0 + G1STG + w * 4096;
      const unsigned ar = lb + (8 * g + (c >> 2)) * 128 + ((((c >> 3) | ((g & 1) << 1))) << 5) + (c & 3) * 8;
      lp pe[4];
#pragma unroll
      for (int e = 0; e < 4; e++) pe[e] = LP(ar ^ (e << 5));
      auto GW = [&](int k, int j) { glds16(s1, (j & 1) ? vw1 : vw0, (32 * k + 8 * j) * 2752, lb + (k % 3) * 16384 + j * 1024); };
      auto LB = [&](int k, int j) { fb[k][j] = bload1(rht, vb, ((w + j) & 3) * 65536 + k * 1024); };
      u32x2 bb[4];
      asm volatile("global_load_dwordx2 %0, %1, off sc1" : "=v"(ep) : "v"(ws + WS_EPOCH) : "memory");
      if (c1on) asm volatile("global_atomic_add_x2 %0, %1, %0, off sc0" : "+v"(c1v) : "v"(c1w) : "memory");
      GW(0, 0); GW(0, 1);
      WAITV(13); RBAR();
      GW(0, 2); GW(0, 3); GW(1, 0); GW(1, 1); GW(1, 2); GW(1, 3);
      WAITV(10); RBAR();
      {
        const rsrc_t rb1 = mk_rsrc(b1, F * 2);
#pragma unroll
        for (int i = 0; i < 4; i++) bb[i] = __builtin_amdgcn_raw_buffer_load_b64(rb1, (64 * ft + 4 * g) * 2 + i * 32, 0, 0);
      }
      c1_done();
      LB(0, 0); LB(0, 1); LB(0, 2); LB(0, 3);
      GW(2, 0); GW(2, 1); GW(2, 2); GW(2, 3);
      LB(1, 0); LB(1, 1); LB(1, 2); LB(1, 3);
      put_stats();
      LB(2, 0); LB(2, 1); LB(2, 2); LB(2, 3);
      LB(3, 0); LB(3, 1); LB(3, 2); LB(3, 3);
      WAITV(28);
      f32x4 acc[4][4];
#pragma unroll
      for (int i = 0; i < 4; i++) {
        acc[i][0] = (f32x4){bflo(bb[i][0]), bfhi(bb[i][0]), bflo(bb[i][1]), bfhi(bb[i][1])};
#pragma unroll
        for (int j = 1; j < 4; j++) acc[i][j] = z4;
      }
      bf16x4 al[2][4], ah[2][4];
#pragma unroll
      for (int e = 0; e < 4; e++) { al[0][e] = ltr(pe[e]); ah[0][e] = ltr(pe[e] + 512); }
      SCHED();
#pragma unroll
      for (int k = 0; k < 15; k++) {
#pragma unroll
        for (int m = 0; m < 16; m++) {
          const int i = m & 3, j = m >> 2;
          mfma_k(k == 0, acc[i][j], cat8(al[k & 1][i], ah[k & 1][i]), as_bf(fb[k][j]));
          SCHED();
          if (m == 5) {
            if (k == 0) WAITV(24); else if (k == 1) WAITV(20); else if (k < 13) WAITV(12); else if (k == 13) WAITV(8); else WAITV(0);
          }
          if (m >= 6 && m < 14) {
            const int e = (m - 6) >> 1;
            if (m & 1) ah[(k + 1) & 1][e] = ltr(pe[e] + ((k + 1) % 3) * 16384 + 512); else al[(k + 1) & 1][e] = ltr(pe[e] + ((k + 1) % 3) * 16384);
          }
          if (m == 15 && k + 3 < 16) { GW(k + 3, 0); GW(k + 3, 1); GW(k + 3, 2); GW(k + 3, 3); }
          if (m == 15 && k + 4 < 16) { LB(k + 4, 0); LB(k + 4, 1); LB(k + 4, 2); LB(k + 4, 3); }
          SCHED();
        }
        if (k == 12) sy_put<Y_ISS>(a1, 1);
      }
#pragma unroll
      for (int jj = 1; jj <= 4; jj++) {
#pragma unroll
        for (int i = 0; i < 4; i++) {
          acc[i][jj & 3] = mfma32(cat8(al[1][i], ah[1][i]), as_bf(fb[15][jj & 3]), acc[i][jj & 3]); SCHED();
          if (jj >= 2) *(LAS f32x4*)(LP(lb + lane * 16) + (jj - 2) * 16384 + i * 1024) = acc[i][jj - 1];
          SCHED();
        }
      }
      __builtin_amdgcn_s_setprio(3);
      const lp pin[3] = {LP(L0 + G1STG + ((w - 1) & 3) * 4096 + lane * 16), LP(L0 + G1STG + 16384 + ((w - 2) & 3) * 4096 + lane * 16), LP(L0 + G1STG + 32768 + ((w - 3) & 3) * 4096 + lane * 16)};
      BAR();
      f32x4 pt[4][3];
#pragma unroll
      for (int i = 0; i < 4; i++)
#pragma unroll
        for (int j = 0; j < 3; j++) pt[i][j] = lf4(pin[j] + i * 1024);
      SCHED();
      u32x2 gp[4];
#pragma unroll
      for (int i = 0; i < 4; i++) {
        const f32x4 u = (acc[i][0] + pt[i][0]) + (pt[i][1] + pt[i][2]);
#pragma unroll
        for (int hf = 0; hf < 2; hf++) {
          constexpr float K1 = -2.f * 0.7978845608028654f * 1.4426950408889634f, K2 = K1 * 0.044715f;
          const f32x2 t = hf ? hi2(u) : lo2(u);
          const f32x2 z = t * fma2(t * t, bc2(K2), bc2(K1));
          f32x2 e = {__builtin_amdgcn_exp2f(z[0]), __builtin_amdgcn_exp2f(z[1])};
          e += bc2(1.f);
          const f32x2 rc = {__builtin_amdgcn_rcpf(e[0]), __builtin_amdgcn_rcpf(e[1])};
          gp[i][hf] = pkv(t * rc);
        }
      }
      const rsrc_t rgg = mk_rsrc(ws + WS_G, G_BYTES);
#pragma unroll
      for (int i = 0; i < 4; i += 2) {
        const auto r0 = __builtin_amdgcn_permlane16_swap(gp[i][0], gp[i + 1][0], false, false);
        const auto r1v = __builtin_amdgcn_permlane16_swap(gp[i][1], gp[i + 1][1], false, false);
        const u32x4 v = {r0[0], r1v[0], r0[1], r1v[1]};
        if (i == 0 || ft < 21) bstore1(v, rgg, (2 * (g & 1) + (g >> 1)) * 256 + c * 16, ((4 * dt + w) * 43 + 2 * ft + (i >> 1)) * 1024);
      }
      WAITV(0);
      DRAIN();
      {
        const int sl0 = (2 * ft) / 11, sl1 = (2 * ft + 1) / 11;
        char* const c3a = ws + WS_C3 + (((lane & 31) * 8 + dt) * 4 + (lane < 32 ? sl0 : sl1)) * 128;
        RBAR();
        if ((lane >> 4) == w && (lane < 32 || sl1 != sl0)) gadd(c3a);
      }
    } else {
      if (gi >= 24) {
        unsigned pv[8];
        sy_wait<Y_SIG>(a4, 1);
        {
          const rsrc_t r1 = mk_rsrc(w1, S * F * 2);
          const unsigned c0 = ((22 * sq) >> 2) * 128;
#pragma unroll
          for (int n = 0; n < 8; n++) {
            const unsigned row = w * 512 + (2 * (gi - 24) + 16 * (n >> 1) + ((n & 1) ^ hh)) * 8 + r8;
            pv[n] = __builtin_amdgcn_raw_buffer_load_b32(r1, pc < 6 + (r8 & 1) ? ((row * 2752 + c0) & ~127u) + pc * 128 : OOB, 0, 0);
            if (n >= 2 && n < 7) WAITV(0);
          }
        }
        const int so = sq * 4 + ((gi - 24) >> 1), dq = hh * 2 + (gi & 1);
        const u32x4 swo = mk_srd(wo, D * D * 2);
        const unsigned vwo = (8 * w + r8) * 1024 + dq * 256 + vsw;
        const rsrc_t ro = mk_rsrc(ws + WS_O, O_BYTES), rb = mk_rsrc(ws + WS_BASE, S * D * 2);
        const int srow = 128 * so + 32 * w + c;
        unsigned bi0 = b2[srow], bi1 = b2[srow + 16];
        asm volatile("" : "+v"(bi0), "+v"(bi1) : "v"(pv[0]), "v"(pv[1]), "v"(pv[2]), "v"(pv[3]), "v"(pv[4]), "v"(pv[5]), "v"(pv[6]), "v"(pv[7]));
        const float bias0 = bflo(bi0), bias1 = bflo(bi1);
        const char* const c2p = ws + WS_C2 + ((so * 4 + w) * 4 + dq) * 128;
        u64 c2v = 0;
#pragma unroll 1
        for (int n = 0; n < 16; n++) {
          if (n == 11 && lane == 0) asm volatile("global_atomic_add_x2 %0, %1, %0, off sc0" : "+v"(c2v) : "v"(c2p) : "memory");
          glds16(swo, vwo, n * 32768, L0 + (n * 4 + w) * 1024);
          glds16(swo, vwo, n * 32768 + 128, L0 + 65536 + (n * 4 + w) * 1024);
        }
        if (landed<10>(c2v) != tag + 8) {
          sy_put<Y_OK2>(a1, 1);
          gwait<2>(c2p, lane == 0, ~0ull, tag + 8);
        }
        u32x4 of[16][2];
        const unsigned vo = lane * 16 + (8 * so + 2 * w) * 16384;
        constexpr int OD = 8;
#pragma unroll
        for (int k = 0; k < OD; k++) { of[k][0] = bload1(ro, vo, k * 1024); of[k][1] = bload1(ro, vo, 16384 + k * 1024); }
        WAITV(16);
        sy_put<Y_WO>(a1, 1);
        sy_put<Y_OK2>(a1, 1);
        sy_wait<Y_WO>(a4, 1);
        const unsigned ar = L0 + (8 * g + (c >> 2)) * 128 + ((((c >> 3) | ((g & 1) << 1))) << 5) + (c & 3) * 8;
        lp pe[8];
#pragma unroll
        for (int e = 0; e < 8; e++) pe[e] = LP((ar ^ ((e & 3) << 5)) + (e >> 2) * 65536);
        f32x4 acc[8][2];
#pragma unroll
        for (int i = 0; i < 8; i++) { acc[i][0] = (f32x4){bias0, bias0, bias0, bias0}; acc[i][1] = (f32x4){bias1, bias1, bias1, bias1}; }
        bf16x4 tl[2][8], th[2][8];
#pragma unroll
        for (int e = 0; e < 8; e++) { tl[0][e] = ltr(pe[e]); th[0][e] = ltr(pe[e] + 512); }
        SCHED();
#pragma unroll
        for (int k = 0; k < 16; k++) {
#pragma unroll
          for (int e = 0; e < 8; e++) {
            mfma_k(k == 0, acc[e][0], cat8(tl[k & 1][e], th[k & 1][e]), as_bf(of[k][0])); SCHED();
            if (k < 15) tl[(k + 1) & 1][e] = ltr(pe[e] + (k + 1) * 4096);
            SCHED();
            mfma_k(k == 0, acc[e][1], cat8(tl[k & 1][e], th[k & 1][e]), as_bf(of[k][1])); SCHED();
            if (k < 15) th[(k + 1) & 1][e] = ltr(pe[e] + (k + 1) * 4096 + 512);
            SCHED();
          }
          if (k + OD < 16) { of[k + OD][0] = bload1(ro, vo, (k + OD) * 1024); of[k + OD][1] = bload1(ro, vo, 16384 + (k + OD) * 1024); }
          if (k == 14) { FENCE(); sy_put<Y_ISS>(a1, 3); __builtin_amdgcn_s_setprio(3); }
        }
        MFMA_DONE();
        SCHED();
#pragma unroll
        for (int j = 0; j < 2; j++)
#pragma unroll
          for (int i = 0; i < 4; i++) {
            const u32x4 o = {pkv(lo2(acc[2 * i][j])), pkv(hi2(acc[2 * i][j])), pkv(lo2(acc[2 * i + 1][j])), pkv(hi2(acc[2 * i + 1][j]))};
            bstore1(o, rb, lane * 16, ((8 * so + 2 * w + j) * 16 + 4 * dq + i) * 1024);
          }
        WAITV(0);
        if (last_of(L0 + SY + Y_N3, 4, lane) && lane < 4) gadd(ws + WS_C4 + ((2 * so + (lane >> 1)) * 8 + 2 * dq + (lane & 1)) * 128);
      } else {
        const rsrc_t rw2 = mk_rsrc(w2, F * S * 2);
        unsigned pv[6];
#pragma unroll
        for (int n = 0; n < 6; n++) pv[n] = __builtin_amdgcn_raw_buffer_load_b32(rw2, ((44 * w + 2 * (10 + gi - 22 + 2 * n) + hh) * 8 + r8) * 4096 + sq * 1024 + pc * 128, 0, 0);
        asm volatile("" ::"v"(pv[0]), "v"(pv[1]), "v"(pv[2]), "v"(pv[3]), "v"(pv[4]), "v"(pv[5]));
        sy_put<Y_ISS>(a1, 3);
        __builtin_amdgcn_s_sleep(64);
      }
      DRAIN();
    }
    asm volatile("" ::"v"(pfc));
    if (w == 1) {
      gwait<4>(ws + WS_C4 + (st * 8 + ct) * 128, lane == 0, ~0ull, tag + 1);
      sy_put<Y_OK4>(a4, 1);
      if (gi == 22 || gi == 23) { sy_wait<Y_HA>(a4, 1); sy_wait<Y_ISS>(a4, 3); if (lane == 0) gadd(ws + WS_C5); }
    }
    if (w == 2 && blockIdx.x == 0) {
      gwait<4>(ws + (lane < 32 ? WS_C3 + lane * 128 : WS_C5), lane <= 32, ~0ull, tag + (lane < 32 ? 6 : 16));
      if (lane == 0) __hip_atomic_store((GAS u64*)(ws + WS_EPOCH), tag >> 16, __ATOMIC_RELAXED, __HIP_MEMORY_SCOPE_AGENT);
    }
    BAR();
  }
}

extern "C" void kernel_launch(void* const* d_in, const int* in_sizes, int n_in, void* d_out, int out_size, void* d_ws, size_t ws_size, hipStream_t stream) {
  (void)hipFuncSetAttribute((const void*)fused, hipFuncAttributeMaxDynamicSharedMemorySize, LDS_BYTES);
  fused<<<256, 512, LDS_BYTES, stream>>>((const u16*)d_in[0], (const u16*)d_in[1], (const u16*)d_in[2], (const u16*)d_in[4], (const u16*)d_in[5],
                                         (const u16*)d_in[6], (const u16*)d_in[7], (char*)d_ws, (u16*)d_out);
}
```

```cpp
#include <hip/hip_runtime.h>
#include <stdint.h>

typedef unsigned short u16;
typedef unsigned long long u64;
typedef short s16x4 __attribute__((ext_vector_type(4)));
typedef __bf16 bf16x2 __attribute__((ext_vector_type(2)));
typedef __bf16 bf16x4 __attribute__((ext_vector_type(4)));
typedef __bf16 bf16x8 __attribute__((ext_vector_type(8)));
typedef float f32x2 __attribute__((ext_vector_type(2)));
typedef float f32x4 __attribute__((ext_vector_type(4)));
typedef unsigned u32x2 __attribute__((ext_vector_type(2)));
typedef unsigned u32x4 __attribute__((ext_vector_type(4)));
#define LAS __attribute__((address_space(3)))
#define GAS __attribute__((address_space(1)))
#define DI __device__ __forceinline__

constexpr int S = 2048, D = 512, F = 1376;
constexpr float EPS = 1e-5f;
constexpr unsigned HT_BYTES = 32u * 64u * 1024u;
constexpr unsigned G_BYTES = 32u * 43u * 1024u;
constexpr unsigned O_BYTES = 128u * 16u * 1024u;
constexpr size_t WS_EPOCH = 0;
constexpr size_t WS_C1 = 4096;
constexpr size_t WS_C2 = WS_C1 + 128 * 128;
constexpr size_t WS_C3 = WS_C2 + 256 * 128;
constexpr size_t WS_C4 = WS_C3 + 1024 * 128;
constexpr size_t WS_C5 = WS_C4 + 256 * 128;
constexpr size_t WS_STAT = WS_C5 + 1024;
constexpr size_t WS_HT = WS_STAT + 16384;
constexpr size_t WS_G = WS_HT + HT_BYTES;
constexpr size_t WS_O = WS_G + G_BYTES;
constexpr size_t WS_BASE = WS_O + O_BYTES;
constexpr int SC1 = 16;
constexpr unsigned OOB = 0x80000000u;
constexpr unsigned CODE_LEN = 34816;

typedef __amdgpu_buffer_rsrc_t rsrc_t;
DI rsrc_t mk_rsrc(const void* p, unsigned bytes) { return __builtin_amdgcn_make_buffer_rsrc((void*)p, 0, bytes, 0x00020000); }
DI u32x4 mk_srd(const void* p, unsigned bytes) {
  const uint64_t a = (uint64_t)p;
  u32x4 r = {(unsigned)a, (unsigned)(a >> 32) & 0xffffu, bytes, 0x00020000u};
  return r;
}
DI void glds16(u32x4 srd, unsigned voff, unsigned soff, unsigned lds) {
  asm volatile("s_mov_b32 m0, %3\n\ts_nop 0\n\tbuffer_load_dwordx4 %0, %1, %2 offen lds" ::"v"(voff), "s"(srd), "s"(soff), "s"(lds) : "memory");
}
DI void glds16x3(u32x4 srd, unsigned voff, unsigned soff, unsigned lds) {
  asm volatile("s_mov_b32 m0, %3\n\ts_nop 0\n\tbuffer_load_dwordx4 %0, %1, %2 offen lds\n\tbuffer_load_dwordx4 %0, %1, %2 offen offset:1024 lds\n\t"
               "buffer_load_dwordx4 %0, %1, %2 offen offset:2048 lds" ::"v"(voff), "s"(srd), "s"(soff), "s"(lds) : "memory");
}
#define WAITV(n) asm volatile("s_waitcnt vmcnt(" #n ")" ::: "memory")
#define FENCE() asm volatile("" ::: "memory")
#define SCHED() __builtin_amdgcn_sched_barrier(0)
#define DRAIN() __builtin_amdgcn_s_waitcnt(0x0F70)
#define RBAR() do { FENCE(); __builtin_amdgcn_s_barrier(); FENCE(); } while (0)
#define BAR() do { asm volatile("s_waitcnt lgkmcnt(0)" ::: "memory"); __builtin_amdgcn_s_barrier(); FENCE(); } while (0)

typedef LAS char* lp;
DI lp LP(unsigned a) { return (lp)(uintptr_t)a; }
DI bf16x8 cat8(bf16x4 a, bf16x4 b) { return __builtin_shufflevector(a, b, 0, 1, 2, 3, 4, 5, 6, 7); }
DI bf16x8 l128(lp p) { return *(const LAS bf16x8*)p; }
DI f32x4 lf4(lp p) { return *(const LAS f32x4*)p; }
DI bf16x4 ltr(lp p) { return __builtin_amdgcn_ds_read_tr16_b64_v4bf16((LAS bf16x4*)p); }
DI bf16x8 ltr2(lp p, int d) { return cat8(ltr(p), ltr(p + d)); }
DI f32x4 mfma32(bf16x8 a, bf16x8 b, f32x4 c) { return __builtin_amdgcn_mfma_f32_16x16x32_bf16(a, b, c, 0, 0, 0); }
DI void mfma_ip(f32x4& c, bf16x8 a, bf16x8 b) { asm volatile("v_mfma_f32_16x16x32_bf16 %0, %1, %2, %0" : "+v"(c) : "v"(a), "v"(b)); }
DI void mfma_k(bool first, f32x4& c, bf16x8 a, bf16x8 b) { if (first) c = mfma32(a, b, c); else mfma_ip(c, a, b); }
#define MFMA_DONE() asm volatile("s_nop 7\n\ts_nop 3" ::: "memory")
DI f32x4 mfma16(u32x2 a, u32x2 b, f32x4 c) { return __builtin_amdgcn_mfma_f32_16x16x16bf16_1k(__builtin_bit_cast(s16x4, a), __builtin_bit_cast(s16x4, b), c, 0, 0, 0); }
DI unsigned pkv(f32x2 f) { return __builtin_bit_cast(unsigned, __builtin_convertvector(f, bf16x2)); }
DI unsigned pk2(float a, float b) { f32x2 f = {a, b}; return pkv(f); }
DI f32x2 lo2(f32x4 v) { return __builtin_shufflevector(v, v, 0, 1); }
DI f32x2 hi2(f32x4 v) { return __builtin_shufflevector(v, v, 2, 3); }
DI f32x2 fma2(f32x2 a, f32x2 b, f32x2 c) { return __builtin_elementwise_fma(a, b, c); }
DI f32x2 bc2(float a) { f32x2 r = {a, a}; return r; }
DI float bflo(unsigned u) { return __uint_as_float(u << 16); }
DI float bfhi(unsigned u) { return __uint_as_float(u & 0xffff0000u); }
template <int CTRL> DI float dpp(float v) { return __uint_as_float(__builtin_amdgcn_update_dpp(0u, __float_as_uint(v), CTRL, 0xf, 0xf, true)); }
DI float sum16(float v) { v += dpp<0xB1>(v); v += dpp<0x4E>(v); v += dpp<0x141>(v); v += dpp<0x140>(v); return v; }
DI f32x2 swap16(float a, float b) { auto r = __builtin_amdgcn_permlane16_swap(__float_as_uint(a), __float_as_uint(b), false, false); f32x2 o = {__uint_as_float(r[0]), __uint_as_float(r[1])}; return o; }
DI f32x2 swap32(float a, float b) { auto r = __builtin_amdgcn_permlane32_swap(__float_as_uint(a), __float_as_uint(b), false, false); f32x2 o = {__uint_as_float(r[0]), __uint_as_float(r[1])}; return o; }
DI float sel(unsigned m, float a, float b) { return __uint_as_float((__float_as_uint(a) & m) | (__float_as_uint(b) & ~m)); }
DI u32x4 bload1(rsrc_t r, unsigned v, unsigned s) { return __builtin_amdgcn_raw_buffer_load_b128(r, v, s, SC1); }
DI void bstore1(u32x4 d, rsrc_t r, unsigned v, unsigned s) { __builtin_amdgcn_raw_buffer_store_b128(d, r, v + s, 0, SC1); }
DI bf16x8 as_bf(u32x4 v) { return __builtin_bit_cast(bf16x8, v); }
DI u64 uni64(u64 v) { return ((u64)(unsigned)__builtin_amdgcn_readfirstlane((unsigned)(v >> 32)) << 32) | (unsigned)__builtin_amdgcn_readfirstlane((unsigned)v); }

template <int N> DI u64 landed(u64 v) {
  unsigned lo, hi;
  asm volatile("s_waitcnt vmcnt(%4)\n\tv_readfirstlane_b32 %0, %2\n\tv_readfirstlane_b32 %1, %3\n\ts_nop 4" : "=s"(lo), "=s"(hi) : "v"((unsigned)v), "v"((unsigned)(v >> 32)), "i"(N) : "memory");
  return ((u64)hi << 32) | lo;
}
template <int N> DI u64 landed_v(u64 v) {
  u64 r;
  asm volatile("s_waitcnt vmcnt(%2)\n\tv_mov_b64 %0, %1" : "=&v"(r) : "v"(v), "i"(N) : "memory");
  return r;
}

constexpr unsigned LDS_BYTES = 163840;
constexpr unsigned QXB = 14336;
constexpr unsigned QST = QXB + 24576;
constexpr unsigned QSTAT = 4 * QST;
constexpr unsigned G1STG = 0;
constexpr unsigned P2STG = 49152;
constexpr unsigned SY = LDS_BYTES - 512;
enum { Y_HA = 0, Y_ST = 16, Y_QS = 32, Y_ISS = 48, Y_OK2 = 64, Y_OK4 = 80, Y_WO = 96, Y_SIG = 112, Y_N1 = 128, Y_N2 = 132, Y_N3 = 136, Y_TAG = 144 };
static_assert(QSTAT + 896 <= SY && P2STG + 98304 <= SY, "lds");
constexpr unsigned SPIN_GLB = 1u << 17;

template <int OFF> DI void sy_put(unsigned a, unsigned v) { asm volatile("ds_write_b32 %0, %1 offset:%2" ::"v"(a), "v"(v), "i"(OFF) : "memory"); }
template <int OFF> DI void sy_wait(unsigned a4, unsigned target) {
  unsigned t, n;
  asm volatile("s_mov_b32 %1, 0x200000\n"
               "1:\n\tds_read_b32 %0, %2 offset:%4\n\ts_waitcnt lgkmcnt(0)\n\tv_cmp_gt_u32 vcc, %3, %0\n\ts_cbranch_vccz 2f\n\t"
               "s_sleep 1\n\ts_sub_u32 %1, %1, 1\n\ts_cmp_lg_u32 %1, 0\n\ts_cbranch_scc1 1b\n"
               "2:" : "=&v"(t), "=&s"(n) : "v"(a4), "s"(target), "i"(OFF) : "vcc", "scc", "memory");
}
DI bool last_of(unsigned cnt, unsigned n, int lane) {
  unsigned old = 0;
  if (lane == 0) asm volatile("ds_add_rtn_u32 %0, %1, %2\n\ts_waitcnt lgkmcnt(0)" : "=&v"(old) : "v"(cnt), "v"(1u) : "memory");
  return (unsigned)__builtin_amdgcn_readfirstlane(old) == n - 1;
}
DI void gadd(char* p) { (void)__hip_atomic_fetch_add((GAS u64*)p, 1ull, __ATOMIC_RELAXED, __HIP_MEMORY_SCOPE_AGENT); }
DI u64 gpeek(const char* p) {
  u64 v = 0;
  asm volatile("global_atomic_add_x2 %0, %1, %0, off sc0\n\ts_waitcnt vmcnt(0)" : "+v"(v) : "v"(p) : "memory");
  return v;
}
template <int SL> DI void gwait(const char* p, bool on, u64 mask, u64 want) {
  FENCE();
  bool ok = !on;
#pragma unroll 1
  for (unsigned n = 0; n < SPIN_GLB; n++) {
    if (!ok) ok = (gpeek(p) & mask) == want;
    if (__all(ok)) break;
    __builtin_amdgcn_s_sleep(SL);
  }
  FENCE();
}

__global__ __launch_bounds__(512, 2) void fused(const u16* __restrict__ x, const u16* __restrict__ wqkv, const u16* __restrict__ wo, const u16* __restrict__ w1,
                                                const u16* __restrict__ b1, const u16* __restrict__ w2, const u16* __restrict__ b2, char* __restrict__ ws,
                                                u16* __restrict__ out) {
  extern __shared__ __attribute__((aligned(16))) char smem[];
  uint64_t pc0; asm volatile("s_getpc_b64 %0" : "=s"(pc0));
  const unsigned L0 = (unsigned)(uintptr_t)(LAS char*)smem;
  const int tid = threadIdx.x, lane = tid & 63;
  const int W = __builtin_amdgcn_readfirstlane(tid >> 6), w = W & 3;
  const int xcd = blockIdx.x & 7, idx = blockIdx.x >> 3;
  const int sq = xcd >> 1, hh = xcd & 1;
  const int st = sq * 8 + (idx >> 2), ct = hh * 4 + (idx & 3);
  const int gi = idx;
  const int rt = (22 * sq + gi) >> 2, hg = hh * 4 + ((22 * sq + gi) & 3);
  const int c = lane & 15, g = lane >> 4, r8 = lane >> 3, pc = lane & 7;
  const f32x4 z4 = {0.f, 0.f, 0.f, 0.f};
  const unsigned pfc = __builtin_amdgcn_raw_buffer_load_b32(mk_rsrc((const void*)(pc0 & ~255ull), CODE_LEN), ((idx & 7) * 8 + W) * 1024 + (lane & 7) * 128, 0, 0);
  {
    const unsigned on = (W >> 1) & 1;
    *(LAS unsigned*)LP(L0 + (on ? 0 : SY) + ((tid >> 5) & 3) * (on ? QST : 128) + (tid & 31) * 4) = on ? 0x3f803f80u : 0u;
  }
  const unsigned a4 = L0 + SY + (lane & 3) * 4, a1 = L0 + SY + w * 4;
  asm volatile("" ::"s"(x), "s"(wqkv), "s"(wo), "s"(w1), "s"(b1), "s"(w2), "s"(b2), "s"(ws), "s"(out));
  const u32x4 sx = mk_srd(x, S * D * 2), sw = mk_srd(wqkv, D * 3 * D * 2);
  const unsigned vx0 = (unsigned)((96 * rt - 16 + 8 * w + r8) * 1024 + ((pc ^ (((w & 1) << 2) | (r8 >> 1))) << 4));
  const unsigned vsw = (((pc >> 1) ^ (((r8 >> 1) & 1) | ((w & 1) << 1))) << 5) | ((pc & 1) << 4);
  const unsigned vwq = (8 * w + r8) * 3072 + hg * 128 + vsw;
  auto head = [&](int s) { glds16x3(sw, vwq, s * 64 * 3072, L0 + (s & 3) * QST + QXB + w * 3072); };
  auto rest = [&](int s) {
    const unsigned b = L0 + (s & 3) * QST + w * 1024;
    if (lane >= 8 || w != 0) glds16(sx, vx0, s * 128, b);
    glds16(sx, vx0 + 32768u, s * 128, b + 4096);
    glds16(sx, vx0 + 65536u, s * 128, b + 8192);
    if (w < 2) glds16(sx, vx0 + 98304u, s * 128, b + 12288);
    glds16x3(sw, vwq, s * 64 * 3072 + 32 * 3072, b + QXB + 12288 + w * 2048);
  };
  const int un = sq * 16 + hh * 8 + gi - 24;
  u32x4 xr[4];
  if (gi >= 24) {
    asm volatile("buffer_load_dwordx4 %0, %4, %5, %6 offen\n\tbuffer_load_dwordx4 %1, %4, %5, %6 offen offset:1024\n\t"
                 "buffer_load_dwordx4 %2, %4, %5, %6 offen offset:2048\n\tbuffer_load_dwordx4 %3, %4, %5, %6 offen offset:3072"
                 : "=&v"(xr[0]), "=&v"(xr[1]), "=&v"(xr[2]), "=&v"(xr[3]) : "v"(lane * 16), "s"(sx), "s"((un * 32 + 4 * W) * 1024) : "memory");
  } else {
    asm volatile("" : "=v"(xr[0]), "=v"(xr[1]), "=v"(xr[2]), "=v"(xr[3]));
    if (gi < 22 && W >= 4) { head(0); rest(0); }
  }
  BAR();
  u64 tag = 0;

  if (gi >= 24) {
    const u64 ep = __hip_atomic_load((const GAS u64*)(ws + WS_EPOCH), __ATOMIC_RELAXED, __HIP_MEMORY_SCOPE_AGENT);
    const unsigned pfw = *(const GAS unsigned*)((lane & 31) < 24 ? (const char*)wqkv + (un * 8 + W) * 3072 + (lane & 31) * 128 : (const char*)wo + (un * 8 + W) * 1024 + (lane & 7) * 128);
    WAITV(2);
    asm volatile("" : "+v"(xr[0]), "+v"(xr[1]), "+v"(xr[2]), "+v"(xr[3]));
    f32x2 v[4][4];
    float p[4], s1[4], s2[4];
    auto red4 = [&](const float (&a)[4]) {
      f32x2 t = swap16(a[0], a[1]), u = swap16(a[2], a[3]);
      t = swap32(t[0] + t[1], u[0] + u[1]);
      return sum16(t[0] + t[1]);
    };
#pragma unroll
    for (int rr = 0; rr < 4; rr++) {
      p[rr] = bflo(__builtin_amdgcn_readfirstlane(xr[rr][0]));
      f32x2 a = {0.f, 0.f}, b = {0.f, 0.f};
#pragma unroll
      for (int j = 0; j < 4; j++) { const f32x2 t = {bflo(xr[rr][j]), bfhi(xr[rr][j])}; v[rr][j] = t - bc2(p[rr]); a += v[rr][j]; b = fma2(v[rr][j], v[rr][j], b); }
      s1[rr] = a[0] + a[1]; s2[rr] = b[0] + b[1];
    }
    {
      const float m = red4(s1) * (1.f / D), var = fmaxf(red4(s2) * (1.f / D) - m * m, 0.f);
      const float r = __builtin_amdgcn_rsqf(var + EPS);
      const float a = r * __builtin_amdgcn_rsqf(var * r * r + EPS), ma = -m * a;
#pragma unroll
      for (int rr = 0; rr < 4; rr++) {
        const f32x2 av = bc2(__uint_as_float(__builtin_amdgcn_readlane(__float_as_uint(a), 16 * rr))), cv = bc2(__uint_as_float(__builtin_amdgcn_readlane(__float_as_uint(ma), 16 * rr)));
        u32x4 h;
#pragma unroll
        for (int j = 0; j < 4; j++) h[j] = pkv(fma2(v[rr][j], av, cv));
        *(LAS u32x4*)LP(L0 + (4 * W + rr) * 1088 + ((W >> 1) & 1) * 32 + lane * 16) = h;
      }
      const float pg = g == 0 ? p[0] : g == 1 ? p[1] : g == 2 ? p[2] : p[3];
      *(LAS float*)LP(L0 + QSTAT + (W >> 1) * 64 + (W & 1) * 16 + g * 4 + (lane & 1) * 32) = (lane & 1) ? (m + pg) * r : r;
    }
    if (W == 0) {
      const unsigned o = lane * 128 + (lane < 2 ? (unsigned)WS_C1 + un * 256 : (unsigned)WS_C2 - 256 + un * 3072);
      u64 e = ep;
      asm volatile("" : "+v"(e));
      if (lane < (un == 63 ? 27 : 26)) __hip_atomic_store((GAS u64*)(ws + o), (e + 1) << 16, __ATOMIC_RELAXED, __HIP_MEMORY_SCOPE_AGENT);
    }
    char* const c1p = ws + WS_C1 + (sq * 32 + (lane & 31)) * 128;
    u64 chk = 0;
    BAR();
    {
      bf16x8 tv[4];
#pragma unroll
      for (int n = 0; n < 4; n++) tv[n] = ltr2(LP(L0 + (8 * g + (c >> 2)) * 1088 + (g & 1) * 32 + W * 128 + (c & 3) * 8) + n * 32, 4 * 1088);
      const u32x4 sv = *(const LAS u32x4*)LP(L0 + QSTAT + c * 16);
      const rsrc_t rht = mk_rsrc(ws + WS_HT, HT_BYTES);
#pragma unroll
      for (int n = 0; n < 4; n++) bstore1(__builtin_bit_cast(u32x4, tv[n]), rht, lane * 16, ((4 * W + n) * 64 + un) * 1024);
      if (W == 2) bstore1(sv, mk_rsrc(ws + WS_STAT, S * 8), lane < 16 ? un * 256 + lane * 16 : OOB, 0);
    }
    if (W == 1 && lane < 32) asm volatile("global_atomic_add_x2 %0, %1, %0, off sc0" : "+v"(chk) : "v"(c1p) : "memory");
    WAITV(0);
    DRAIN();
    u64 e = ep;
    asm volatile("" : "+v"(e) : "v"(pfw));
    tag = (uni64(e) + 1) << 16;
    if (W == 1 && __any(lane < 32 && (landed_v<0>(chk) >> 16) != (tag >> 16))) gwait<2>(c1p, lane < 32, ~0xffffull, tag);
    if (last_of(L0 + SY + Y_N1, 8, lane)) { if (lane < 32) gadd(c1p); sy_put<Y_SIG>(a4, 1); }
  } else if (gi >= 22) {
    unsigned p3[3] = {0, 0, 0};
    if (W >= 4) {
#pragma unroll
      for (int n = 0; n < 3; n++) {
        const unsigned early = (n < 2 && lane < 48) ? (64 + n * 64 + ((sq * 2 + gi - 22) * 4 + w) * 2) * 3072 + lane * 128 : OOB;
        p3[n] = __builtin_amdgcn_raw_buffer_load_b32(mk_rsrc(wqkv, D * 3 * D * 2), hh ? early : ((n * 8 + (gi - 22) * 4 + w) * 64 + lane) * 1024 + 384, 0, 0);
      }
    } else if (W > 0) {
#pragma unroll
      for (int n = 0; n < 2; n++) p3[n] = __builtin_amdgcn_raw_buffer_load_b32(mk_rsrc(x, S * D * 2), (unsigned)((((22 * sq) >> 2) * 96 - 16 + (n * 6 + (gi - 22) * 3 + W - 1) * 52 + min(lane, 51)) * 1024 + 384), 0, 0);
    }
    tag = (uni64(__hip_atomic_load((const GAS u64*)(ws + WS_EPOCH), __ATOMIC_RELAXED, __HIP_MEMORY_SCOPE_AGENT)) + 1) << 16;
    if (W < 4) sy_put<Y_HA>(a1, 1);
    asm volatile("" ::"v"(p3[0]), "v"(p3[1]), "v"(p3[2]));
  }

  if (W < 4) {
    if (gi < 22) {
      f32x4 aq[7], ak[7], av[7];
#pragma unroll
      for (int b = 0; b < 7; b++) { ak[b] = z4; av[b] = z4; aq[b] = z4; }
      const unsigned ax = L0 + c * 128 + ((g ^ (c >> 1)) << 4);
      const unsigned aw = L0 + QXB + g * 3072 + (c >> 2) * 128 + ((w ^ ((c >> 3) | ((g & 1) << 1))) << 5) + (c & 3) * 8;
      bf16x8 xf[2][7];
      bf16x4 wl[2][3], wh[2][3];
      const lp pxa[2][2] = {{LP(ax), LP(ax ^ 64)}, {LP(ax + 2 * QST), LP((ax ^ 64) + 2 * QST)}}, pwa[2] = {LP(aw), LP(aw + 2 * QST)};
      auto RD = [&](int n, int i, int slot, int kk) {
        const lp px = pxa[slot >> 1][kk] + (slot & 1) * QST, pw = pwa[slot >> 1] + (slot & 1) * QST + kk * 12288;
        if (i == 0) wl[n][1] = ltr(pw + 1024);
        if (i == 1) wh[n][1] = ltr(pw + 1024 + 512);
        if (i == 2) xf[n][6] = l128(px + 12288);
        if (i == 3) wl[n][2] = ltr(pw + 2048);
        if (i == 4) wh[n][2] = ltr(pw + 2048 + 512);
        if (i == 5) wl[n][0] = ltr(pw);
        if (i == 6) wh[n][0] = ltr(pw + 512);
        if (i >= 7) xf[n][12 - i] = l128(px + (12 - i) * 2048);
      };
      auto MM = [&](int cur, int m) {
        const int t = 6 - m / 3;
        if (m % 3 == 0) mfma_ip(ak[t], cat8(wl[cur][1], wh[cur][1]), xf[cur][t]);
        if (m % 3 == 1) mfma_ip(av[t], xf[cur][t], cat8(wl[cur][2], wh[cur][2]));
        if (m % 3 == 2) mfma_ip(aq[t], cat8(wl[cur][0], wh[cur][0]), xf[cur][t]);
      };
      RBAR();
#pragma unroll
      for (int i = 0; i < 13; i++) RD(0, i, 0, 0);
      SCHED();
#pragma unroll 1
      for (int s4 = 0; s4 < 8; s4 += 4) {
#pragma unroll
        for (int sl = 0; sl < 4; sl++) {
#pragma unroll
          for (int m = 0; m < 21; m++) {
            MM(0, m); SCHED();
            if (m < 13) RD(1, m, sl, 1);
            if (m == 19 && (sl < 3 || s4 == 0)) BAR();
            SCHED();
          }
#pragma unroll
          for (int m = 0; m < 21; m++) {
            MM(1, m); SCHED();
            if (m < 13) RD(0, m, (sl + 1) & 3, 0);
            SCHED();
          }
        }
      }
      MFMA_DONE();
      SCHED();
      f32x2 cq[2], ck[2];
#pragma unroll
      for (int j = 0; j < 4; j++) { cq[j >> 1][j & 1] = -dpp<0x150>(aq[0][j]); ck[j >> 1][j & 1] = -dpp<0x150>(ak[0][j]); }
      f32x2 cvt = swap16(av[0][0], av[0][0]);
      cvt = swap32(cvt[0], cvt[0]);
      const f32x2 cv = bc2(-cvt[0]);
      sy_wait<Y_ST>(a4, 1);
      u32x2 qf[6], kf[7], vf[7];
      {
        float sr[7], sm[7]; f32x4 vr[7], vm[7];
        const lp pcs = LP(L0 + QSTAT + (c >> 3) * 64 + (c & 7) * 4), prs = LP(L0 + QSTAT + (g >> 1) * 64 + (g & 1) * 16);
#pragma unroll
        for (int t = 0; t < 7; t++) {
          sr[t] = *(const LAS float*)(pcs + t * 128); sm[t] = *(const LAS float*)(pcs + t * 128 + 32);
          vr[t] = lf4(prs + t * 128); vm[t] = lf4(prs + t * 128 + 32);
        }
        SCHED();
#pragma unroll
        for (int t = 0; t < 7; t++) {
          const f32x2 r = bc2(sr[t]), m = bc2(sm[t]);
          f32x2 y[6] = {m * ck[0], m * ck[1], m * cq[0], m * cq[1], lo2(vm[t]) * cv, hi2(vm[t]) * cv};
          SCHED();
          y[0] = fma2(r, lo2(ak[t]), y[0]); y[1] = fma2(r, hi2(ak[t]), y[1]); y[2] = fma2(r, lo2(aq[t]), y[2]); y[3] = fma2(r, hi2(aq[t]), y[3]);
          y[4] = fma2(lo2(vr[t]), lo2(av[t]), y[4]); y[5] = fma2(hi2(vr[t]), hi2(av[t]), y[5]);
          SCHED();
          kf[t][0] = pkv(y[0]); kf[t][1] = pkv(y[1]);
          if (t > 0) { qf[t - 1][0] = pkv(y[2]); qf[t - 1][1] = pkv(y[3]); }
          vf[t][0] = pkv(y[4]); vf[t][1] = pkv(y[5]);
        }
        SCHED();
      }
      u32x2 op[6];
      {
        unsigned om = 0u - (unsigned)(c & 1);
        asm("" : "+v"(om));
        const bool cur0 = 4 * g + (c & 1) <= c, cur1 = 4 * g + (c & 1) + 2 <= c;
        constexpr float SC = 0.25f * 1.4426950408889634f;
        f32x4 sp[6], su[6];
#pragma unroll
        for (int t = 0; t < 6; t++) { sp[t] = mfma16(kf[t], qf[t], z4); su[t] = mfma16(kf[t + 1], qf[t], z4); }
        float l0[6], l1[6], mx[8];
#pragma unroll
        for (int t = 0; t < 6; t++) {
          const float p0 = sel(om, sp[t][1], sp[t][0]), p1 = sel(om, sp[t][3], sp[t][2]);
          const float u0 = sel(om, su[t][1], su[t][0]), u1 = sel(om, su[t][3], su[t][2]);
          l0[t] = cur0 ? u0 : p0; l1[t] = cur1 ? u1 : p1;
          if (t == 0 && rt == 0) { l0[t] = cur0 ? l0[t] : -4e9f; l1[t] = cur1 ? l1[t] : -4e9f; }
          mx[t] = fmaxf(l0[t], l1[t]);
        }
        {
          const f32x2 a = swap16(mx[0], mx[1]), b = swap16(mx[2], mx[3]), a2 = swap16(mx[4], mx[5]);
          const float n2 = fmaxf(a2[0], a2[1]);
          const f32x2 d = swap32(fmaxf(a[0], a[1]), fmaxf(b[0], b[1])), d2 = swap32(n2, n2);
          const float m = fmaxf(d[0], d[1]) * -SC, m2 = fmaxf(d2[0], d2[1]) * -SC;
          const f32x2 e = swap32(m, m), f0 = swap16(e[0], e[0]), f1 = swap16(e[1], e[1]), f2 = swap16(m2, m2);
          mx[0] = f0[0]; mx[1] = f0[1]; mx[2] = f1[0]; mx[3] = f1[1]; mx[4] = f2[0]; mx[5] = f2[1];
        }
        const u32x4 ones = {0x3f803f80u, 0x3f803f80u, 0x3f803f80u, 0x3f803f80u};
        const unsigned keep = om ^ 0x0000ffffu;
        const unsigned k00 = cur0 ? 0u : keep, k01 = cur1 ? 0u : keep, k10 = cur0 ? keep : 0u, k11 = cur1 ? keep : 0u;
        f32x4 o[6], den[6];
#pragma unroll
        for (int t = 0; t < 6; t++) {
          const float e0 = __builtin_amdgcn_exp2f(__builtin_fmaf(l0[t], SC, mx[t])), e1 = __builtin_amdgcn_exp2f(__builtin_fmaf(l1[t], SC, mx[t]));
          const unsigned b0 = pk2(e0, e0), b1v = pk2(e1, e1);
          const u32x4 pu = {b0 & k00, b1v & k01, b0 & k10, b1v & k11}, vv = {vf[t][0], vf[t][1], vf[t + 1][0], vf[t + 1][1]};
          o[t] = mfma32(as_bf(vv), as_bf(pu), z4);
          den[t] = mfma32(as_bf(ones), as_bf(pu), z4);
        }
#pragma unroll
        for (int t = 0; t < 6; t++) {
          const f32x2 inv = bc2(__builtin_amdgcn_rcpf(den[t][0]));
          op[t][0] = pkv(lo2(o[t]) * inv); op[t][1] = pkv(hi2(o[t]) * inv);
        }
      }
      {
        const rsrc_t ro = mk_rsrc(ws + WS_O, O_BYTES);
#pragma unroll
        for (int t = 0; t < 6; t += 2) {
          const auto r0 = __builtin_amdgcn_permlane16_swap(op[t][0], op[t + 1][0], false, false);
          const auto r1 = __builtin_amdgcn_permlane16_swap(op[t][1], op[t + 1][1], false, false);
          const u32x4 v = {r0[0], r1[0], r0[1], r1[1]};
          if (t == 0 || rt < 21) bstore1(v, ro, (g & 1) * 16384 + ((g >> 1) * 16 + c) * 16, ((6 * rt + t) * 16 + 2 * hg + (w >> 1)) * 1024 + (w & 1) * 512);
        }
      }
      sy_put<Y_QS>(a1, 1);
      WAITV(0);
      if (last_of(L0 + SY + Y_N2, 4, lane) && lane < (rt < 21 ? 12 : 4)) gadd(ws + WS_C2 + (12 * rt + lane) * 128);
      { const u32x2 tg = *(const LAS u32x2*)LP(L0 + SY + Y_TAG); tag = ((u64)(unsigned)__builtin_amdgcn_readfirstlane(tg[1]) << 32) | (unsigned)__builtin_amdgcn_readfirstlane(tg[0]); }
    } else {
      const rsrc_t rw2 = mk_rsrc(w2, F * S * 2);
      if (gi >= 24) {
        sy_wait<Y_OK2>(a4, 1);
        const int m = gi - 24 + 8 * r8;
        const unsigned t = __builtin_amdgcn_raw_buffer_load_b32(rw2, m < 22 ? ((44 * w + 2 * m + (hh ^ 1)) * 8 + pc) * 4096 + sq * 1024 + 384 : OOB, 0, 0);
        asm volatile("" ::"v"(t));
      } else {
        unsigned pv[5];
#pragma unroll
        for (int n = 0; n < 5; n++) pv[n] = __builtin_amdgcn_raw_buffer_load_b32(rw2, ((44 * w + 2 * (gi - 22 + 2 * n) + hh) * 8 + r8) * 4096 + sq * 1024 + pc * 128, 0, 0);
        asm volatile("" ::"v"(pv[0]), "v"(pv[1]), "v"(pv[2]), "v"(pv[3]), "v"(pv[4]));
      }
    }
    asm volatile("" ::"v"(pfc));

    const rsrc_t rgg = mk_rsrc(ws + WS_G, G_BYTES);
    const unsigned vg = lane * 16 + (4 * ct * 43 + 11 * w) * 1024;
    const u32x4 sw2 = mk_srd(w2, F * S * 2);
    const unsigned vw2 = (352 * w + r8) * 4096 + st * 128 + ((((pc >> 1) ^ ((r8 >> 1) & 1)) << 5) | ((pc & 1) << 4));
    const unsigned lw = L0 + P2STG + w * 24576;
    const unsigned ar = lw + (8 * g + (c >> 2)) * 128 + ((((c >> 3) | ((g & 1) << 1))) << 5) + (c & 3) * 8;
    u32x4 gf[11][4];
    auto GW = [&](int k, int slot) {
#pragma unroll
      for (int j = 0; j < 4; j++) glds16(sw2, (k == 10 && w == 3) ? OOB : vw2 ^ ((j & 1) << 6), (32 * k + 8 * j) * 4096, lw + slot * 4096 + j * 1024);
    };
    auto LG1 = [&](int k, int j) { gf[k][j] = bload1(rgg, (k == 10 && w == 3) ? OOB : vg, j * 43 * 1024 + k * 1024); };
    lp pr[4];
#pragma unroll
    for (int j = 0; j < 4; j++) pr[j] = LP(ar ^ (((w + j) & 3) << 5));
    bf16x8 tw[2][4];
    bf16x8 tk[4][4];
    auto TR = [&](bf16x8 (&t)[4], int slot) {
#pragma unroll
      for (int j = 0; j < 4; j++) t[j] = ltr2(pr[j] + slot * 4096, 512);
      asm volatile("" ::"v"(t[0]), "v"(t[1]), "v"(t[2]), "v"(t[3]) : "memory");
    };
    const int srow = 64 * st + 16 * w + c;
    u32x4 xq[2];
    if (gi < 22) { sy_wait<Y_QS>(a4, 1); sy_wait<Y_ISS>(a4, 1); }
    asm volatile("buffer_load_dwordx4 %0, %2, %3, 0 offen\n\tbuffer_load_dwordx4 %1, %2, %3, 0 offen offset:64"
                 : "=&v"(xq[0]), "=&v"(xq[1]) : "v"((srow * D + 64 * ct + 16 * (g & 1) + 8 * (g >> 1)) * 2), "s"(sx) : "memory");
    const char* const c3p = ws + WS_C3 + ((st * 8 + ct) * 4 + w) * 128;
    u64 c3v = 0;
    const int s6 = -12 - 5 * w;
    if (gi < 22) {
      GW(7, 0); GW(8, 1);
      RBAR();
      GW(9, 2); GW(10, 3); GW(4, 4); GW(5, 5);
      WAITV(16); TR(tk[0], 0); TR(tk[1], 1);
      WAITV(8); TR(tk[2], 2); TR(tk[3], 3);
      RBAR();
      GW(0, 0); GW(1, 1); GW(2, 2);
      if (lane == 0) asm volatile("global_atomic_add_x2 %0, %1, %0, off sc0" : "+v"(c3v) : "v"(c3p) : "memory");
      GW(3, 3); GW(6, s6);
      WAITV(17); TR(tw[0], 0);
    } else {
      const int sc = 20 - 5 * w;
      auto TS = [&](bf16x8 (&t)[4], int k) {
#pragma unroll
        for (int j = 0; j < 4; j++) glds16(sw2, (k == 10 && w == 3) ? OOB : vw2 ^ ((j & 1) << 6), (32 * k + 8 * j) * 4096, lw + sc * 4096 + j * 1024);
        WAITV(0);
#pragma unroll
        for (int j = 0; j < 4; j++) t[j] = ltr2(pr[j] + sc * 4096, 512);
        asm volatile("" ::"v"(t[0]), "v"(t[1]), "v"(t[2]), "v"(t[3]) : "memory");
      };
      TS(tk[0], 7); TS(tk[1], 8); TS(tk[2], 9); TS(tk[3], 10); TS(tw[0], 0);
      sy_put<Y_QS>(a1, 1);
      sy_wait<Y_QS>(a4, 1);
      sy_wait<Y_ISS>(a4, 3);
      GW(1, 1); GW(2, 2); GW(3, 3); GW(4, 4);
      if (lane == 0) asm volatile("global_atomic_add_x2 %0, %1, %0, off sc0" : "+v"(c3v) : "v"(c3p) : "memory");
      GW(5, 5); GW(6, s6);
    }
    asm volatile("" : "+v"(xq[0]), "+v"(xq[1]));
    u32x2 xv[4];
#pragma unroll
    for (int i = 0; i < 2; i++) {
      const auto r0 = __builtin_amdgcn_permlane16_swap(xq[i][0], xq[i][2], false, false), r1 = __builtin_amdgcn_permlane16_swap(xq[i][1], xq[i][3], false, false);
      xv[2 * i][0] = r0[0]; xv[2 * i][1] = r1[0]; xv[2 * i + 1][0] = r0[1]; xv[2 * i + 1][1] = r1[1];
    }
    f32x4 acc[4][4];
#pragma unroll
    for (int i = 0; i < 4; i++) {
      acc[i][0] = (f32x4){bflo(xv[i][0]), bfhi(xv[i][0]), bflo(xv[i][1]), bfhi(xv[i][1])};
#pragma unroll
      for (int j = 1; j < 4; j++) acc[i][j] = z4;
      asm volatile("" : "+v"(acc[i][0]));
    }
    if (landed<8>(c3v) != tag + 6) gwait<0>(c3p, lane == 0, ~0ull, tag + 6);
    constexpr int GD = 3;
#pragma unroll
    for (int k = 0; k < GD; k++)
#pragma unroll
      for (int j = 0; j < 4; j++) LG1(k, j);
    WAITV(12);
    SCHED();
    u32x4 bv[2];
#pragma unroll
    for (int k = 0; k < 10; k++) {
      if (k + GD == 11) {
        sy_wait<Y_OK4>(a4, 1);
        const rsrc_t rb = mk_rsrc(ws + WS_BASE, S * D * 2);
#pragma unroll
        for (int i = 0; i < 2; i++) bv[i] = bload1(rb, lane * 16, ((4 * st + w) * 16 + 2 * ct + i) * 1024);
      }
#pragma unroll
      for (int i = 0; i < 4; i++) {
#pragma unroll
        for (int j = 0; j < 4; j++) {
          mfma_k(k == 0, acc[i][j], as_bf(gf[k][i]), k < 7 ? tw[k & 1][j] : tk[k < 7 ? 0 : k - 7][j]);
          if (j == 0 && k + 1 < 7) { SCHED(); tw[(k + 1) & 1][i] = ltr2(pr[i] + (k + 1 < 6 ? k + 1 : s6) * 4096, 512); SCHED(); }
        }
        SCHED();
        if (k + GD < 11) LG1(k + GD, i);
        SCHED();
      }
    }
#pragma unroll
    for (int jj = 1; jj <= 4; jj++) {
#pragma unroll
      for (int i = 0; i < 4; i++) {
        acc[i][jj & 3] = mfma32(as_bf(gf[10][i]), tk[3][jj & 3], acc[i][jj & 3]); SCHED();
        if (jj >= 2) *(LAS f32x4*)(LP(lw + lane * 16) + ((jj - 2) * 4 + i) * 1024) = acc[i][jj - 1];
        SCHED();
      }
    }
    BAR();
    f32x4 u[4];
#pragma unroll
    for (int i = 0; i < 4; i++) {
      u[i] = acc[i][0] + (f32x4){bflo(bv[i >> 1][2 * (i & 1)]), bfhi(bv[i >> 1][2 * (i & 1)]), bflo(bv[i >> 1][2 * (i & 1) + 1]), bfhi(bv[i >> 1][2 * (i & 1) + 1])};
#pragma unroll
      for (int j = 1; j < 4; j++) u[i] += lf4(LP(L0 + P2STG + ((w - j) & 3) * 24576 + lane * 16) + ((j - 1) * 4 + i) * 1024);
    }
#pragma unroll
    for (int i = 0; i < 4; i += 2) {
      const auto r0 = __builtin_amdgcn_permlane16_swap(pkv(lo2(u[i])), pkv(lo2(u[i + 1])), false, false);
      const auto r1 = __builtin_amdgcn_permlane16_swap(pkv(hi2(u[i])), pkv(hi2(u[i + 1])), false, false);
      const u32x4 o = {r0[0], r1[0], r0[1], r1[1]};
      *(u32x4*)(out + srow * D + 64 * ct + 16 * i + 16 * (g & 1) + 8 * (g >> 1)) = o;
    }
  } else {
    if (gi < 22) {
      const bool c1on = lane < (w == 3 ? 4 : 1);
      const char* const c1w = ws + WS_C1 + (((w == 3 ? 0 : w) + (lane & 3)) * 32 + (blockIdx.x & 31)) * 128;
      u64 c1v = 0, ep = 0;
      head(1); rest(1);
#pragma unroll 1
      for (int s = 2; s < 8; s++) {
        head(s);
        WAITV(12); RBAR();
        rest(s);
      }
      u32x4 stv4;
      auto c1_done = [&]() {
        tag = (landed<12>(ep) + 1) << 16;
        const u64 cv = landed_v<12>(c1v);
        if (__any(c1on && cv != tag + 16)) gwait<2>(c1w, c1on, ~0ull, tag + 16);
        if (w == 3) stv4 = bload1(mk_rsrc(ws + WS_STAT, S * 8), (unsigned)((12 * rt - 2) * 64 + min(lane, 55) * 16), 0);
      };
      auto put_stats = [&]() {
        if (w == 3) {
          const u32x2 tg = {(unsigned)tag, (unsigned)(tag >> 32)};
          *(LAS u32x2*)LP(L0 + SY + Y_TAG) = tg;
          *(LAS u32x4*)LP(L0 + QSTAT + min(lane, 55) * 16) = stv4;
          sy_put<Y_ST>(a4, 1);
        }
      };
      const int ft = rt, dt = hg;
      const rsrc_t rht = mk_rsrc(ws + WS_HT, HT_BYTES);
      const u32x4 s1 = mk_srd(w1, S * F * 2);
      u32x4 fb[16][4];
      const unsigned sz = (((pc >> 1) ^ ((r8 >> 1) & 1)) << 5) | ((pc & 1) << 4), vwb = (512 * w + r8) * 2752 + ft * 128;
      const unsigned vw0 = (ft == 21 && sz >= 64) ? OOB : vwb + sz, vw1 = (ft == 21 && sz < 64) ? OOB : vwb + (sz ^ 64);
      const unsigned vb = lane * 16 + (4 * dt * 64 + 16 * w) * 1024;
      const unsigned lb = L0 + G1STG + w * 4096;
      const unsigned ar = lb + (8 * g + (c >> 2)) * 128 + ((((c >> 3) | ((g & 1) << 1))) << 5) + (c & 3) * 8;
      lp pe[4];
#pragma unroll
      for (int e = 0; e < 4; e++) pe[e] = LP(ar ^ (e << 5));
      auto GW = [&](int k, int j) { glds16(s1, (j & 1) ? vw1 : vw0, (32 * k + 8 * j) * 2752, lb + (k % 3) * 16384 + j * 1024); };
      auto LB = [&](int k, int j) { fb[k][j] = bload1(rht, vb, ((w + j) & 3) * 65536 + k * 1024); };
      u32x2 bb[4];
      asm volatile("global_load_dwordx2 %0, %1, off sc1" : "=v"(ep) : "v"(ws + WS_EPOCH) : "memory");
      if (c1on) asm volatile("global_atomic_add_x2 %0, %1, %0, off sc0" : "+v"(c1v) : "v"(c1w) : "memory");
      GW(0, 0); GW(0, 1);
      WAITV(13); RBAR();
      GW(0, 2); GW(0, 3); GW(1, 0); GW(1, 1); GW(1, 2); GW(1, 3);
      WAITV(10); RBAR();
      {
        const rsrc_t rb1 = mk_rsrc(b1, F * 2);
#pragma unroll
        for (int i = 0; i < 4; i++) bb[i] = __builtin_amdgcn_raw_buffer_load_b64(rb1, (64 * ft + 4 * g) * 2 + i * 32, 0, 0);
      }
      c1_done();
      LB(0, 0); LB(0, 1); LB(0, 2); LB(0, 3);
      GW(2, 0); GW(2, 1); GW(2, 2); GW(2, 3);
      LB(1, 0); LB(1, 1); LB(1, 2); LB(1, 3);
      put_stats();
      LB(2, 0); LB(2, 1); LB(2, 2); LB(2, 3);
      LB(3, 0); LB(3, 1); LB(3, 2); LB(3, 3);
      WAITV(28);
      f32x4 acc[4][4];
#pragma unroll
      for (int i = 0; i < 4; i++) {
        acc[i][0] = (f32x4){bflo(bb[i][0]), bfhi(bb[i][0]), bflo(bb[i][1]), bfhi(bb[i][1])};
#pragma unroll
        for (int j = 1; j < 4; j++) acc[i][j] = z4;
      }
      bf16x4 al[2][4], ah[2][4];
#pragma unroll
      for (int e = 0; e < 4; e++) { al[0][e] = ltr(pe[e]); ah[0][e] = ltr(pe[e] + 512); }
      SCHED();
#pragma unroll
      for (int k = 0; k < 15; k++) {
#pragma unroll
        for (int m = 0; m < 16; m++) {
          const int i = m & 3, j = m >> 2;
          mfma_k(k == 0, acc[i][j], cat8(al[k & 1][i], ah[k & 1][i]), as_bf(fb[k][j]));
          SCHED();
          if (m == 5) {
            if (k == 0) WAITV(26); else if (k == 1) WAITV(22); else if (k < 12) WAITV(11); else if (k == 12) WAITV(10); else if (k == 13) WAITV(5); else WAITV(0);
          }
          if (m >= 6 && m < 14) {
            const int e = (m - 6) >> 1;
            if (m & 1) ah[(k + 1) & 1][e] = ltr(pe[e] + ((k + 1) % 3) * 16384 + 512); else al[(k + 1) & 1][e] = ltr(pe[e] + ((k + 1) % 3) * 16384);
          }
          if ((m == 4 || m == 6 || m == 9 || m == 13) && k + 3 < 16) GW(k + 3, m == 4 ? 0 : m == 6 ? 1 : m == 9 ? 2 : 3);
          if (i == 3 && k + 4 < 16) LB(k + 4, j);
          SCHED();
        }
        if (k == 12) sy_put<Y_ISS>(a1, 1);
      }
#pragma unroll
      for (int jj = 1; jj <= 4; jj++) {
#pragma unroll
        for (int i = 0; i < 4; i++) {
          acc[i][jj & 3] = mfma32(cat8(al[1][i], ah[1][i]), as_bf(fb[15][jj & 3]), acc[i][jj & 3]); SCHED();
          if (jj >= 2) *(LAS f32x4*)(LP(lb + lane * 16) + (jj - 2) * 16384 + i * 1024) = acc[i][jj - 1];
          SCHED();
        }
      }
      __builtin_amdgcn_s_setprio(3);
      const lp pin[3] = {LP(L0 + G1STG + ((w - 1) & 3) * 4096 + lane * 16), LP(L0 + G1STG + 16384 + ((w - 2) & 3) * 4096 + lane * 16), LP(L0 + G1STG + 32768 + ((w - 3) & 3) * 4096 + lane * 16)};
      BAR();
      f32x4 pt[4][3];
#pragma unroll
      for (int i = 0; i < 4; i++)
#pragma unroll
        for (int j = 0; j < 3; j++) pt[i][j] = lf4(pin[j] + i * 1024);
      SCHED();
      u32x2 gp[4];
#pragma unroll
      for (int i = 0; i < 4; i++) {
        const f32x4 u = (acc[i][0] + pt[i][0]) + (pt[i][1] + pt[i][2]);
#pragma unroll
        for (int hf = 0; hf < 2; hf++) {
          constexpr float K1 = -2.f * 0.7978845608028654f * 1.4426950408889634f, K2 = K1 * 0.044715f;
          const f32x2 t = hf ? hi2(u) : lo2(u);
          const f32x2 z = t * fma2(t * t, bc2(K2), bc2(K1));
          f32x2 e = {__builtin_amdgcn_exp2f(z[0]), __builtin_amdgcn_exp2f(z[1])};
          e += bc2(1.f);
          const f32x2 rc = {__builtin_amdgcn_rcpf(e[0]), __builtin_amdgcn_rcpf(e[1])};
          gp[i][hf] = pkv(t * rc);
        }
      }
      const rsrc_t rgg = mk_rsrc(ws + WS_G, G_BYTES);
#pragma unroll
      for (int i = 0; i < 4; i += 2) {
        const auto r0 = __builtin_amdgcn_permlane16_swap(gp[i][0], gp[i + 1][0], false, false);
        const auto r1v = __builtin_amdgcn_permlane16_swap(gp[i][1], gp[i + 1][1], false, false);
        const u32x4 v = {r0[0], r1v[0], r0[1], r1v[1]};
        if (i == 0 || ft < 21) bstore1(v, rgg, (2 * (g & 1) + (g >> 1)) * 256 + c * 16, ((4 * dt + w) * 43 + 2 * ft + (i >> 1)) * 1024);
      }
      WAITV(0);
      DRAIN();
      {
        const int sl0 = (2 * ft) / 11, sl1 = (2 * ft + 1) / 11;
        char* const c3a = ws + WS_C3 + (((lane & 31) * 8 + dt) * 4 + (lane < 32 ? sl0 : sl1)) * 128;
        RBAR();
        if ((lane >> 4) == w && (lane < 32 || sl1 != sl0)) gadd(c3a);
      }
    } else {
      if (gi >= 24) {
        unsigned pv[8];
        sy_wait<Y_SIG>(a4, 1);
        {
          const rsrc_t r1 = mk_rsrc(w1, S * F * 2);
          const unsigned c0 = ((22 * sq) >> 2) * 128;
#pragma unroll
          for (int n = 0; n < 8; n++) {
            const unsigned row = w * 512 + (2 * (gi - 24) + 16 * (n >> 1) + ((n & 1) ^ hh)) * 8 + r8;
            pv[n] = __builtin_amdgcn_raw_buffer_load_b32(r1, pc < 6 + (r8 & 1) ? ((row * 2752 + c0) & ~127u) + pc * 128 : OOB, 0, 0);
            if (n >= 2 && n < 7) WAITV(0);
          }
        }
        const int so = sq * 4 + ((gi - 24) >> 1), dq = hh * 2 + (gi & 1);
        const u32x4 swo = mk_srd(wo, D * D * 2);
        const unsigned vwo = (8 * w + r8) * 1024 + dq * 256 + vsw;
        const rsrc_t ro = mk_rsrc(ws + WS_O, O_BYTES), rb = mk_rsrc(ws + WS_BASE, S * D * 2);
        const int srow = 128 * so + 32 * w + c;
        unsigned bi0 = b2[srow], bi1 = b2[srow + 16];
        asm volatile("" : "+v"(bi0), "+v"(bi1) : "v"(pv[0]), "v"(pv[1]), "v"(pv[2]), "v"(pv[3]), "v"(pv[4]), "v"(pv[5]), "v"(pv[6]), "v"(pv[7]));
        const float bias0 = bflo(bi0), bias1 = bflo(bi1);
        const char* const c2p = ws + WS_C2 + ((so * 4 + w) * 4 + dq) * 128;
        u64 c2v = 0;
#pragma unroll 1
        for (int n = 0; n < 16; n++) {
          if (n == 11 && lane == 0) asm volatile("global_atomic_add_x2 %0, %1, %0, off sc0" : "+v"(c2v) : "v"(c2p) : "memory");
          glds16(swo, vwo, n * 32768, L0 + (n * 4 + w) * 1024);
          glds16(swo, vwo, n * 32768 + 128, L0 + 65536 + (n * 4 + w) * 1024);
        }
        if (landed<10>(c2v) != tag + 8) {
          sy_put<Y_OK2>(a1, 1);
          gwait<2>(c2p, lane == 0, ~0ull, tag + 8);
        }
        u32x4 of[16][2];
        const unsigned vo = lane * 16 + (8 * so + 2 * w) * 16384;
        constexpr int OD = 8;
#pragma unroll
        for (int k = 0; k < OD; k++) { of[k][0] = bload1(ro, vo, k * 1024); of[k][1] = bload1(ro, vo, 16384 + k * 1024); }
        WAITV(16);
        sy_put<Y_WO>(a1, 1);
        sy_put<Y_OK2>(a1, 1);
        sy_wait<Y_WO>(a4, 1);
        const unsigned ar = L0 + (8 * g + (c >> 2)) * 128 + ((((c >> 3) | ((g & 1) << 1))) << 5) + (c & 3) * 8;
        lp pe[8];
#pragma unroll
        for (int e = 0; e < 8; e++) pe[e] = LP((ar ^ ((e & 3) << 5)) + (e >> 2) * 65536);
        f32x4 acc[8][2];
#pragma unroll
        for (int i = 0; i < 8; i++) { acc[i][0] = (f32x4){bias0, bias0, bias0, bias0}; acc[i][1] = (f32x4){bias1, bias1, bias1, bias1}; }
        bf16x4 tl[2][8], th[2][8];
#pragma unroll
        for (int e = 0; e < 8; e++) { tl[0][e] = ltr(pe[e]); th[0][e] = ltr(pe[e] + 512); }
        SCHED();
#pragma unroll
        for (int k = 0; k < 16; k++) {
#pragma unroll
          for (int e = 0; e < 8; e++) {
            mfma_k(k == 0, acc[e][0], cat8(tl[k & 1][e], th[k & 1][e]), as_bf(of[k][0])); SCHED();
            if (k < 15) tl[(k + 1) & 1][e] = ltr(pe[e] + (k + 1) * 4096);
            SCHED();
            mfma_k(k == 0, acc[e][1], cat8(tl[k & 1][e], th[k & 1][e]), as_bf(of[k][1])); SCHED();
            if (k < 15) th[(k + 1) & 1][e] = ltr(pe[e] + (k + 1) * 4096 + 512);
            SCHED();
          }
          if (k + OD < 16) { of[k + OD][0] = bload1(ro, vo, (k + OD) * 1024); of[k + OD][1] = bload1(ro, vo, 16384 + (k + OD) * 1024); }
          if (k == 14) { FENCE(); sy_put<Y_ISS>(a1, 3); __builtin_amdgcn_s_setprio(3); }
        }
        MFMA_DONE();
        SCHED();
#pragma unroll
        for (int j = 0; j < 2; j++)
#pragma unroll
          for (int i = 0; i < 4; i++) {
            const u32x4 o = {pkv(lo2(acc[2 * i][j])), pkv(hi2(acc[2 * i][j])), pkv(lo2(acc[2 * i + 1][j])), pkv(hi2(acc[2 * i + 1][j]))};
            bstore1(o, rb, lane * 16, ((8 * so + 2 * w + j) * 16 + 4 * dq + i) * 1024);
          }
        WAITV(0);
        if (last_of(L0 + SY + Y_N3, 4, lane) && lane < 4) gadd(ws + WS_C4 + ((2 * so + (lane >> 1)) * 8 + 2 * dq + (lane & 1)) * 128);
      } else {
        const rsrc_t rw2 = mk_rsrc(w2, F * S * 2);
        unsigned pv[6];
#pragma unroll
        for (int n = 0; n < 6; n++) pv[n] = __builtin_amdgcn_raw_buffer_load_b32(rw2, ((44 * w + 2 * (10 + gi - 22 + 2 * n) + hh) * 8 + r8) * 4096 + sq * 1024 + pc * 128, 0, 0);
        asm volatile("" ::"v"(pv[0]), "v"(pv[1]), "v"(pv[2]), "v"(pv[3]), "v"(pv[4]), "v"(pv[5]));
        sy_put<Y_ISS>(a1, 3);
        __builtin_amdgcn_s_sleep(64);
      }
      DRAIN();
    }
    asm volatile("" ::"v"(pfc));
    if (w == 1) {
      gwait<4>(ws + WS_C4 + (st * 8 + ct) * 128, lane == 0, ~0ull, tag + 1);
      sy_put<Y_OK4>(a4, 1);
      if (gi == 22 || gi == 23) { sy_wait<Y_HA>(a4, 1); sy_wait<Y_ISS>(a4, 3); if (lane == 0) gadd(ws + WS_C5); }
    }
    if (w == 2 && blockIdx.x == 0) {
      gwait<4>(ws + (lane < 32 ? WS_C3 + lane * 128 : WS_C5), lane <= 32, ~0ull, tag + (lane < 32 ? 6 : 16));
      if (lane == 0) __hip_atomic_store((GAS u64*)(ws + WS_EPOCH), tag >> 16, __ATOMIC_RELAXED, __HIP_MEMORY_SCOPE_AGENT);
    }
    BAR();
  }
}

extern "C" void kernel_launch(void* const* d_in, const int* in_sizes, int n_in, void* d_out, int out_size, void* d_ws, size_t ws_size, hipStream_t stream) {
  (void)hipFuncSetAttribute((const void*)fused, hipFuncAttributeMaxDynamicSharedMemorySize, LDS_BYTES);
  fused<<<256, 512, LDS_BYTES, stream>>>((const u16*)d_in[0], (const u16*)d_in[1], (const u16*)d_in[2], (const u16*)d_in[4], (const u16*)d_in[5],
                                         (const u16*)d_in[6], (const u16*)d_in[7], (char*)d_ws, (u16*)d_out);
}
```
